```python
import torch

def _softmax(x, axis=-1):
    return torch.softmax(x, dim=axis)
def _gelu(x):
    return torch.nn.functional.gelu(x, approximate="tanh")
def _silu(x):
    return torch.nn.functional.silu(x)
def _sigmoid(x):
    return torch.sigmoid(x)
def _layernorm(x, axis=-1, eps=1e-5):
    m = x.mean(dim=axis, keepdim=True)
    v = x.var(dim=axis, keepdim=True, unbiased=False)
    return (x - m) / torch.sqrt(v + eps)
def _scatter_add(tgt, idx, src, axis=0):
    return tgt.clone().index_add_(axis, idx, src)
def _segment_sum(src, seg, n):
    out = torch.zeros((n,) + src.shape[1:], dtype=src.dtype)
    return out.index_add_(0, seg, src)
def _pad(x, pw):
    flat = tuple(v for pair in reversed(pw) for v in pair)
    return torch.nn.functional.pad(x, flat)

kernel_name = 'concat_heads__gqa_block_cached__mha_block_cached__near_ridge'

def setup_inputs():

    _w = (lambda a: (lambda t: t.to(torch.bfloat16) if t.is_floating_point() else t)(torch.as_tensor(a)))
    def _A_setup():
        B, Sq, D = 8, 160, 512
        Hq, Hkv, Sk, Dh = 32, 32, 512, 128
        assert Hq % Hkv == 0, (Hq, Hkv)
        return {
            "x": _w((lambda *s: torch.randn(*s, dtype=torch.bfloat16) * 0.8)(B, Sq, D)),
            "k_cache": _w((lambda *s: torch.randn(*s, dtype=torch.bfloat16) * 0.8)(B, Hkv, Sk, Dh)),
            "v_cache": _w((lambda *s: torch.randn(*s, dtype=torch.bfloat16) * 0.05)(B, Hkv, Sk, Dh)),
            "wq": _w((lambda *s: torch.randn(*s, dtype=torch.bfloat16) * 0.8)(D, Hq * Dh) * (D ** -0.5)),
            "wo": _w((lambda *s: torch.randn(*s, dtype=torch.bfloat16) * 0.05)(Hq * Dh, D) * ((Hq * Dh) ** -0.5)),
        }
    _A_in = _A_setup()

    def _B_setup():
        B, Sq, D = 8, 160, 512
        H, Sk, Dh = 32, 512, 128
        return {
            "x": _w((lambda *s: torch.randn(*s, dtype=torch.bfloat16) * 0.8)(B, Sq, D)),
            "k_cache": _w((lambda *s: torch.randn(*s, dtype=torch.bfloat16) * 0.8)(B, H, Sk, Dh)),
            "v_cache": _w((lambda *s: torch.randn(*s, dtype=torch.bfloat16) * 0.05)(B, H, Sk, Dh)),
            "wq": _w((lambda *s: torch.randn(*s, dtype=torch.bfloat16) * 0.8)(D, H * Dh) * (D ** -0.5)),
            "wo": _w((lambda *s: torch.randn(*s, dtype=torch.bfloat16) * 0.05)(H * Dh, D) * ((H * Dh) ** -0.5)),
        }
    _B_in = _B_setup()

    return {
        "A_x": _A_in["x"],
        "A_k_cache": _A_in["k_cache"],
        "A_v_cache": _A_in["v_cache"],
        "A_wq": _A_in["wq"],
        "A_wo": _A_in["wo"],
        "B_x": _B_in["x"],
        "B_k_cache": _B_in["k_cache"],
        "B_v_cache": _B_in["v_cache"],
        "B_wq": _B_in["wq"],
        "B_wo": _B_in["wo"],
    }

def reference(A_x, A_k_cache, A_v_cache, A_wq, A_wo, B_x, B_k_cache, B_v_cache, B_wq, B_wo):
    def _A_ref(x, k_cache, v_cache, wq, wo):
        B, Sq, D = x.shape
        Hq, Dh = 32, 128
        Hkv = k_cache.shape[1]
        rep = Hq // Hkv
        q = (lambda x, *d: x.permute(*d))((x @ wq).reshape(B, Sq, Hq, Dh), 0, 2, 1, 3)
        k = (lambda x, n, axis: torch.repeat_interleave(x, n, dim=axis))(k_cache, rep, axis=1)
        v = (lambda x, n, axis: torch.repeat_interleave(x, n, dim=axis))(v_cache, rep, axis=1)
        scores = torch.einsum("bhqd,bhkd->bhqk", q, k) * (Dh ** -0.5)
        p = _softmax(scores, axis=-1)
        o = torch.einsum("bhqk,bhkd->bhqd", p, v)
        return (lambda x, *d: x.permute(*d))(o, 0, 2, 1, 3).reshape(B, Sq, Hq * Dh) @ wo

    def _B_ref(x, k_cache, v_cache, wq, wo):
        B, Sq, D = x.shape
        H, Dh = 32, 128
        q = (lambda x, *d: x.permute(*d))((x @ wq).reshape(B, Sq, H, Dh), 0, 2, 1, 3)
        scores = torch.einsum("bhqd,bhkd->bhqk", q, k_cache) * (Dh ** -0.5)
        p = _softmax(scores, axis=-1)
        o = torch.einsum("bhqk,bhkd->bhqd", p, v_cache)
        return (lambda x, *d: x.permute(*d))(o, 0, 2, 1, 3).reshape(B, Sq, H * Dh) @ wo

    a = _A_ref(A_x, A_k_cache, A_v_cache, A_wq, A_wo)
    b = _B_ref(B_x, B_k_cache, B_v_cache, B_wq, B_wo)
    return torch.concatenate([a, b], axis=-1)
```

```cpp
#include <hip/hip_runtime.h>
#include <hip/hip_bf16.h>
#include <stdint.h>

#define K1_DEBUG 0
#define K1_NSTAMP 0
#define K1_NBAR 22

typedef unsigned short u16;
typedef float f32x4 __attribute__((ext_vector_type(4)));
typedef short bf16x8 __attribute__((ext_vector_type(8)));
typedef unsigned u32x4 __attribute__((ext_vector_type(4)));

namespace {
constexpr int NB = 8, SQ = 160, DM = 512, NH = 32, SK = 512, DH = 128;
constexpr size_t OWS_BYTES = (size_t)2 * NB * NH * SQ * DH * 2;
}

__device__ __forceinline__ int pi4(int i) { return (((i & 1) ^ (i >> 1)) << 1) | (i >> 1); }

__global__ __launch_bounds__(256, 1) void fused_attn(const u16* __restrict__ Ax, const u16* __restrict__ Ak, const u16* __restrict__ Av, const u16* __restrict__ Awq, const u16* __restrict__ Awo,
                                                      const u16* __restrict__ Bx, const u16* __restrict__ Bk, const u16* __restrict__ Bv, const u16* __restrict__ Bwq, const u16* __restrict__ Bwo,
                                                      char* __restrict__ ws, u16* __restrict__ out) {
  __shared__ __attribute__((aligned(1024))) char lds[163840];

  const int bid = blockIdx.x, xcd = bid & 7, j = bid >> 3;
  const int br = xcd >> 2, hq = xcd & 3, b = j >> 2, pr = j & 3;
  const int head0 = 8 * hq + 2 * pr, rot = (2 * b + 4 * pr) & 15;
  const int tid = threadIdx.x, l = tid & 63;
  const int w = __builtin_amdgcn_readfirstlane(tid >> 6), hsel = w >> 1, w1 = w & 1;
  const int head = head0 + hsel;
  const unsigned L0 = (unsigned)(uintptr_t)lds;
  const int g = l >> 4, r16 = l & 15, q4 = (l >> 2) & 3, p = l & 3;
  const unsigned vXrd = L0 + 16384 + w1 * 5120 + r16 * 64 + ((g ^ pi4(q4)) * 16);
  const unsigned vXd = (l >> 2) * 1024 + ((p ^ pi4(g)) * 16);
  const unsigned vWrd0 = L0 + hsel * 8192 + g * 2048 + q4 * 64 + (((p >> 1) ^ ((g & 1) << 1)) * 16) + (p & 1) * 8;
  const unsigned vWd0 = ((l >> 2) & 7) * 8192 + (l >> 5) * 64 + p * 16;
  const unsigned vKd0 = (l >> 3) * 256 + (((l & 7) ^ g) * 16);
  const unsigned vVd = ((l >> 2) & 7) * 256 + (l >> 5) * 64 + ((p ^ ((g & 1) << 1)) * 16);
  const unsigned vKrd0 = L0 + 16384 + hsel * 8192 + (r16 >> 3) * 2048 + (r16 & 7) * 128 + ((g ^ ((r16 >> 1) & 7)) * 16);
  const unsigned vVrd0 = L0 + hsel * 8192 + (g >> 1) * 2048 + (4 * (g & 1) + q4) * 64 + (((p >> 1) ^ ((g & 1) << 1)) * 16) + (p & 1) * 8;
  int sb = bid;
  if (l >= 1 && l <= 3) sb = xcd + 8 * (4 * b + ((pr + l) & 3));
  if (l >= 4 && l <= 6) sb = (4 * br + ((hq + l - 3) & 3)) + 8 * j;
  const unsigned vSync = (sb * 4 + w) * 512;
  const unsigned woLane = ((l >> 2) & 7) * 1024 + ((p ^ ((g & 1) << 1)) * 16);
  const unsigned vWoD0 = woLane + ((hq + (l >> 5)) & 3) * 64;
  const char* px = (const char*)(br ? Bx : Ax) + (size_t)b * (SQ * DM * 2);
  const char* pw = (const char*)(br ? Bwq : Awq) + head * (DH * 2);
  const char* pk = (const char*)(br ? Bk : Ak) + (size_t)(b * NH + head) * (SK * DH * 2);
  const char* pv = (const char*)(br ? Bv : Av) + (size_t)(b * NH + head) * (SK * DH * 2);
  const char* pwo = (const char*)(br ? Bwo : Awo) + (size_t)(8 * hq * 128) * 1024 + pr * 256;
  char* pout = (char*)out + ((size_t)(b * SQ + 80 * w1) * 1024 + br * 512 + 128 * pr + 16 * (2 * hq + hsel)) * 2;
  const unsigned sMw = L0 + hsel * 8192 + w1 * 4096, sMx = L0 + 16384 + 3 * w * 1024;
  const unsigned sXo0 = 3 * w * 16384, sW1o = w1 * 4096, sWo0 = w1 * 131072, sRot = rot;
  const unsigned sPack = hq | (pr << 2) | (hsel << 4) | (w1 << 5) | (br << 6) | (b << 7);
  const unsigned sAreaW = L0 + 32768 + 20480 * w, sAreaR = L0 + 32768 + 20480 * (w ^ 2);
  const unsigned sPeer0 = ((br * NB + b) * NH + 8 * hq + hsel) * 40960 + w1 * 20480;
  asm volatile(
    "s_mov_b64 s[56:57], %14\n\t"
    "s_add_u32 s56, s56, 0x1e00000\n\t"
    "s_addc_u32 s57, s57, 0\n\t"
    "s_and_b32 s57, s57, 0xffff\n\t"
    "s_mov_b32 s58, 0x80000\n\t"
    "s_mov_b32 s59, 0x20000\n\t"
    "buffer_load_dword v218, %8, s[56:59], 0 offen sc1\n\t"
    "s_mov_b64 s[40:41], %11\n\t"
    "s_and_b32 s41, s41, 0xffff\n\t"
    "s_mov_b32 s42, 0x3fe100\n\t"
    "s_mov_b32 s43, 0x20000\n\t"
    "s_mov_b64 s[36:37], %10\n\t"
    "s_and_b32 s37, s37, 0xffff\n\t"
    "s_mov_b32 s38, 0x28000\n\t"
    "s_mov_b32 s39, 0x20000\n\t"
    "v_add_u32 v172, 0x80, %3\n\t"
    "v_xor_b32 v173, 32, %3\n\t"
    "v_add_u32 v173, 0x10000, v173\n\t"
    "v_add_u32 v174, 0x80, v173\n\t"
    "v_add_u32 v175, 0x4000, %1\n\t"
    "v_add_u32 v176, 0x8000, %1\n\t"
    "s_mov_b32 s62, %22\n\t"
    "s_mov_b32 s61, 0\n\t"
    "s_lshl_b32 s63, s62, 18\n\t"
    "s_add_u32 s63, s63, %21\n\t"
    "s_lshl_b32 s64, s62, 6\n\t"
    "s_add_u32 s64, s64, %19\n\t"
    "s_add_u32 m0, s61, %17\n\t"
    "s_nop 0\n\t"
    "buffer_load_dwordx4 %3, s[40:43], s63 offen lds\n\t"
    "s_add_u32 m0, m0, 0x400\n\t"
    "s_nop 0\n\t"
    "buffer_load_dwordx4 v172, s[40:43], s63 offen lds\n\t"
    "s_add_u32 m0, m0, 0x400\n\t"
    "s_nop 0\n\t"
    "buffer_load_dwordx4 v173, s[40:43], s63 offen lds\n\t"
    "s_add_u32 m0, m0, 0x400\n\t"
    "s_nop 0\n\t"
    "buffer_load_dwordx4 v174, s[40:43], s63 offen lds\n\t"
    "s_add_u32 m0, s61, %18\n\t"
    "s_nop 0\n\t"
    "buffer_load_dwordx4 %1, s[36:39], s64 offen lds\n\t"
    "s_add_u32 m0, m0, 0x400\n\t"
    "s_nop 0\n\t"
    "buffer_load_dwordx4 v175, s[36:39], s64 offen lds\n\t"
    "s_add_u32 m0, m0, 0x400\n\t"
    "s_nop 0\n\t"
    "buffer_load_dwordx4 v176, s[36:39], s64 offen lds\n\t"
    "s_add_u32 s62, s62, 1\n\t"
    "s_and_b32 s62, s62, 15\n\t"
    "s_add_u32 s61, s61, 0x7000\n\t"
    "s_lshl_b32 s63, s62, 18\n\t"
    "s_add_u32 s63, s63, %21\n\t"
    "s_lshl_b32 s64, s62, 6\n\t"
    "s_add_u32 s64, s64, %19\n\t"
    "s_add_u32 m0, s61, %17\n\t"
    "s_nop 0\n\t"
    "buffer_load_dwordx4 %3, s[40:43], s63 offen lds\n\t"
    "s_add_u32 m0, m0, 0x400\n\t"
    "s_nop 0\n\t"
    "buffer_load_dwordx4 v172, s[40:43], s63 offen lds\n\t"
    "s_add_u32 m0, m0, 0x400\n\t"
    "s_nop 0\n\t"
    "buffer_load_dwordx4 v173, s[40:43], s63 offen lds\n\t"
    "s_add_u32 m0, m0, 0x400\n\t"
    "s_nop 0\n\t"
    "buffer_load_dwordx4 v174, s[40:43], s63 offen lds\n\t"
    "s_add_u32 m0, s61, %18\n\t"
    "s_nop 0\n\t"
    "buffer_load_dwordx4 %1, s[36:39], s64 offen lds\n\t"
    "s_add_u32 m0, m0, 0x400\n\t"
    "s_nop 0\n\t"
    "buffer_load_dwordx4 v175, s[36:39], s64 offen lds\n\t"
    "s_add_u32 m0, m0, 0x400\n\t"
    "s_nop 0\n\t"
    "buffer_load_dwordx4 v176, s[36:39], s64 offen lds\n\t"
    "s_add_u32 s62, s62, 1\n\t"
    "s_and_b32 s62, s62, 15\n\t"
    "s_add_u32 s61, s61, 0x7000\n\t"
    "s_mov_b64 s[44:45], %12\n\t"
    "s_and_b32 s45, s45, 0xffff\n\t"
    "s_mov_b32 s46, 0x20000\n\t"
    "s_mov_b32 s47, 0x20000\n\t"
    "s_mov_b64 s[48:49], %13\n\t"
    "s_and_b32 s49, s49, 0xffff\n\t"
    "s_mov_b32 s50, 0x20000\n\t"
    "s_mov_b32 s51, 0x20000\n\t"
    "s_mov_b64 s[52:53], %14\n\t"
    "s_and_b32 s53, s53, 0xffff\n\t"
    "s_mov_b32 s54, 0x1400000\n\t"
    "s_mov_b32 s55, 0x20000\n\t"
    "v_xor_b32 v230, 32, %2\n\t"
    "v_xor_b32 v229, 64, %6\n\t"
    "v_xor_b32 v228, 32, %7\n\t"
    "v_add_u32 v231, 0x80, %5\n\t"
    "v_xor_b32 v232, 64, %4\n\t"
    "v_add_u32 v233, 0x80, %4\n\t"
    "v_add_u32 v234, 0x80, v232\n\t"
    "v_mbcnt_lo_u32_b32 v239, -1, 0\n\t"
    "v_mbcnt_hi_u32_b32 v239, -1, v239\n\t"
    "v_lshlrev_b32 v239, 4, v239\n\t"
    "v_mov_b32 v12, 0\n\t"
    "v_mov_b32 v13, 0\n\t"
    "v_mov_b32 v14, 0\n\t"
    "v_mov_b32 v15, 0\n\t"
    "v_mov_b32 v16, 0\n\t"
    "v_mov_b32 v17, 0\n\t"
    "v_mov_b32 v18, 0\n\t"
    "v_mov_b32 v19, 0\n\t"
    "v_mov_b32 v20, 0\n\t"
    "v_mov_b32 v21, 0\n\t"
    "v_mov_b32 v22, 0\n\t"
    "v_mov_b32 v23, 0\n\t"
    "v_mov_b32 v24, 0\n\t"
    "v_mov_b32 v25, 0\n\t"
    "v_mov_b32 v26, 0\n\t"
    "v_mov_b32 v27, 0\n\t"
    "v_mov_b32 v28, 0\n\t"
    "v_mov_b32 v29, 0\n\t"
    "v_mov_b32 v30, 0\n\t"
    "v_mov_b32 v31, 0\n\t"
    "v_mov_b32 v32, 0\n\t"
    "v_mov_b32 v33, 0\n\t"
    "v_mov_b32 v34, 0\n\t"
    "v_mov_b32 v35, 0\n\t"
    "v_mov_b32 v36, 0\n\t"
    "v_mov_b32 v37, 0\n\t"
    "v_mov_b32 v38, 0\n\t"
    "v_mov_b32 v39, 0\n\t"
    "v_mov_b32 v40, 0\n\t"
    "v_mov_b32 v41, 0\n\t"
    "v_mov_b32 v42, 0\n\t"
    "v_mov_b32 v43, 0\n\t"
    "v_mov_b32 v44, 0\n\t"
    "v_mov_b32 v45, 0\n\t"
    "v_mov_b32 v46, 0\n\t"
    "v_mov_b32 v47, 0\n\t"
    "v_mov_b32 v48, 0\n\t"
    "v_mov_b32 v49, 0\n\t"
    "v_mov_b32 v50, 0\n\t"
    "v_mov_b32 v51, 0\n\t"
    "v_mov_b32 v52, 0\n\t"
    "v_mov_b32 v53, 0\n\t"
    "v_mov_b32 v54, 0\n\t"
    "v_mov_b32 v55, 0\n\t"
    "v_mov_b32 v56, 0\n\t"
    "v_mov_b32 v57, 0\n\t"
    "v_mov_b32 v58, 0\n\t"
    "v_mov_b32 v59, 0\n\t"
    "v_mov_b32 v60, 0\n\t"
    "v_mov_b32 v61, 0\n\t"
    "v_mov_b32 v62, 0\n\t"
    "v_mov_b32 v63, 0\n\t"
    "v_mov_b32 v64, 0\n\t"
    "v_mov_b32 v65, 0\n\t"
    "v_mov_b32 v66, 0\n\t"
    "v_mov_b32 v67, 0\n\t"
    "v_mov_b32 v68, 0\n\t"
    "v_mov_b32 v69, 0\n\t"
    "v_mov_b32 v70, 0\n\t"
    "v_mov_b32 v71, 0\n\t"
    "v_mov_b32 v72, 0\n\t"
    "v_mov_b32 v73, 0\n\t"
    "v_mov_b32 v74, 0\n\t"
    "v_mov_b32 v75, 0\n\t"
    "v_mov_b32 v76, 0\n\t"
    "v_mov_b32 v77, 0\n\t"
    "v_mov_b32 v78, 0\n\t"
    "v_mov_b32 v79, 0\n\t"
    "v_mov_b32 v80, 0\n\t"
    "v_mov_b32 v81, 0\n\t"
    "v_mov_b32 v82, 0\n\t"
    "v_mov_b32 v83, 0\n\t"
    "v_mov_b32 v84, 0\n\t"
    "v_mov_b32 v85, 0\n\t"
    "v_mov_b32 v86, 0\n\t"
    "v_mov_b32 v87, 0\n\t"
    "v_mov_b32 v88, 0\n\t"
    "v_mov_b32 v89, 0\n\t"
    "v_mov_b32 v90, 0\n\t"
    "v_mov_b32 v91, 0\n\t"
    "v_mov_b32 v92, 0\n\t"
    "v_mov_b32 v93, 0\n\t"
    "v_mov_b32 v94, 0\n\t"
    "v_mov_b32 v95, 0\n\t"
    "v_mov_b32 v96, 0\n\t"
    "v_mov_b32 v97, 0\n\t"
    "v_mov_b32 v98, 0\n\t"
    "v_mov_b32 v99, 0\n\t"
    "v_mov_b32 v100, 0\n\t"
    "v_mov_b32 v101, 0\n\t"
    "v_mov_b32 v102, 0\n\t"
    "v_mov_b32 v103, 0\n\t"
    "v_mov_b32 v104, 0\n\t"
    "v_mov_b32 v105, 0\n\t"
    "v_mov_b32 v106, 0\n\t"
    "v_mov_b32 v107, 0\n\t"
    "v_mov_b32 v108, 0\n\t"
    "v_mov_b32 v109, 0\n\t"
    "v_mov_b32 v110, 0\n\t"
    "v_mov_b32 v111, 0\n\t"
    "v_mov_b32 v112, 0\n\t"
    "v_mov_b32 v113, 0\n\t"
    "v_mov_b32 v114, 0\n\t"
    "v_mov_b32 v115, 0\n\t"
    "v_mov_b32 v116, 0\n\t"
    "v_mov_b32 v117, 0\n\t"
    "v_mov_b32 v118, 0\n\t"
    "v_mov_b32 v119, 0\n\t"
    "v_mov_b32 v120, 0\n\t"
    "v_mov_b32 v121, 0\n\t"
    "v_mov_b32 v122, 0\n\t"
    "v_mov_b32 v123, 0\n\t"
    "v_mov_b32 v124, 0\n\t"
    "v_mov_b32 v125, 0\n\t"
    "v_mov_b32 v126, 0\n\t"
    "v_mov_b32 v127, 0\n\t"
    "v_mov_b32 v128, 0\n\t"
    "v_mov_b32 v129, 0\n\t"
    "v_mov_b32 v130, 0\n\t"
    "v_mov_b32 v131, 0\n\t"
    "v_mov_b32 v132, 0\n\t"
    "v_mov_b32 v133, 0\n\t"
    "v_mov_b32 v134, 0\n\t"
    "v_mov_b32 v135, 0\n\t"
    "v_mov_b32 v136, 0\n\t"
    "v_mov_b32 v137, 0\n\t"
    "v_mov_b32 v138, 0\n\t"
    "v_mov_b32 v139, 0\n\t"
    "v_mov_b32 v140, 0\n\t"
    "v_mov_b32 v141, 0\n\t"
    "v_mov_b32 v142, 0\n\t"
    "v_mov_b32 v143, 0\n\t"
    "v_mov_b32 v144, 0\n\t"
    "v_mov_b32 v145, 0\n\t"
    "v_mov_b32 v146, 0\n\t"
    "v_mov_b32 v147, 0\n\t"
    "v_mov_b32 v148, 0\n\t"
    "v_mov_b32 v149, 0\n\t"
    "v_mov_b32 v150, 0\n\t"
    "v_mov_b32 v151, 0\n\t"
    "v_mov_b32 v152, 0\n\t"
    "v_mov_b32 v153, 0\n\t"
    "v_mov_b32 v154, 0\n\t"
    "v_mov_b32 v155, 0\n\t"
    "v_mov_b32 v156, 0\n\t"
    "v_mov_b32 v157, 0\n\t"
    "v_mov_b32 v158, 0\n\t"
    "v_mov_b32 v159, 0\n\t"
    "v_mov_b32 v160, 0\n\t"
    "v_mov_b32 v161, 0\n\t"
    "v_mov_b32 v162, 0\n\t"
    "v_mov_b32 v163, 0\n\t"
    "v_mov_b32 v164, 0\n\t"
    "v_mov_b32 v165, 0\n\t"
    "v_mov_b32 v166, 0\n\t"
    "v_mov_b32 v167, 0\n\t"
    "v_mov_b32 v168, 0\n\t"
    "v_mov_b32 v169, 0\n\t"
    "v_mov_b32 v170, 0\n\t"
    "v_mov_b32 v171, 0\n\t"
    "v_accvgpr_write_b32 a0, 0\n\t"
    "v_accvgpr_write_b32 a1, 0\n\t"
    "v_accvgpr_write_b32 a2, 0\n\t"
    "v_accvgpr_write_b32 a3, 0\n\t"
    "v_accvgpr_write_b32 a4, 0\n\t"
    "v_accvgpr_write_b32 a5, 0\n\t"
    "v_accvgpr_write_b32 a6, 0\n\t"
    "v_accvgpr_write_b32 a7, 0\n\t"
    "v_accvgpr_write_b32 a8, 0\n\t"
    "v_accvgpr_write_b32 a9, 0\n\t"
    "v_accvgpr_write_b32 a10, 0\n\t"
    "v_accvgpr_write_b32 a11, 0\n\t"
    "v_accvgpr_write_b32 a12, 0\n\t"
    "v_accvgpr_write_b32 a13, 0\n\t"
    "v_accvgpr_write_b32 a14, 0\n\t"
    "v_accvgpr_write_b32 a15, 0\n\t"
    "v_accvgpr_write_b32 a16, 0\n\t"
    "v_accvgpr_write_b32 a17, 0\n\t"
    "v_accvgpr_write_b32 a18, 0\n\t"
    "v_accvgpr_write_b32 a19, 0\n\t"
    "v_accvgpr_write_b32 a20, 0\n\t"
    "v_accvgpr_write_b32 a21, 0\n\t"
    "v_accvgpr_write_b32 a22, 0\n\t"
    "v_accvgpr_write_b32 a23, 0\n\t"
    "v_accvgpr_write_b32 a24, 0\n\t"
    "v_accvgpr_write_b32 a25, 0\n\t"
    "v_accvgpr_write_b32 a26, 0\n\t"
    "v_accvgpr_write_b32 a27, 0\n\t"
    "v_accvgpr_write_b32 a28, 0\n\t"
    "v_accvgpr_write_b32 a29, 0\n\t"
    "v_accvgpr_write_b32 a30, 0\n\t"
    "v_accvgpr_write_b32 a31, 0\n\t"
    "v_accvgpr_write_b32 a32, 0\n\t"
    "v_accvgpr_write_b32 a33, 0\n\t"
    "v_accvgpr_write_b32 a34, 0\n\t"
    "v_accvgpr_write_b32 a35, 0\n\t"
    "v_accvgpr_write_b32 a36, 0\n\t"
    "v_accvgpr_write_b32 a37, 0\n\t"
    "v_accvgpr_write_b32 a38, 0\n\t"
    "v_accvgpr_write_b32 a39, 0\n\t"
    "v_accvgpr_write_b32 a40, 0\n\t"
    "v_accvgpr_write_b32 a41, 0\n\t"
    "v_accvgpr_write_b32 a42, 0\n\t"
    "v_accvgpr_write_b32 a43, 0\n\t"
    "v_accvgpr_write_b32 a44, 0\n\t"
    "v_accvgpr_write_b32 a45, 0\n\t"
    "v_accvgpr_write_b32 a46, 0\n\t"
    "v_accvgpr_write_b32 a47, 0\n\t"
    "v_accvgpr_write_b32 a48, 0\n\t"
    "v_accvgpr_write_b32 a49, 0\n\t"
    "v_accvgpr_write_b32 a50, 0\n\t"
    "v_accvgpr_write_b32 a51, 0\n\t"
    "v_accvgpr_write_b32 a52, 0\n\t"
    "v_accvgpr_write_b32 a53, 0\n\t"
    "v_accvgpr_write_b32 a54, 0\n\t"
    "v_accvgpr_write_b32 a55, 0\n\t"
    "v_accvgpr_write_b32 a56, 0\n\t"
    "v_accvgpr_write_b32 a57, 0\n\t"
    "v_accvgpr_write_b32 a58, 0\n\t"
    "v_accvgpr_write_b32 a59, 0\n\t"
    "v_accvgpr_write_b32 a60, 0\n\t"
    "v_accvgpr_write_b32 a61, 0\n\t"
    "v_accvgpr_write_b32 a62, 0\n\t"
    "v_accvgpr_write_b32 a63, 0\n\t"
    "v_accvgpr_write_b32 a64, 0\n\t"
    "v_accvgpr_write_b32 a65, 0\n\t"
    "v_accvgpr_write_b32 a66, 0\n\t"
    "v_accvgpr_write_b32 a67, 0\n\t"
    "v_accvgpr_write_b32 a68, 0\n\t"
    "v_accvgpr_write_b32 a69, 0\n\t"
    "v_accvgpr_write_b32 a70, 0\n\t"
    "v_accvgpr_write_b32 a71, 0\n\t"
    "v_accvgpr_write_b32 a72, 0\n\t"
    "v_accvgpr_write_b32 a73, 0\n\t"
    "v_accvgpr_write_b32 a74, 0\n\t"
    "v_accvgpr_write_b32 a75, 0\n\t"
    "v_accvgpr_write_b32 a76, 0\n\t"
    "v_accvgpr_write_b32 a77, 0\n\t"
    "v_accvgpr_write_b32 a78, 0\n\t"
    "v_accvgpr_write_b32 a79, 0\n\t"
    "v_accvgpr_write_b32 a80, 0\n\t"
    "v_accvgpr_write_b32 a81, 0\n\t"
    "v_accvgpr_write_b32 a82, 0\n\t"
    "v_accvgpr_write_b32 a83, 0\n\t"
    "v_accvgpr_write_b32 a84, 0\n\t"
    "v_accvgpr_write_b32 a85, 0\n\t"
    "v_accvgpr_write_b32 a86, 0\n\t"
    "v_accvgpr_write_b32 a87, 0\n\t"
    "v_accvgpr_write_b32 a88, 0\n\t"
    "v_accvgpr_write_b32 a89, 0\n\t"
    "v_accvgpr_write_b32 a90, 0\n\t"
    "v_accvgpr_write_b32 a91, 0\n\t"
    "v_accvgpr_write_b32 a92, 0\n\t"
    "v_accvgpr_write_b32 a93, 0\n\t"
    "v_accvgpr_write_b32 a94, 0\n\t"
    "v_accvgpr_write_b32 a95, 0\n\t"
    "v_accvgpr_write_b32 a96, 0\n\t"
    "v_accvgpr_write_b32 a97, 0\n\t"
    "v_accvgpr_write_b32 a98, 0\n\t"
    "v_accvgpr_write_b32 a99, 0\n\t"
    "v_accvgpr_write_b32 a100, 0\n\t"
    "v_accvgpr_write_b32 a101, 0\n\t"
    "v_accvgpr_write_b32 a102, 0\n\t"
    "v_accvgpr_write_b32 a103, 0\n\t"
    "v_accvgpr_write_b32 a104, 0\n\t"
    "v_accvgpr_write_b32 a105, 0\n\t"
    "v_accvgpr_write_b32 a106, 0\n\t"
    "v_accvgpr_write_b32 a107, 0\n\t"
    "v_accvgpr_write_b32 a108, 0\n\t"
    "v_accvgpr_write_b32 a109, 0\n\t"
    "v_accvgpr_write_b32 a110, 0\n\t"
    "v_accvgpr_write_b32 a111, 0\n\t"
    "v_accvgpr_write_b32 a112, 0\n\t"
    "v_accvgpr_write_b32 a113, 0\n\t"
    "v_accvgpr_write_b32 a114, 0\n\t"
    "v_accvgpr_write_b32 a115, 0\n\t"
    "v_accvgpr_write_b32 a116, 0\n\t"
    "v_accvgpr_write_b32 a117, 0\n\t"
    "v_accvgpr_write_b32 a118, 0\n\t"
    "v_accvgpr_write_b32 a119, 0\n\t"
    "v_accvgpr_write_b32 a120, 0\n\t"
    "v_accvgpr_write_b32 a121, 0\n\t"
    "v_accvgpr_write_b32 a122, 0\n\t"
    "v_accvgpr_write_b32 a123, 0\n\t"
    "v_accvgpr_write_b32 a124, 0\n\t"
    "v_accvgpr_write_b32 a125, 0\n\t"
    "v_accvgpr_write_b32 a126, 0\n\t"
    "v_accvgpr_write_b32 a127, 0\n\t"
    "v_accvgpr_write_b32 a128, 0\n\t"
    "v_accvgpr_write_b32 a129, 0\n\t"
    "v_accvgpr_write_b32 a130, 0\n\t"
    "v_accvgpr_write_b32 a131, 0\n\t"
    "v_accvgpr_write_b32 a132, 0\n\t"
    "v_accvgpr_write_b32 a133, 0\n\t"
    "v_accvgpr_write_b32 a134, 0\n\t"
    "v_accvgpr_write_b32 a135, 0\n\t"
    "v_accvgpr_write_b32 a136, 0\n\t"
    "v_accvgpr_write_b32 a137, 0\n\t"
    "v_accvgpr_write_b32 a138, 0\n\t"
    "v_accvgpr_write_b32 a139, 0\n\t"
    "v_accvgpr_write_b32 a140, 0\n\t"
    "v_accvgpr_write_b32 a141, 0\n\t"
    "v_accvgpr_write_b32 a142, 0\n\t"
    "v_accvgpr_write_b32 a143, 0\n\t"
    "v_accvgpr_write_b32 a144, 0\n\t"
    "v_accvgpr_write_b32 a145, 0\n\t"
    "v_accvgpr_write_b32 a146, 0\n\t"
    "v_accvgpr_write_b32 a147, 0\n\t"
    "v_accvgpr_write_b32 a148, 0\n\t"
    "v_accvgpr_write_b32 a149, 0\n\t"
    "v_accvgpr_write_b32 a150, 0\n\t"
    "v_accvgpr_write_b32 a151, 0\n\t"
    "s_mov_b32 s79, 0x3e0293ee\n\t"
    "s_lshl_b32 s63, s62, 18\n\t"
    "s_add_u32 s63, s63, %21\n\t"
    "s_lshl_b32 s64, s62, 6\n\t"
    "s_add_u32 s64, s64, %19\n\t"
    "s_add_u32 m0, s61, %17\n\t"
    "s_nop 0\n\t"
    "buffer_load_dwordx4 %3, s[40:43], s63 offen lds\n\t"
    "s_add_u32 m0, m0, 0x400\n\t"
    "s_nop 0\n\t"
    "buffer_load_dwordx4 v172, s[40:43], s63 offen lds\n\t"
    "s_add_u32 m0, m0, 0x400\n\t"
    "s_nop 0\n\t"
    "buffer_load_dwordx4 v173, s[40:43], s63 offen lds\n\t"
    "s_add_u32 m0, m0, 0x400\n\t"
    "s_nop 0\n\t"
    "buffer_load_dwordx4 v174, s[40:43], s63 offen lds\n\t"
    "s_add_u32 m0, s61, %18\n\t"
    "s_nop 0\n\t"
    "buffer_load_dwordx4 %1, s[36:39], s64 offen lds\n\t"
    "s_add_u32 m0, m0, 0x400\n\t"
    "s_nop 0\n\t"
    "buffer_load_dwordx4 v175, s[36:39], s64 offen lds\n\t"
    "s_add_u32 m0, m0, 0x400\n\t"
    "s_nop 0\n\t"
    "buffer_load_dwordx4 v176, s[36:39], s64 offen lds\n\t"
    "s_add_u32 s62, s62, 1\n\t"
    "s_and_b32 s62, s62, 15\n\t"
    "s_getpc_b64 s[70:71]\n\t"
    "K1_PC0:\n\t"
    "s_add_u32 s72, s70, K1_RARE1-K1_PC0\n\t"
    "s_addc_u32 s73, s71, 0\n\t"
    "s_add_u32 s70, s70, K1_RARE0-K1_PC0\n\t"
    "s_addc_u32 s71, s71, 0\n\t"
    "s_waitcnt vmcnt(14) lgkmcnt(0)\n\t"
    "s_barrier\n\t"
    "v_add_u32 v235, 0, %0\n\t"
    "v_add_u32 v236, 0, %2\n\t"
    "v_add_u32 v237, 0, v230\n\t"
    "ds_read_b128 a[216:219], v235 offset:0\n\t"
    "ds_read_b128 a[220:223], v235 offset:1024\n\t"
    "ds_read_b128 a[224:227], v235 offset:2048\n\t"
    "ds_read_b128 a[228:231], v235 offset:3072\n\t"
    "ds_read_b128 a[232:235], v235 offset:4096\n\t"
    "ds_read_b64_tr_b16 a[152:153], v236 offset:0\n\t"
    "ds_read_b64_tr_b16 a[154:155], v236 offset:256\n\t"
    "ds_read_b64_tr_b16 a[156:157], v237 offset:0\n\t"
    "ds_read_b64_tr_b16 a[158:159], v237 offset:256\n\t"
    "ds_read_b64_tr_b16 a[160:161], v236 offset:512\n\t"
    "ds_read_b64_tr_b16 a[162:163], v236 offset:768\n\t"
    "ds_read_b64_tr_b16 a[164:165], v237 offset:512\n\t"
    "ds_read_b64_tr_b16 a[166:167], v237 offset:768\n\t"
    "ds_read_b64_tr_b16 a[168:169], v236 offset:1024\n\t"
    "ds_read_b64_tr_b16 a[170:171], v236 offset:1280\n\t"
    "s_waitcnt lgkmcnt(14)\n\t"
    "ds_read_b64_tr_b16 a[172:173], v237 offset:1024\n\t"
    "s_waitcnt lgkmcnt(14)\n\t"
    "ds_read_b64_tr_b16 a[174:175], v237 offset:1280\n\t"
    "s_waitcnt lgkmcnt(14)\n\t"
    "ds_read_b64_tr_b16 a[176:177], v236 offset:1536\n\t"
    "s_waitcnt lgkmcnt(14)\n\t"
    "ds_read_b64_tr_b16 a[178:179], v236 offset:1792\n\t"
    "s_waitcnt lgkmcnt(14)\n\t"
    "ds_read_b64_tr_b16 a[180:181], v237 offset:1536\n\t"
    "s_waitcnt lgkmcnt(14)\n\t"
    "ds_read_b64_tr_b16 a[182:183], v237 offset:1792\n\t"
    "v_add_u32 v212, 1, v218\n\t"
    "v_xor_b32 v213, 0x5bd1e995, v212\n\t"
    "v_add_u32 v216, 0x80, %8\n\t"
    "v_add_u32 v217, 0x100, %8\n\t"
    "s_mov_b32 s60, 0x7000\n\t"
    "s_mov_b32 s61, 0\n\t"
    "s_mov_b32 s69, 6\n\t"
    "K1_QLOOP:\n\t"
    "s_waitcnt vmcnt(7) lgkmcnt(0)\n\t"
    "s_barrier\n\t"
    "v_mfma_f32_16x16x32_bf16 v[12:15], a[152:155], a[216:219], v[12:15]\n\t"
    "s_lshl_b32 s63, s62, 18\n\t"
    "s_add_u32 s63, s63, %21\n\t"
    "v_mfma_f32_16x16x32_bf16 v[44:47], a[152:155], a[220:223], v[44:47]\n\t"
    "s_lshl_b32 s64, s62, 6\n\t"
    "s_add_u32 s64, s64, %19\n\t"
    "v_mfma_f32_16x16x32_bf16 v[76:79], a[152:155], a[224:227], v[76:79]\n\t"
    "s_add_u32 m0, s61, %17\n\t"
    "v_add_u32 v235, s60, %0\n\t"
    "v_mfma_f32_16x16x32_bf16 v[108:111], a[152:155], a[228:231], v[108:111]\n\t"
    "buffer_load_dwordx4 %3, s[40:43], s63 offen lds\n\t"
    "v_mfma_f32_16x16x32_bf16 v[140:143], a[152:155], a[232:235], v[140:143]\n\t"
    "s_add_u32 m0, m0, 0x400\n\t"
    "v_add_u32 v236, s60, %2\n\t"
    "v_mfma_f32_16x16x32_bf16 v[16:19], a[156:159], a[216:219], v[16:19]\n\t"
    "buffer_load_dwordx4 v172, s[40:43], s63 offen lds\n\t"
    "v_mfma_f32_16x16x32_bf16 v[48:51], a[156:159], a[220:223], v[48:51]\n\t"
    "s_add_u32 m0, m0, 0x400\n\t"
    "v_add_u32 v237, s60, v230\n\t"
    "v_mfma_f32_16x16x32_bf16 v[80:83], a[156:159], a[224:227], v[80:83]\n\t"
    "buffer_load_dwordx4 v173, s[40:43], s63 offen lds\n\t"
    "v_mfma_f32_16x16x32_bf16 v[112:115], a[156:159], a[228:231], v[112:115]\n\t"
    "s_add_u32 m0, m0, 0x400\n\t"
    "ds_read_b128 a[236:239], v235 offset:0\n\t"
    "v_mfma_f32_16x16x32_bf16 v[144:147], a[156:159], a[232:235], v[144:147]\n\t"
    "buffer_load_dwordx4 v174, s[40:43], s63 offen lds\n\t"
    "v_mfma_f32_16x16x32_bf16 v[20:23], a[160:163], a[216:219], v[20:23]\n\t"
    "s_add_u32 m0, s61, %18\n\t"
    "s_mov_b32 s61, s60\n\t"
    "v_mfma_f32_16x16x32_bf16 v[52:55], a[160:163], a[220:223], v[52:55]\n\t"
    "buffer_load_dwordx4 %1, s[36:39], s64 offen lds\n\t"
    "v_mfma_f32_16x16x32_bf16 v[84:87], a[160:163], a[224:227], v[84:87]\n\t"
    "s_add_u32 m0, m0, 0x400\n\t"
    "ds_read_b128 a[240:243], v235 offset:1024\n\t"
    "v_mfma_f32_16x16x32_bf16 v[116:119], a[160:163], a[228:231], v[116:119]\n\t"
    "buffer_load_dwordx4 v175, s[36:39], s64 offen lds\n\t"
    "v_mfma_f32_16x16x32_bf16 v[148:151], a[160:163], a[232:235], v[148:151]\n\t"
    "s_add_u32 m0, m0, 0x400\n\t"
    "s_add_u32 s62, s62, 1\n\t"
    "v_mfma_f32_16x16x32_bf16 v[24:27], a[164:167], a[216:219], v[24:27]\n\t"
    "s_and_b32 s62, s62, 15\n\t"
    "s_add_u32 s60, s60, 0x7000\n\t"
    "v_mfma_f32_16x16x32_bf16 v[56:59], a[164:167], a[220:223], v[56:59]\n\t"
    "s_cmp_eq_u32 s60, 0x15000\n\t"
    "ds_read_b128 a[244:247], v235 offset:2048\n\t"
    "v_mfma_f32_16x16x32_bf16 v[88:91], a[164:167], a[224:227], v[88:91]\n\t"
    "ds_read_b128 a[248:251], v235 offset:3072\n\t"
    "ds_read_b128 a[252:255], v235 offset:4096\n\t"
    "v_mfma_f32_16x16x32_bf16 v[120:123], a[164:167], a[228:231], v[120:123]\n\t"
    "ds_read_b64_tr_b16 a[184:185], v236 offset:0\n\t"
    "ds_read_b64_tr_b16 a[186:187], v236 offset:256\n\t"
    "v_mfma_f32_16x16x32_bf16 v[152:155], a[164:167], a[232:235], v[152:155]\n\t"
    "ds_read_b64_tr_b16 a[188:189], v237 offset:0\n\t"
    "ds_read_b64_tr_b16 a[190:191], v237 offset:256\n\t"
    "v_mfma_f32_16x16x32_bf16 v[28:31], a[168:171], a[216:219], v[28:31]\n\t"
    "ds_read_b64_tr_b16 a[192:193], v236 offset:512\n\t"
    "ds_read_b64_tr_b16 a[194:195], v236 offset:768\n\t"
    "v_mfma_f32_16x16x32_bf16 v[60:63], a[168:171], a[220:223], v[60:63]\n\t"
    "ds_read_b64_tr_b16 a[196:197], v237 offset:512\n\t"
    "ds_read_b64_tr_b16 a[198:199], v237 offset:768\n\t"
    "v_mfma_f32_16x16x32_bf16 v[92:95], a[168:171], a[224:227], v[92:95]\n\t"
    "ds_read_b64_tr_b16 a[200:201], v236 offset:1024\n\t"
    "ds_read_b64_tr_b16 a[202:203], v236 offset:1280\n\t"
    "v_mfma_f32_16x16x32_bf16 v[124:127], a[168:171], a[228:231], v[124:127]\n\t"
    "s_waitcnt lgkmcnt(13)\n\t"
    "ds_read_b64_tr_b16 a[204:205], v237 offset:1024\n\t"
    "ds_read_b64_tr_b16 a[206:207], v237 offset:1280\n\t"
    "v_mfma_f32_16x16x32_bf16 v[156:159], a[168:171], a[232:235], v[156:159]\n\t"
    "s_waitcnt lgkmcnt(14)\n\t"
    "ds_read_b64_tr_b16 a[208:209], v236 offset:1536\n\t"
    "s_waitcnt lgkmcnt(14)\n\t"
    "ds_read_b64_tr_b16 a[210:211], v236 offset:1792\n\t"
    "v_mfma_f32_16x16x32_bf16 v[32:35], a[172:175], a[216:219], v[32:35]\n\t"
    "s_waitcnt lgkmcnt(14)\n\t"
    "ds_read_b64_tr_b16 a[212:213], v237 offset:1536\n\t"
    "s_waitcnt lgkmcnt(14)\n\t"
    "ds_read_b64_tr_b16 a[214:215], v237 offset:1792\n\t"
    "v_mfma_f32_16x16x32_bf16 v[64:67], a[172:175], a[220:223], v[64:67]\n\t"
    "buffer_load_dwordx4 v176, s[36:39], s64 offen lds\n\t"
    "v_mfma_f32_16x16x32_bf16 v[96:99], a[172:175], a[224:227], v[96:99]\n\t"
    "s_cselect_b32 s60, 0, s60\n\t"
    "v_mfma_f32_16x16x32_bf16 v[128:131], a[172:175], a[228:231], v[128:131]\n\t"
    "v_mfma_f32_16x16x32_bf16 v[160:163], a[172:175], a[232:235], v[160:163]\n\t"
    "v_mfma_f32_16x16x32_bf16 v[36:39], a[176:179], a[216:219], v[36:39]\n\t"
    "v_mfma_f32_16x16x32_bf16 v[68:71], a[176:179], a[220:223], v[68:71]\n\t"
    "v_mfma_f32_16x16x32_bf16 v[100:103], a[176:179], a[224:227], v[100:103]\n\t"
    "v_mfma_f32_16x16x32_bf16 v[132:135], a[176:179], a[228:231], v[132:135]\n\t"
    "v_mfma_f32_16x16x32_bf16 v[164:167], a[176:179], a[232:235], v[164:167]\n\t"
    "v_mfma_f32_16x16x32_bf16 v[40:43], a[180:183], a[216:219], v[40:43]\n\t"
    "v_mfma_f32_16x16x32_bf16 v[72:75], a[180:183], a[220:223], v[72:75]\n\t"
    "v_mfma_f32_16x16x32_bf16 v[104:107], a[180:183], a[224:227], v[104:107]\n\t"
    "v_mfma_f32_16x16x32_bf16 v[136:139], a[180:183], a[228:231], v[136:139]\n\t"
    "v_mfma_f32_16x16x32_bf16 v[168:171], a[180:183], a[232:235], v[168:171]\n\t"
    "s_waitcnt vmcnt(7) lgkmcnt(0)\n\t"
    "s_barrier\n\t"
    "v_mfma_f32_16x16x32_bf16 v[12:15], a[184:187], a[236:239], v[12:15]\n\t"
    "s_lshl_b32 s63, s62, 18\n\t"
    "s_add_u32 s63, s63, %21\n\t"
    "v_mfma_f32_16x16x32_bf16 v[44:47], a[184:187], a[240:243], v[44:47]\n\t"
    "s_lshl_b32 s64, s62, 6\n\t"
    "s_add_u32 s64, s64, %19\n\t"
    "v_mfma_f32_16x16x32_bf16 v[76:79], a[184:187], a[244:247], v[76:79]\n\t"
    "s_add_u32 m0, s61, %17\n\t"
    "v_add_u32 v235, s60, %0\n\t"
    "v_mfma_f32_16x16x32_bf16 v[108:111], a[184:187], a[248:251], v[108:111]\n\t"
    "buffer_load_dwordx4 %3, s[40:43], s63 offen lds\n\t"
    "v_mfma_f32_16x16x32_bf16 v[140:143], a[184:187], a[252:255], v[140:143]\n\t"
    "s_add_u32 m0, m0, 0x400\n\t"
    "v_add_u32 v236, s60, %2\n\t"
    "v_mfma_f32_16x16x32_bf16 v[16:19], a[188:191], a[236:239], v[16:19]\n\t"
    "buffer_load_dwordx4 v172, s[40:43], s63 offen lds\n\t"
    "v_mfma_f32_16x16x32_bf16 v[48:51], a[188:191], a[240:243], v[48:51]\n\t"
    "s_add_u32 m0, m0, 0x400\n\t"
    "v_add_u32 v237, s60, v230\n\t"
    "v_mfma_f32_16x16x32_bf16 v[80:83], a[188:191], a[244:247], v[80:83]\n\t"
    "buffer_load_dwordx4 v173, s[40:43], s63 offen lds\n\t"
    "v_mfma_f32_16x16x32_bf16 v[112:115], a[188:191], a[248:251], v[112:115]\n\t"
    "s_add_u32 m0, m0, 0x400\n\t"
    "ds_read_b128 a[216:219], v235 offset:0\n\t"
    "v_mfma_f32_16x16x32_bf16 v[144:147], a[188:191], a[252:255], v[144:147]\n\t"
    "buffer_load_dwordx4 v174, s[40:43], s63 offen lds\n\t"
    "v_mfma_f32_16x16x32_bf16 v[20:23], a[192:195], a[236:239], v[20:23]\n\t"
    "s_add_u32 m0, s61, %18\n\t"
    "s_mov_b32 s61, s60\n\t"
    "v_mfma_f32_16x16x32_bf16 v[52:55], a[192:195], a[240:243], v[52:55]\n\t"
    "buffer_load_dwordx4 %1, s[36:39], s64 offen lds\n\t"
    "v_mfma_f32_16x16x32_bf16 v[84:87], a[192:195], a[244:247], v[84:87]\n\t"
    "s_add_u32 m0, m0, 0x400\n\t"
    "ds_read_b128 a[220:223], v235 offset:1024\n\t"
    "v_mfma_f32_16x16x32_bf16 v[116:119], a[192:195], a[248:251], v[116:119]\n\t"
    "buffer_load_dwordx4 v175, s[36:39], s64 offen lds\n\t"
    "v_mfma_f32_16x16x32_bf16 v[148:151], a[192:195], a[252:255], v[148:151]\n\t"
    "s_add_u32 m0, m0, 0x400\n\t"
    "s_add_u32 s62, s62, 1\n\t"
    "v_mfma_f32_16x16x32_bf16 v[24:27], a[196:199], a[236:239], v[24:27]\n\t"
    "s_and_b32 s62, s62, 15\n\t"
    "s_add_u32 s60, s60, 0x7000\n\t"
    "v_mfma_f32_16x16x32_bf16 v[56:59], a[196:199], a[240:243], v[56:59]\n\t"
    "s_cmp_eq_u32 s60, 0x15000\n\t"
    "ds_read_b128 a[224:227], v235 offset:2048\n\t"
    "v_mfma_f32_16x16x32_bf16 v[88:91], a[196:199], a[244:247], v[88:91]\n\t"
    "ds_read_b128 a[228:231], v235 offset:3072\n\t"
    "ds_read_b128 a[232:235], v235 offset:4096\n\t"
    "v_mfma_f32_16x16x32_bf16 v[120:123], a[196:199], a[248:251], v[120:123]\n\t"
    "ds_read_b64_tr_b16 a[152:153], v236 offset:0\n\t"
    "ds_read_b64_tr_b16 a[154:155], v236 offset:256\n\t"
    "v_mfma_f32_16x16x32_bf16 v[152:155], a[196:199], a[252:255], v[152:155]\n\t"
    "ds_read_b64_tr_b16 a[156:157], v237 offset:0\n\t"
    "ds_read_b64_tr_b16 a[158:159], v237 offset:256\n\t"
    "v_mfma_f32_16x16x32_bf16 v[28:31], a[200:203], a[236:239], v[28:31]\n\t"
    "ds_read_b64_tr_b16 a[160:161], v236 offset:512\n\t"
    "ds_read_b64_tr_b16 a[162:163], v236 offset:768\n\t"
    "v_mfma_f32_16x16x32_bf16 v[60:63], a[200:203], a[240:243], v[60:63]\n\t"
    "ds_read_b64_tr_b16 a[164:165], v237 offset:512\n\t"
    "ds_read_b64_tr_b16 a[166:167], v237 offset:768\n\t"
    "v_mfma_f32_16x16x32_bf16 v[92:95], a[200:203], a[244:247], v[92:95]\n\t"
    "ds_read_b64_tr_b16 a[168:169], v236 offset:1024\n\t"
    "ds_read_b64_tr_b16 a[170:171], v236 offset:1280\n\t"
    "v_mfma_f32_16x16x32_bf16 v[124:127], a[200:203], a[248:251], v[124:127]\n\t"
    "s_waitcnt lgkmcnt(13)\n\t"
    "ds_read_b64_tr_b16 a[172:173], v237 offset:1024\n\t"
    "ds_read_b64_tr_b16 a[174:175], v237 offset:1280\n\t"
    "v_mfma_f32_16x16x32_bf16 v[156:159], a[200:203], a[252:255], v[156:159]\n\t"
    "s_waitcnt lgkmcnt(14)\n\t"
    "ds_read_b64_tr_b16 a[176:177], v236 offset:1536\n\t"
    "s_waitcnt lgkmcnt(14)\n\t"
    "ds_read_b64_tr_b16 a[178:179], v236 offset:1792\n\t"
    "v_mfma_f32_16x16x32_bf16 v[32:35], a[204:207], a[236:239], v[32:35]\n\t"
    "s_waitcnt lgkmcnt(14)\n\t"
    "ds_read_b64_tr_b16 a[180:181], v237 offset:1536\n\t"
    "s_waitcnt lgkmcnt(14)\n\t"
    "ds_read_b64_tr_b16 a[182:183], v237 offset:1792\n\t"
    "v_mfma_f32_16x16x32_bf16 v[64:67], a[204:207], a[240:243], v[64:67]\n\t"
    "buffer_load_dwordx4 v176, s[36:39], s64 offen lds\n\t"
    "v_mfma_f32_16x16x32_bf16 v[96:99], a[204:207], a[244:247], v[96:99]\n\t"
    "s_cselect_b32 s60, 0, s60\n\t"
    "v_mfma_f32_16x16x32_bf16 v[128:131], a[204:207], a[248:251], v[128:131]\n\t"
    "v_mfma_f32_16x16x32_bf16 v[160:163], a[204:207], a[252:255], v[160:163]\n\t"
    "v_mfma_f32_16x16x32_bf16 v[36:39], a[208:211], a[236:239], v[36:39]\n\t"
    "v_mfma_f32_16x16x32_bf16 v[68:71], a[208:211], a[240:243], v[68:71]\n\t"
    "v_mfma_f32_16x16x32_bf16 v[100:103], a[208:211], a[244:247], v[100:103]\n\t"
    "v_mfma_f32_16x16x32_bf16 v[132:135], a[208:211], a[248:251], v[132:135]\n\t"
    "v_mfma_f32_16x16x32_bf16 v[164:167], a[208:211], a[252:255], v[164:167]\n\t"
    "v_mfma_f32_16x16x32_bf16 v[40:43], a[212:215], a[236:239], v[40:43]\n\t"
    "v_mfma_f32_16x16x32_bf16 v[72:75], a[212:215], a[240:243], v[72:75]\n\t"
    "v_mfma_f32_16x16x32_bf16 v[104:107], a[212:215], a[244:247], v[104:107]\n\t"
    "v_mfma_f32_16x16x32_bf16 v[136:139], a[212:215], a[248:251], v[136:139]\n\t"
    "v_mfma_f32_16x16x32_bf16 v[168:171], a[212:215], a[252:255], v[168:171]\n\t"
    "s_sub_u32 s69, s69, 1\n\t"
    "s_cmp_lg_u32 s69, 0\n\t"
    "s_cbranch_scc1 K1_QLOOP\n\t"
    "s_waitcnt vmcnt(7) lgkmcnt(0)\n\t"
    "s_barrier\n\t"
    "v_mfma_f32_16x16x32_bf16 v[12:15], a[152:155], a[216:219], v[12:15]\n\t"
    "s_lshl_b32 s63, s62, 18\n\t"
    "s_add_u32 s63, s63, %21\n\t"
    "v_mfma_f32_16x16x32_bf16 v[44:47], a[152:155], a[220:223], v[44:47]\n\t"
    "s_lshl_b32 s64, s62, 6\n\t"
    "s_add_u32 s64, s64, %19\n\t"
    "v_mfma_f32_16x16x32_bf16 v[76:79], a[152:155], a[224:227], v[76:79]\n\t"
    "s_add_u32 m0, s61, %17\n\t"
    "v_add_u32 v235, s60, %0\n\t"
    "v_mfma_f32_16x16x32_bf16 v[108:111], a[152:155], a[228:231], v[108:111]\n\t"
    "buffer_load_dwordx4 %3, s[40:43], s63 offen lds\n\t"
    "v_mfma_f32_16x16x32_bf16 v[140:143], a[152:155], a[232:235], v[140:143]\n\t"
    "s_add_u32 m0, m0, 0x400\n\t"
    "v_add_u32 v236, s60, %2\n\t"
    "v_mfma_f32_16x16x32_bf16 v[16:19], a[156:159], a[216:219], v[16:19]\n\t"
    "buffer_load_dwordx4 v172, s[40:43], s63 offen lds\n\t"
    "v_mfma_f32_16x16x32_bf16 v[48:51], a[156:159], a[220:223], v[48:51]\n\t"
    "s_add_u32 m0, m0, 0x400\n\t"
    "v_add_u32 v237, s60, v230\n\t"
    "v_mfma_f32_16x16x32_bf16 v[80:83], a[156:159], a[224:227], v[80:83]\n\t"
    "buffer_load_dwordx4 v173, s[40:43], s63 offen lds\n\t"
    "v_mfma_f32_16x16x32_bf16 v[112:115], a[156:159], a[228:231], v[112:115]\n\t"
    "s_add_u32 m0, m0, 0x400\n\t"
    "ds_read_b128 a[236:239], v235 offset:0\n\t"
    "v_mfma_f32_16x16x32_bf16 v[144:147], a[156:159], a[232:235], v[144:147]\n\t"
    "buffer_load_dwordx4 v174, s[40:43], s63 offen lds\n\t"
    "v_mfma_f32_16x16x32_bf16 v[20:23], a[160:163], a[216:219], v[20:23]\n\t"
    "s_add_u32 m0, s61, %18\n\t"
    "s_mov_b32 s61, s60\n\t"
    "v_mfma_f32_16x16x32_bf16 v[52:55], a[160:163], a[220:223], v[52:55]\n\t"
    "buffer_load_dwordx4 %1, s[36:39], s64 offen lds\n\t"
    "v_mfma_f32_16x16x32_bf16 v[84:87], a[160:163], a[224:227], v[84:87]\n\t"
    "s_add_u32 m0, m0, 0x400\n\t"
    "ds_read_b128 a[240:243], v235 offset:1024\n\t"
    "v_mfma_f32_16x16x32_bf16 v[116:119], a[160:163], a[228:231], v[116:119]\n\t"
    "buffer_load_dwordx4 v175, s[36:39], s64 offen lds\n\t"
    "v_mfma_f32_16x16x32_bf16 v[148:151], a[160:163], a[232:235], v[148:151]\n\t"
    "s_add_u32 m0, m0, 0x400\n\t"
    "s_add_u32 s62, s62, 1\n\t"
    "v_mfma_f32_16x16x32_bf16 v[24:27], a[164:167], a[216:219], v[24:27]\n\t"
    "s_and_b32 s62, s62, 15\n\t"
    "s_add_u32 s60, s60, 0x7000\n\t"
    "v_mfma_f32_16x16x32_bf16 v[56:59], a[164:167], a[220:223], v[56:59]\n\t"
    "s_cmp_eq_u32 s60, 0x15000\n\t"
    "ds_read_b128 a[244:247], v235 offset:2048\n\t"
    "v_mfma_f32_16x16x32_bf16 v[88:91], a[164:167], a[224:227], v[88:91]\n\t"
    "ds_read_b128 a[248:251], v235 offset:3072\n\t"
    "ds_read_b128 a[252:255], v235 offset:4096\n\t"
    "v_mfma_f32_16x16x32_bf16 v[120:123], a[164:167], a[228:231], v[120:123]\n\t"
    "ds_read_b64_tr_b16 a[184:185], v236 offset:0\n\t"
    "ds_read_b64_tr_b16 a[186:187], v236 offset:256\n\t"
    "v_mfma_f32_16x16x32_bf16 v[152:155], a[164:167], a[232:235], v[152:155]\n\t"
    "ds_read_b64_tr_b16 a[188:189], v237 offset:0\n\t"
    "ds_read_b64_tr_b16 a[190:191], v237 offset:256\n\t"
    "v_mfma_f32_16x16x32_bf16 v[28:31], a[168:171], a[216:219], v[28:31]\n\t"
    "ds_read_b64_tr_b16 a[192:193], v236 offset:512\n\t"
    "ds_read_b64_tr_b16 a[194:195], v236 offset:768\n\t"
    "v_mfma_f32_16x16x32_bf16 v[60:63], a[168:171], a[220:223], v[60:63]\n\t"
    "ds_read_b64_tr_b16 a[196:197], v237 offset:512\n\t"
    "ds_read_b64_tr_b16 a[198:199], v237 offset:768\n\t"
    "v_mfma_f32_16x16x32_bf16 v[92:95], a[168:171], a[224:227], v[92:95]\n\t"
    "ds_read_b64_tr_b16 a[200:201], v236 offset:1024\n\t"
    "ds_read_b64_tr_b16 a[202:203], v236 offset:1280\n\t"
    "v_mfma_f32_16x16x32_bf16 v[124:127], a[168:171], a[228:231], v[124:127]\n\t"
    "s_waitcnt lgkmcnt(13)\n\t"
    "ds_read_b64_tr_b16 a[204:205], v237 offset:1024\n\t"
    "ds_read_b64_tr_b16 a[206:207], v237 offset:1280\n\t"
    "v_mfma_f32_16x16x32_bf16 v[156:159], a[168:171], a[232:235], v[156:159]\n\t"
    "s_waitcnt lgkmcnt(14)\n\t"
    "ds_read_b64_tr_b16 a[208:209], v236 offset:1536\n\t"
    "s_waitcnt lgkmcnt(14)\n\t"
    "ds_read_b64_tr_b16 a[210:211], v236 offset:1792\n\t"
    "v_mfma_f32_16x16x32_bf16 v[32:35], a[172:175], a[216:219], v[32:35]\n\t"
    "s_waitcnt lgkmcnt(14)\n\t"
    "ds_read_b64_tr_b16 a[212:213], v237 offset:1536\n\t"
    "s_waitcnt lgkmcnt(14)\n\t"
    "ds_read_b64_tr_b16 a[214:215], v237 offset:1792\n\t"
    "v_mfma_f32_16x16x32_bf16 v[64:67], a[172:175], a[220:223], v[64:67]\n\t"
    "buffer_load_dwordx4 v176, s[36:39], s64 offen lds\n\t"
    "v_mfma_f32_16x16x32_bf16 v[96:99], a[172:175], a[224:227], v[96:99]\n\t"
    "s_cselect_b32 s60, 0, s60\n\t"
    "v_mfma_f32_16x16x32_bf16 v[128:131], a[172:175], a[228:231], v[128:131]\n\t"
    "v_mfma_f32_16x16x32_bf16 v[160:163], a[172:175], a[232:235], v[160:163]\n\t"
    "v_mfma_f32_16x16x32_bf16 v[36:39], a[176:179], a[216:219], v[36:39]\n\t"
    "v_mfma_f32_16x16x32_bf16 v[68:71], a[176:179], a[220:223], v[68:71]\n\t"
    "v_mfma_f32_16x16x32_bf16 v[100:103], a[176:179], a[224:227], v[100:103]\n\t"
    "v_mfma_f32_16x16x32_bf16 v[132:135], a[176:179], a[228:231], v[132:135]\n\t"
    "v_mfma_f32_16x16x32_bf16 v[164:167], a[176:179], a[232:235], v[164:167]\n\t"
    "v_mfma_f32_16x16x32_bf16 v[40:43], a[180:183], a[216:219], v[40:43]\n\t"
    "v_mfma_f32_16x16x32_bf16 v[72:75], a[180:183], a[220:223], v[72:75]\n\t"
    "v_mfma_f32_16x16x32_bf16 v[104:107], a[180:183], a[224:227], v[104:107]\n\t"
    "v_mfma_f32_16x16x32_bf16 v[136:139], a[180:183], a[228:231], v[136:139]\n\t"
    "v_mfma_f32_16x16x32_bf16 v[168:171], a[180:183], a[232:235], v[168:171]\n\t"
    "s_waitcnt vmcnt(7) lgkmcnt(0)\n\t"
    "s_barrier\n\t"
    "v_mfma_f32_16x16x32_bf16 v[12:15], a[184:187], a[236:239], v[12:15]\n\t"
    "s_mov_b32 s76, 0x18000\n\t"
    "s_mov_b32 s77, 0x20000\n\t"
    "v_mfma_f32_16x16x32_bf16 v[44:47], a[184:187], a[240:243], v[44:47]\n\t"
    "s_mov_b32 s68, %20\n\t"
    "s_add_u32 s67, %20, 0x2000\n\t"
    "v_mfma_f32_16x16x32_bf16 v[76:79], a[184:187], a[244:247], v[76:79]\n\t"
    "s_add_u32 m0, s76, %17\n\t"
    "v_add_u32 v235, s60, %0\n\t"
    "v_mfma_f32_16x16x32_bf16 v[108:111], a[184:187], a[248:251], v[108:111]\n\t"
    "buffer_load_dwordx4 %4, s[44:47], s68 offen nt lds\n\t"
    "v_mfma_f32_16x16x32_bf16 v[140:143], a[184:187], a[252:255], v[140:143]\n\t"
    "buffer_load_dwordx4 v232, s[44:47], s68 offen offset:2048 nt lds\n\t"
    "v_mfma_f32_16x16x32_bf16 v[16:19], a[188:191], a[236:239], v[16:19]\n\t"
    "s_add_u32 m0, m0, 0x400\n\t"
    "v_add_u32 v236, s60, %2\n\t"
    "v_mfma_f32_16x16x32_bf16 v[48:51], a[188:191], a[240:243], v[48:51]\n\t"
    "buffer_load_dwordx4 v233, s[44:47], s68 offen nt lds\n\t"
    "v_mfma_f32_16x16x32_bf16 v[80:83], a[188:191], a[244:247], v[80:83]\n\t"
    "buffer_load_dwordx4 v234, s[44:47], s68 offen offset:2048 nt lds\n\t"
    "v_mfma_f32_16x16x32_bf16 v[112:115], a[188:191], a[248:251], v[112:115]\n\t"
    "s_add_u32 m0, s76, %17\n\t"
    "s_add_u32 m0, m0, 0x4000\n\t"
    "v_mfma_f32_16x16x32_bf16 v[144:147], a[188:191], a[252:255], v[144:147]\n\t"
    "buffer_load_dwordx4 %4, s[44:47], s67 offen nt lds\n\t"
    "v_mfma_f32_16x16x32_bf16 v[20:23], a[192:195], a[236:239], v[20:23]\n\t"
    "buffer_load_dwordx4 v232, s[44:47], s67 offen offset:2048 nt lds\n\t"
    "v_mfma_f32_16x16x32_bf16 v[52:55], a[192:195], a[240:243], v[52:55]\n\t"
    "s_add_u32 m0, m0, 0x400\n\t"
    "v_add_u32 v237, s60, v230\n\t"
    "v_mfma_f32_16x16x32_bf16 v[84:87], a[192:195], a[244:247], v[84:87]\n\t"
    "buffer_load_dwordx4 v233, s[44:47], s67 offen nt lds\n\t"
    "v_mfma_f32_16x16x32_bf16 v[116:119], a[192:195], a[248:251], v[116:119]\n\t"
    "buffer_load_dwordx4 v234, s[44:47], s67 offen offset:2048 nt lds\n\t"
    "v_mfma_f32_16x16x32_bf16 v[148:151], a[192:195], a[252:255], v[148:151]\n\t"
    "s_add_u32 m0, s77, %17\n\t"
    "s_mov_b32 s61, s60\n\t"
    "v_mfma_f32_16x16x32_bf16 v[24:27], a[196:199], a[236:239], v[24:27]\n\t"
    "buffer_load_dwordx4 %5, s[48:51], s68 offen nt lds\n\t"
    "v_mfma_f32_16x16x32_bf16 v[56:59], a[196:199], a[240:243], v[56:59]\n\t"
    "buffer_load_dwordx4 %5, s[48:51], s68 offen offset:2048 nt lds\n\t"
    "v_mfma_f32_16x16x32_bf16 v[88:91], a[196:199], a[244:247], v[88:91]\n\t"
    "s_add_u32 m0, m0, 0x400\n\t"
    "s_add_u32 s60, s60, 0x7000\n\t"
    "v_mfma_f32_16x16x32_bf16 v[120:123], a[196:199], a[248:251], v[120:123]\n\t"
    "buffer_load_dwordx4 v231, s[48:51], s68 offen nt lds\n\t"
    "v_mfma_f32_16x16x32_bf16 v[152:155], a[196:199], a[252:255], v[152:155]\n\t"
    "buffer_load_dwordx4 v231, s[48:51], s68 offen offset:2048 nt lds\n\t"
    "v_mfma_f32_16x16x32_bf16 v[28:31], a[200:203], a[236:239], v[28:31]\n\t"
    "s_cmp_eq_u32 s60, 0x15000\n\t"
    "ds_read_b128 a[216:219], v235 offset:0\n\t"
    "v_mfma_f32_16x16x32_bf16 v[60:63], a[200:203], a[240:243], v[60:63]\n\t"
    "ds_read_b128 a[220:223], v235 offset:1024\n\t"
    "ds_read_b128 a[224:227], v235 offset:2048\n\t"
    "v_mfma_f32_16x16x32_bf16 v[92:95], a[200:203], a[244:247], v[92:95]\n\t"
    "ds_read_b128 a[228:231], v235 offset:3072\n\t"
    "ds_read_b128 a[232:235], v235 offset:4096\n\t"
    "v_mfma_f32_16x16x32_bf16 v[124:127], a[200:203], a[248:251], v[124:127]\n\t"
    "ds_read_b64_tr_b16 a[152:153], v236 offset:0\n\t"
    "ds_read_b64_tr_b16 a[154:155], v236 offset:256\n\t"
    "v_mfma_f32_16x16x32_bf16 v[156:159], a[200:203], a[252:255], v[156:159]\n\t"
    "ds_read_b64_tr_b16 a[156:157], v237 offset:0\n\t"
    "ds_read_b64_tr_b16 a[158:159], v237 offset:256\n\t"
    "v_mfma_f32_16x16x32_bf16 v[32:35], a[204:207], a[236:239], v[32:35]\n\t"
    "ds_read_b64_tr_b16 a[160:161], v236 offset:512\n\t"
    "ds_read_b64_tr_b16 a[162:163], v236 offset:768\n\t"
    "v_mfma_f32_16x16x32_bf16 v[64:67], a[204:207], a[240:243], v[64:67]\n\t"
    "ds_read_b64_tr_b16 a[164:165], v237 offset:512\n\t"
    "ds_read_b64_tr_b16 a[166:167], v237 offset:768\n\t"
    "v_mfma_f32_16x16x32_bf16 v[96:99], a[204:207], a[244:247], v[96:99]\n\t"
    "ds_read_b64_tr_b16 a[168:169], v236 offset:1024\n\t"
    "ds_read_b64_tr_b16 a[170:171], v236 offset:1280\n\t"
    "v_mfma_f32_16x16x32_bf16 v[128:131], a[204:207], a[248:251], v[128:131]\n\t"
    "s_waitcnt lgkmcnt(14)\n\t"
    "ds_read_b64_tr_b16 a[172:173], v237 offset:1024\n\t"
    "s_waitcnt lgkmcnt(14)\n\t"
    "ds_read_b64_tr_b16 a[174:175], v237 offset:1280\n\t"
    "v_mfma_f32_16x16x32_bf16 v[160:163], a[204:207], a[252:255], v[160:163]\n\t"
    "s_waitcnt lgkmcnt(14)\n\t"
    "ds_read_b64_tr_b16 a[176:177], v236 offset:1536\n\t"
    "s_waitcnt lgkmcnt(14)\n\t"
    "ds_read_b64_tr_b16 a[178:179], v236 offset:1792\n\t"
    "v_mfma_f32_16x16x32_bf16 v[36:39], a[208:211], a[236:239], v[36:39]\n\t"
    "s_waitcnt lgkmcnt(14)\n\t"
    "ds_read_b64_tr_b16 a[180:181], v237 offset:1536\n\t"
    "s_waitcnt lgkmcnt(14)\n\t"
    "ds_read_b64_tr_b16 a[182:183], v237 offset:1792\n\t"
    "v_mfma_f32_16x16x32_bf16 v[68:71], a[208:211], a[240:243], v[68:71]\n\t"
    "s_cselect_b32 s60, 0, s60\n\t"
    "v_mfma_f32_16x16x32_bf16 v[100:103], a[208:211], a[244:247], v[100:103]\n\t"
    "v_mfma_f32_16x16x32_bf16 v[132:135], a[208:211], a[248:251], v[132:135]\n\t"
    "v_mfma_f32_16x16x32_bf16 v[164:167], a[208:211], a[252:255], v[164:167]\n\t"
    "v_mfma_f32_16x16x32_bf16 v[40:43], a[212:215], a[236:239], v[40:43]\n\t"
    "v_mfma_f32_16x16x32_bf16 v[72:75], a[212:215], a[240:243], v[72:75]\n\t"
    "v_mfma_f32_16x16x32_bf16 v[104:107], a[212:215], a[244:247], v[104:107]\n\t"
    "v_mfma_f32_16x16x32_bf16 v[136:139], a[212:215], a[248:251], v[136:139]\n\t"
    "v_mfma_f32_16x16x32_bf16 v[168:171], a[212:215], a[252:255], v[168:171]\n\t"
    "s_waitcnt vmcnt(12) lgkmcnt(0)\n\t"
    "s_barrier\n\t"
    "v_mfma_f32_16x16x32_bf16 v[12:15], a[152:155], a[216:219], v[12:15]\n\t"
    "s_bfe_u32 s85, %23, 0x30007\n\t"
    "s_lshl_b32 s85, s85, 17\n\t"
    "v_mfma_f32_16x16x32_bf16 v[44:47], a[152:155], a[220:223], v[44:47]\n\t"
    "s_bfe_u32 s64, %23, 0x10004\n\t"
    "s_lshl_b32 s64, s64, 16\n\t"
    "v_mfma_f32_16x16x32_bf16 v[76:79], a[152:155], a[224:227], v[76:79]\n\t"
    "s_add_u32 s85, s85, s64\n\t"
    "s_bfe_u32 s64, %23, 0x10005\n\t"
    "v_mfma_f32_16x16x32_bf16 v[108:111], a[152:155], a[228:231], v[108:111]\n\t"
    "s_lshl_b32 s64, s64, 15\n\t"
    "s_add_u32 s85, s85, s64\n\t"
    "v_mfma_f32_16x16x32_bf16 v[140:143], a[152:155], a[232:235], v[140:143]\n\t"
    "s_mov_b64 s[36:37], %15\n\t"
    "s_add_u32 s36, s36, s85\n\t"
    "v_mfma_f32_16x16x32_bf16 v[16:19], a[156:159], a[216:219], v[16:19]\n\t"
    "s_addc_u32 s37, s37, 0\n\t"
    "v_add_u32 v235, s60, %0\n\t"
    "v_mfma_f32_16x16x32_bf16 v[48:51], a[156:159], a[220:223], v[48:51]\n\t"
    "v_add_u32 v236, s60, %2\n\t"
    "v_add_u32 v237, s60, v230\n\t"
    "v_mfma_f32_16x16x32_bf16 v[80:83], a[156:159], a[224:227], v[80:83]\n\t"
    "s_and_b32 s37, s37, 0xffff\n\t"
    "s_mov_b32 s61, s60\n\t"
    "v_mfma_f32_16x16x32_bf16 v[112:115], a[156:159], a[228:231], v[112:115]\n\t"
    "v_lshrrev_b32 v219, 8, v239\n\t"
    "s_add_u32 s60, s60, 0x7000\n\t"
    "v_mfma_f32_16x16x32_bf16 v[144:147], a[156:159], a[232:235], v[144:147]\n\t"
    "v_lshlrev_b32 v219, 10, v219\n\t"
    "v_and_b32 v220, 0xf0, v239\n\t"
    "v_mfma_f32_16x16x32_bf16 v[20:23], a[160:163], a[216:219], v[20:23]\n\t"
    "s_cmp_eq_u32 s60, 0x15000\n\t"
    "ds_read_b128 a[236:239], v235 offset:0\n\t"
    "v_mfma_f32_16x16x32_bf16 v[52:55], a[160:163], a[220:223], v[52:55]\n\t"
    "ds_read_b128 a[240:243], v235 offset:1024\n\t"
    "ds_read_b128 a[244:247], v235 offset:2048\n\t"
    "v_mfma_f32_16x16x32_bf16 v[84:87], a[160:163], a[224:227], v[84:87]\n\t"
    "ds_read_b128 a[248:251], v235 offset:3072\n\t"
    "ds_read_b128 a[252:255], v235 offset:4096\n\t"
    "v_mfma_f32_16x16x32_bf16 v[116:119], a[160:163], a[228:231], v[116:119]\n\t"
    "ds_read_b64_tr_b16 a[184:185], v236 offset:0\n\t"
    "ds_read_b64_tr_b16 a[186:187], v236 offset:256\n\t"
    "v_mfma_f32_16x16x32_bf16 v[148:151], a[160:163], a[232:235], v[148:151]\n\t"
    "ds_read_b64_tr_b16 a[188:189], v237 offset:0\n\t"
    "ds_read_b64_tr_b16 a[190:191], v237 offset:256\n\t"
    "v_mfma_f32_16x16x32_bf16 v[24:27], a[164:167], a[216:219], v[24:27]\n\t"
    "ds_read_b64_tr_b16 a[192:193], v236 offset:512\n\t"
    "ds_read_b64_tr_b16 a[194:195], v236 offset:768\n\t"
    "v_mfma_f32_16x16x32_bf16 v[56:59], a[164:167], a[220:223], v[56:59]\n\t"
    "ds_read_b64_tr_b16 a[196:197], v237 offset:512\n\t"
    "ds_read_b64_tr_b16 a[198:199], v237 offset:768\n\t"
    "v_mfma_f32_16x16x32_bf16 v[88:91], a[164:167], a[224:227], v[88:91]\n\t"
    "ds_read_b64_tr_b16 a[200:201], v236 offset:1024\n\t"
    "ds_read_b64_tr_b16 a[202:203], v236 offset:1280\n\t"
    "v_mfma_f32_16x16x32_bf16 v[120:123], a[164:167], a[228:231], v[120:123]\n\t"
    "s_waitcnt lgkmcnt(14)\n\t"
    "ds_read_b64_tr_b16 a[204:205], v237 offset:1024\n\t"
    "s_waitcnt lgkmcnt(14)\n\t"
    "ds_read_b64_tr_b16 a[206:207], v237 offset:1280\n\t"
    "v_mfma_f32_16x16x32_bf16 v[152:155], a[164:167], a[232:235], v[152:155]\n\t"
    "s_waitcnt lgkmcnt(14)\n\t"
    "ds_read_b64_tr_b16 a[208:209], v236 offset:1536\n\t"
    "s_waitcnt lgkmcnt(14)\n\t"
    "ds_read_b64_tr_b16 a[210:211], v236 offset:1792\n\t"
    "v_mfma_f32_16x16x32_bf16 v[28:31], a[168:171], a[216:219], v[28:31]\n\t"
    "s_waitcnt lgkmcnt(14)\n\t"
    "ds_read_b64_tr_b16 a[212:213], v237 offset:1536\n\t"
    "s_waitcnt lgkmcnt(14)\n\t"
    "ds_read_b64_tr_b16 a[214:215], v237 offset:1792\n\t"
    "v_mfma_f32_16x16x32_bf16 v[60:63], a[168:171], a[220:223], v[60:63]\n\t"
    "s_mov_b32 s38, 0x8000\n\t"
    "s_mov_b32 s39, 0x20000\n\t"
    "v_mfma_f32_16x16x32_bf16 v[92:95], a[168:171], a[224:227], v[92:95]\n\t"
    "s_mov_b32 s63, 0xffffa000\n\t"
    "v_or_b32 v219, v219, v220\n\t"
    "v_mfma_f32_16x16x32_bf16 v[124:127], a[168:171], a[228:231], v[124:127]\n\t"
    "s_cselect_b32 s60, 0, s60\n\t"
    "v_mfma_f32_16x16x32_bf16 v[156:159], a[168:171], a[232:235], v[156:159]\n\t"
    "v_mfma_f32_16x16x32_bf16 v[32:35], a[172:175], a[216:219], v[32:35]\n\t"
    "v_mfma_f32_16x16x32_bf16 v[64:67], a[172:175], a[220:223], v[64:67]\n\t"
    "v_mfma_f32_16x16x32_bf16 v[96:99], a[172:175], a[224:227], v[96:99]\n\t"
    "v_mfma_f32_16x16x32_bf16 v[128:131], a[172:175], a[228:231], v[128:131]\n\t"
    "v_mfma_f32_16x16x32_bf16 v[160:163], a[172:175], a[232:235], v[160:163]\n\t"
    "v_mfma_f32_16x16x32_bf16 v[36:39], a[176:179], a[216:219], v[36:39]\n\t"
    "v_mfma_f32_16x16x32_bf16 v[68:71], a[176:179], a[220:223], v[68:71]\n\t"
    "v_mfma_f32_16x16x32_bf16 v[100:103], a[176:179], a[224:227], v[100:103]\n\t"
    "v_mfma_f32_16x16x32_bf16 v[132:135], a[176:179], a[228:231], v[132:135]\n\t"
    "v_mfma_f32_16x16x32_bf16 v[164:167], a[176:179], a[232:235], v[164:167]\n\t"
    "v_mfma_f32_16x16x32_bf16 v[40:43], a[180:183], a[216:219], v[40:43]\n\t"
    "v_mfma_f32_16x16x32_bf16 v[72:75], a[180:183], a[220:223], v[72:75]\n\t"
    "v_mfma_f32_16x16x32_bf16 v[104:107], a[180:183], a[224:227], v[104:107]\n\t"
    "v_mfma_f32_16x16x32_bf16 v[136:139], a[180:183], a[228:231], v[136:139]\n\t"
    "v_mfma_f32_16x16x32_bf16 v[168:171], a[180:183], a[232:235], v[168:171]\n\t"
    "s_waitcnt lgkmcnt(0)\n\t"
    "s_barrier\n\t"
    "v_mfma_f32_16x16x32_bf16 v[12:15], a[184:187], a[236:239], v[12:15]\n\t"
    "s_mov_b32 s77, 0x20000\n\t"
    "s_mov_b32 s76, 0\n\t"
    "v_mfma_f32_16x16x32_bf16 v[44:47], a[184:187], a[240:243], v[44:47]\n\t"
    "s_add_u32 s67, %20, 0x4000\n\t"
    "s_add_u32 s68, %20, 0x2000\n\t"
    "v_mfma_f32_16x16x32_bf16 v[76:79], a[184:187], a[244:247], v[76:79]\n\t"
    "s_add_u32 m0, s77, %17\n\t"
    "s_add_u32 m0, m0, 0x4000\n\t"
    "v_mfma_f32_16x16x32_bf16 v[108:111], a[184:187], a[248:251], v[108:111]\n\t"
    "buffer_load_dwordx4 %4, s[44:47], s67 offen nt lds\n\t"
    "v_mfma_f32_16x16x32_bf16 v[140:143], a[184:187], a[252:255], v[140:143]\n\t"
    "buffer_load_dwordx4 v232, s[44:47], s67 offen offset:2048 nt lds\n\t"
    "v_mfma_f32_16x16x32_bf16 v[16:19], a[188:191], a[236:239], v[16:19]\n\t"
    "s_add_u32 m0, m0, 0x400\n\t"
    "s_mov_b32 s61, s60\n\t"
    "v_mfma_f32_16x16x32_bf16 v[48:51], a[188:191], a[240:243], v[48:51]\n\t"
    "buffer_load_dwordx4 v233, s[44:47], s67 offen nt lds\n\t"
    "v_mfma_f32_16x16x32_bf16 v[80:83], a[188:191], a[244:247], v[80:83]\n\t"
    "buffer_load_dwordx4 v234, s[44:47], s67 offen offset:2048 nt lds\n\t"
    "v_mfma_f32_16x16x32_bf16 v[112:115], a[188:191], a[248:251], v[112:115]\n\t"
    "s_add_u32 m0, s76, %17\n\t"
    "v_accvgpr_write_b32 a152, 0\n\t"
    "v_mfma_f32_16x16x32_bf16 v[144:147], a[188:191], a[252:255], v[144:147]\n\t"
    "buffer_load_dwordx4 %5, s[48:51], s68 offen nt lds\n\t"
    "v_mfma_f32_16x16x32_bf16 v[20:23], a[192:195], a[236:239], v[20:23]\n\t"
    "buffer_load_dwordx4 %5, s[48:51], s68 offen offset:2048 nt lds\n\t"
    "v_mfma_f32_16x16x32_bf16 v[52:55], a[192:195], a[240:243], v[52:55]\n\t"
    "s_add_u32 m0, m0, 0x400\n\t"
    "s_add_u32 s60, s60, 0x7000\n\t"
    "v_mfma_f32_16x16x32_bf16 v[84:87], a[192:195], a[244:247], v[84:87]\n\t"
    "buffer_load_dwordx4 v231, s[48:51], s68 offen nt lds\n\t"
    "v_mfma_f32_16x16x32_bf16 v[116:119], a[192:195], a[248:251], v[116:119]\n\t"
    "buffer_load_dwordx4 v231, s[48:51], s68 offen offset:2048 nt lds\n\t"
    "v_mfma_f32_16x16x32_bf16 v[148:151], a[192:195], a[252:255], v[148:151]\n\t"
    "s_cmp_eq_u32 s60, 0x15000\n\t"
    "s_cselect_b32 s60, 0, s60\n\t"
    "v_mfma_f32_16x16x32_bf16 v[24:27], a[196:199], a[236:239], v[24:27]\n\t"
    "v_accvgpr_write_b32 a153, 0\n\t"
    "v_accvgpr_write_b32 a154, 0\n\t"
    "v_mfma_f32_16x16x32_bf16 v[56:59], a[196:199], a[240:243], v[56:59]\n\t"
    "v_accvgpr_write_b32 a155, 0\n\t"
    "v_accvgpr_write_b32 a156, 0\n\t"
    "v_mfma_f32_16x16x32_bf16 v[88:91], a[196:199], a[244:247], v[88:91]\n\t"
    "v_accvgpr_write_b32 a157, 0\n\t"
    "v_accvgpr_write_b32 a158, 0\n\t"
    "v_mfma_f32_16x16x32_bf16 v[120:123], a[196:199], a[248:251], v[120:123]\n\t"
    "v_accvgpr_write_b32 a159, 0\n\t"
    "v_accvgpr_write_b32 a160, 0\n\t"
    "v_mfma_f32_16x16x32_bf16 v[152:155], a[196:199], a[252:255], v[152:155]\n\t"
    "v_accvgpr_write_b32 a161, 0\n\t"
    "v_accvgpr_write_b32 a162, 0\n\t"
    "v_mfma_f32_16x16x32_bf16 v[28:31], a[200:203], a[236:239], v[28:31]\n\t"
    "v_accvgpr_write_b32 a163, 0\n\t"
    "v_accvgpr_write_b32 a164, 0\n\t"
    "v_mfma_f32_16x16x32_bf16 v[60:63], a[200:203], a[240:243], v[60:63]\n\t"
    "v_accvgpr_write_b32 a165, 0\n\t"
    "v_accvgpr_write_b32 a166, 0\n\t"
    "v_mfma_f32_16x16x32_bf16 v[92:95], a[200:203], a[244:247], v[92:95]\n\t"
    "v_accvgpr_write_b32 a167, 0\n\t"
    "v_accvgpr_write_b32 a168, 0\n\t"
    "v_mfma_f32_16x16x32_bf16 v[124:127], a[200:203], a[248:251], v[124:127]\n\t"
    "v_accvgpr_write_b32 a169, 0\n\t"
    "v_accvgpr_write_b32 a170, 0\n\t"
    "v_mfma_f32_16x16x32_bf16 v[156:159], a[200:203], a[252:255], v[156:159]\n\t"
    "v_accvgpr_write_b32 a171, 0\n\t"
    "v_accvgpr_write_b32 a172, 0\n\t"
    "v_mfma_f32_16x16x32_bf16 v[32:35], a[204:207], a[236:239], v[32:35]\n\t"
    "v_accvgpr_write_b32 a173, 0\n\t"
    "v_accvgpr_write_b32 a174, 0\n\t"
    "v_mfma_f32_16x16x32_bf16 v[64:67], a[204:207], a[240:243], v[64:67]\n\t"
    "v_accvgpr_write_b32 a175, 0\n\t"
    "v_accvgpr_write_b32 a176, 0\n\t"
    "v_mfma_f32_16x16x32_bf16 v[96:99], a[204:207], a[244:247], v[96:99]\n\t"
    "v_accvgpr_write_b32 a177, 0\n\t"
    "v_accvgpr_write_b32 a178, 0\n\t"
    "v_mfma_f32_16x16x32_bf16 v[128:131], a[204:207], a[248:251], v[128:131]\n\t"
    "v_accvgpr_write_b32 a179, 0\n\t"
    "v_mfma_f32_16x16x32_bf16 v[160:163], a[204:207], a[252:255], v[160:163]\n\t"
    "v_mfma_f32_16x16x32_bf16 v[36:39], a[208:211], a[236:239], v[36:39]\n\t"
    "v_mfma_f32_16x16x32_bf16 v[68:71], a[208:211], a[240:243], v[68:71]\n\t"
    "v_mfma_f32_16x16x32_bf16 v[100:103], a[208:211], a[244:247], v[100:103]\n\t"
    "v_mfma_f32_16x16x32_bf16 v[132:135], a[208:211], a[248:251], v[132:135]\n\t"
    "v_mfma_f32_16x16x32_bf16 v[164:167], a[208:211], a[252:255], v[164:167]\n\t"
    "v_mfma_f32_16x16x32_bf16 v[40:43], a[212:215], a[236:239], v[40:43]\n\t"
    "v_mfma_f32_16x16x32_bf16 v[72:75], a[212:215], a[240:243], v[72:75]\n\t"
    "v_mfma_f32_16x16x32_bf16 v[104:107], a[212:215], a[244:247], v[104:107]\n\t"
    "v_mfma_f32_16x16x32_bf16 v[136:139], a[212:215], a[248:251], v[136:139]\n\t"
    "v_mfma_f32_16x16x32_bf16 v[168:171], a[212:215], a[252:255], v[168:171]\n\t"
    "v_mul_f32 v12, s79, v12\n\t"
    "v_mul_f32 v13, s79, v13\n\t"
    "v_mul_f32 v14, s79, v14\n\t"
    "v_mul_f32 v15, s79, v15\n\t"
    "v_mul_f32 v16, s79, v16\n\t"
    "v_mul_f32 v17, s79, v17\n\t"
    "v_mul_f32 v18, s79, v18\n\t"
    "v_mul_f32 v19, s79, v19\n\t"
    "v_cvt_pk_bf16_f32 v12, v12, v13\n\t"
    "v_cvt_pk_bf16_f32 v13, v14, v15\n\t"
    "v_cvt_pk_bf16_f32 v14, v16, v17\n\t"
    "v_cvt_pk_bf16_f32 v15, v18, v19\n\t"
    "v_mul_f32 v20, s79, v20\n\t"
    "v_permlane32_swap_b32 v12, v14\n\t"
    "v_permlane32_swap_b32 v13, v15\n\t"
    "v_mul_f32 v21, s79, v21\n\t"
    "v_permlane16_swap_b32 v12, v14\n\t"
    "v_permlane16_swap_b32 v13, v15\n\t"
    "v_mul_f32 v22, s79, v22\n\t"
    "v_mul_f32 v23, s79, v23\n\t"
    "v_mul_f32 v24, s79, v24\n\t"
    "v_mul_f32 v25, s79, v25\n\t"
    "v_mul_f32 v26, s79, v26\n\t"
    "v_mul_f32 v27, s79, v27\n\t"
    "v_cvt_pk_bf16_f32 v20, v20, v21\n\t"
    "v_cvt_pk_bf16_f32 v21, v22, v23\n\t"
    "v_cvt_pk_bf16_f32 v22, v24, v25\n\t"
    "v_cvt_pk_bf16_f32 v23, v26, v27\n\t"
    "s_mov_b32 s76, 0\n\t"
    "v_permlane32_swap_b32 v20, v22\n\t"
    "v_permlane32_swap_b32 v21, v23\n\t"
    "s_add_u32 s67, %20, 0x6000\n\t"
    "v_permlane16_swap_b32 v20, v22\n\t"
    "v_permlane16_swap_b32 v21, v23\n\t"
    "s_add_u32 m0, s76, %17\n\t"
    "s_add_u32 m0, m0, 0x4000\n\t"
    "v_mul_f32 v28, s79, v28\n\t"
    "buffer_load_dwordx4 %4, s[44:47], s67 offen nt lds\n\t"
    "v_mul_f32 v29, s79, v29\n\t"
    "v_mul_f32 v30, s79, v30\n\t"
    "v_mul_f32 v31, s79, v31\n\t"
    "v_mul_f32 v32, s79, v32\n\t"
    "v_mul_f32 v33, s79, v33\n\t"
    "v_mul_f32 v34, s79, v34\n\t"
    "v_mul_f32 v35, s79, v35\n\t"
    "v_cvt_pk_bf16_f32 v28, v28, v29\n\t"
    "v_cvt_pk_bf16_f32 v29, v30, v31\n\t"
    "v_cvt_pk_bf16_f32 v30, v32, v33\n\t"
    "v_cvt_pk_bf16_f32 v31, v34, v35\n\t"
    "v_mul_f32 v36, s79, v36\n\t"
    "v_permlane32_swap_b32 v28, v30\n\t"
    "v_permlane32_swap_b32 v29, v31\n\t"
    "v_mul_f32 v37, s79, v37\n\t"
    "v_permlane16_swap_b32 v28, v30\n\t"
    "v_permlane16_swap_b32 v29, v31\n\t"
    "v_mul_f32 v38, s79, v38\n\t"
    "v_mul_f32 v39, s79, v39\n\t"
    "v_mul_f32 v40, s79, v40\n\t"
    "v_mul_f32 v41, s79, v41\n\t"
    "v_mul_f32 v42, s79, v42\n\t"
    "v_mul_f32 v43, s79, v43\n\t"
    "v_cvt_pk_bf16_f32 v36, v36, v37\n\t"
    "v_cvt_pk_bf16_f32 v37, v38, v39\n\t"
    "v_cvt_pk_bf16_f32 v38, v40, v41\n\t"
    "v_cvt_pk_bf16_f32 v39, v42, v43\n\t"
    "v_mul_f32 v44, s79, v44\n\t"
    "v_permlane32_swap_b32 v36, v38\n\t"
    "v_permlane32_swap_b32 v37, v39\n\t"
    "v_mul_f32 v45, s79, v45\n\t"
    "v_permlane16_swap_b32 v36, v38\n\t"
    "v_permlane16_swap_b32 v37, v39\n\t"
    "v_mul_f32 v46, s79, v46\n\t"
    "v_mul_f32 v47, s79, v47\n\t"
    "v_mul_f32 v48, s79, v48\n\t"
    "v_mul_f32 v49, s79, v49\n\t"
    "v_mul_f32 v50, s79, v50\n\t"
    "v_mul_f32 v51, s79, v51\n\t"
    "v_cvt_pk_bf16_f32 v44, v44, v45\n\t"
    "v_cvt_pk_bf16_f32 v45, v46, v47\n\t"
    "v_cvt_pk_bf16_f32 v46, v48, v49\n\t"
    "v_cvt_pk_bf16_f32 v47, v50, v51\n\t"
    "v_mul_f32 v52, s79, v52\n\t"
    "v_permlane32_swap_b32 v44, v46\n\t"
    "v_permlane32_swap_b32 v45, v47\n\t"
    "v_mul_f32 v53, s79, v53\n\t"
    "v_permlane16_swap_b32 v44, v46\n\t"
    "v_permlane16_swap_b32 v45, v47\n\t"
    "v_mul_f32 v54, s79, v54\n\t"
    "v_mul_f32 v55, s79, v55\n\t"
    "v_mul_f32 v56, s79, v56\n\t"
    "v_mul_f32 v57, s79, v57\n\t"
    "v_mul_f32 v58, s79, v58\n\t"
    "v_mul_f32 v59, s79, v59\n\t"
    "v_cvt_pk_bf16_f32 v52, v52, v53\n\t"
    "v_cvt_pk_bf16_f32 v53, v54, v55\n\t"
    "v_cvt_pk_bf16_f32 v54, v56, v57\n\t"
    "v_cvt_pk_bf16_f32 v55, v58, v59\n\t"
    "v_mul_f32 v60, s79, v60\n\t"
    "v_permlane32_swap_b32 v52, v54\n\t"
    "v_permlane32_swap_b32 v53, v55\n\t"
    "buffer_load_dwordx4 v232, s[44:47], s67 offen offset:2048 nt lds\n\t"
    "v_permlane16_swap_b32 v52, v54\n\t"
    "v_permlane16_swap_b32 v53, v55\n\t"
    "v_mul_f32 v61, s79, v61\n\t"
    "v_mul_f32 v62, s79, v62\n\t"
    "v_mul_f32 v63, s79, v63\n\t"
    "v_mul_f32 v64, s79, v64\n\t"
    "v_mul_f32 v65, s79, v65\n\t"
    "v_mul_f32 v66, s79, v66\n\t"
    "v_mul_f32 v67, s79, v67\n\t"
    "v_cvt_pk_bf16_f32 v60, v60, v61\n\t"
    "v_cvt_pk_bf16_f32 v61, v62, v63\n\t"
    "v_cvt_pk_bf16_f32 v62, v64, v65\n\t"
    "v_cvt_pk_bf16_f32 v63, v66, v67\n\t"
    "v_mul_f32 v68, s79, v68\n\t"
    "v_permlane32_swap_b32 v60, v62\n\t"
    "v_permlane32_swap_b32 v61, v63\n\t"
    "v_mul_f32 v69, s79, v69\n\t"
    "v_permlane16_swap_b32 v60, v62\n\t"
    "v_permlane16_swap_b32 v61, v63\n\t"
    "v_mul_f32 v70, s79, v70\n\t"
    "v_mul_f32 v71, s79, v71\n\t"
    "v_mul_f32 v72, s79, v72\n\t"
    "v_mul_f32 v73, s79, v73\n\t"
    "v_mul_f32 v74, s79, v74\n\t"
    "v_mul_f32 v75, s79, v75\n\t"
    "v_cvt_pk_bf16_f32 v68, v68, v69\n\t"
    "v_cvt_pk_bf16_f32 v69, v70, v71\n\t"
    "v_cvt_pk_bf16_f32 v70, v72, v73\n\t"
    "v_cvt_pk_bf16_f32 v71, v74, v75\n\t"
    "v_mul_f32 v76, s79, v76\n\t"
    "v_permlane32_swap_b32 v68, v70\n\t"
    "v_permlane32_swap_b32 v69, v71\n\t"
    "v_mul_f32 v77, s79, v77\n\t"
    "v_permlane16_swap_b32 v68, v70\n\t"
    "v_permlane16_swap_b32 v69, v71\n\t"
    "v_mul_f32 v78, s79, v78\n\t"
    "v_mul_f32 v79, s79, v79\n\t"
    "v_mul_f32 v80, s79, v80\n\t"
    "v_mul_f32 v81, s79, v81\n\t"
    "v_mul_f32 v82, s79, v82\n\t"
    "v_mul_f32 v83, s79, v83\n\t"
    "v_cvt_pk_bf16_f32 v76, v76, v77\n\t"
    "v_cvt_pk_bf16_f32 v77, v78, v79\n\t"
    "v_cvt_pk_bf16_f32 v78, v80, v81\n\t"
    "v_cvt_pk_bf16_f32 v79, v82, v83\n\t"
    "v_mul_f32 v84, s79, v84\n\t"
    "v_permlane32_swap_b32 v76, v78\n\t"
    "v_permlane32_swap_b32 v77, v79\n\t"
    "v_mul_f32 v85, s79, v85\n\t"
    "v_permlane16_swap_b32 v76, v78\n\t"
    "v_permlane16_swap_b32 v77, v79\n\t"
    "v_mul_f32 v86, s79, v86\n\t"
    "v_mul_f32 v87, s79, v87\n\t"
    "v_mul_f32 v88, s79, v88\n\t"
    "v_mul_f32 v89, s79, v89\n\t"
    "v_mul_f32 v90, s79, v90\n\t"
    "v_mul_f32 v91, s79, v91\n\t"
    "v_cvt_pk_bf16_f32 v84, v84, v85\n\t"
    "v_cvt_pk_bf16_f32 v85, v86, v87\n\t"
    "v_cvt_pk_bf16_f32 v86, v88, v89\n\t"
    "v_cvt_pk_bf16_f32 v87, v90, v91\n\t"
    "v_mul_f32 v92, s79, v92\n\t"
    "v_permlane32_swap_b32 v84, v86\n\t"
    "v_permlane32_swap_b32 v85, v87\n\t"
    "v_mul_f32 v93, s79, v93\n\t"
    "v_permlane16_swap_b32 v84, v86\n\t"
    "v_permlane16_swap_b32 v85, v87\n\t"
    "s_add_u32 m0, m0, 0x400\n\t"
    "v_mul_f32 v94, s79, v94\n\t"
    "buffer_load_dwordx4 v233, s[44:47], s67 offen nt lds\n\t"
    "v_mul_f32 v95, s79, v95\n\t"
    "v_mul_f32 v96, s79, v96\n\t"
    "v_mul_f32 v97, s79, v97\n\t"
    "v_mul_f32 v98, s79, v98\n\t"
    "v_mul_f32 v99, s79, v99\n\t"
    "v_cvt_pk_bf16_f32 v92, v92, v93\n\t"
    "v_cvt_pk_bf16_f32 v93, v94, v95\n\t"
    "v_cvt_pk_bf16_f32 v94, v96, v97\n\t"
    "v_cvt_pk_bf16_f32 v95, v98, v99\n\t"
    "v_mul_f32 v100, s79, v100\n\t"
    "v_permlane32_swap_b32 v92, v94\n\t"
    "v_permlane32_swap_b32 v93, v95\n\t"
    "v_mul_f32 v101, s79, v101\n\t"
    "v_permlane16_swap_b32 v92, v94\n\t"
    "v_permlane16_swap_b32 v93, v95\n\t"
    "v_mul_f32 v102, s79, v102\n\t"
    "v_mul_f32 v103, s79, v103\n\t"
    "v_mul_f32 v104, s79, v104\n\t"
    "v_mul_f32 v105, s79, v105\n\t"
    "v_mul_f32 v106, s79, v106\n\t"
    "v_mul_f32 v107, s79, v107\n\t"
    "v_cvt_pk_bf16_f32 v100, v100, v101\n\t"
    "v_cvt_pk_bf16_f32 v101, v102, v103\n\t"
    "v_cvt_pk_bf16_f32 v102, v104, v105\n\t"
    "v_cvt_pk_bf16_f32 v103, v106, v107\n\t"
    "v_mul_f32 v108, s79, v108\n\t"
    "v_permlane32_swap_b32 v100, v102\n\t"
    "v_permlane32_swap_b32 v101, v103\n\t"
    "v_mul_f32 v109, s79, v109\n\t"
    "v_permlane16_swap_b32 v100, v102\n\t"
    "v_permlane16_swap_b32 v101, v103\n\t"
    "v_mul_f32 v110, s79, v110\n\t"
    "v_mul_f32 v111, s79, v111\n\t"
    "v_mul_f32 v112, s79, v112\n\t"
    "v_mul_f32 v113, s79, v113\n\t"
    "v_mul_f32 v114, s79, v114\n\t"
    "v_mul_f32 v115, s79, v115\n\t"
    "v_cvt_pk_bf16_f32 v108, v108, v109\n\t"
    "v_cvt_pk_bf16_f32 v109, v110, v111\n\t"
    "v_cvt_pk_bf16_f32 v110, v112, v113\n\t"
    "v_cvt_pk_bf16_f32 v111, v114, v115\n\t"
    "v_mul_f32 v116, s79, v116\n\t"
    "v_permlane32_swap_b32 v108, v110\n\t"
    "v_permlane32_swap_b32 v109, v111\n\t"
    "v_mul_f32 v117, s79, v117\n\t"
    "v_permlane16_swap_b32 v108, v110\n\t"
    "v_permlane16_swap_b32 v109, v111\n\t"
    "v_mul_f32 v118, s79, v118\n\t"
    "v_mul_f32 v119, s79, v119\n\t"
    "v_mul_f32 v120, s79, v120\n\t"
    "v_mul_f32 v121, s79, v121\n\t"
    "v_mul_f32 v122, s79, v122\n\t"
    "v_mul_f32 v123, s79, v123\n\t"
    "v_cvt_pk_bf16_f32 v116, v116, v117\n\t"
    "v_cvt_pk_bf16_f32 v117, v118, v119\n\t"
    "v_cvt_pk_bf16_f32 v118, v120, v121\n\t"
    "v_cvt_pk_bf16_f32 v119, v122, v123\n\t"
    "v_mul_f32 v124, s79, v124\n\t"
    "v_permlane32_swap_b32 v116, v118\n\t"
    "v_permlane32_swap_b32 v117, v119\n\t"
    "v_mul_f32 v125, s79, v125\n\t"
    "v_permlane16_swap_b32 v116, v118\n\t"
    "v_permlane16_swap_b32 v117, v119\n\t"
    "v_mul_f32 v126, s79, v126\n\t"
    "buffer_load_dwordx4 v234, s[44:47], s67 offen offset:2048 nt lds\n\t"
    "v_mul_f32 v127, s79, v127\n\t"
    "v_mul_f32 v128, s79, v128\n\t"
    "v_mul_f32 v129, s79, v129\n\t"
    "v_mul_f32 v130, s79, v130\n\t"
    "v_mul_f32 v131, s79, v131\n\t"
    "v_cvt_pk_bf16_f32 v124, v124, v125\n\t"
    "v_cvt_pk_bf16_f32 v125, v126, v127\n\t"
    "v_cvt_pk_bf16_f32 v126, v128, v129\n\t"
    "v_cvt_pk_bf16_f32 v127, v130, v131\n\t"
    "v_mul_f32 v132, s79, v132\n\t"
    "v_permlane32_swap_b32 v124, v126\n\t"
    "v_permlane32_swap_b32 v125, v127\n\t"
    "v_mul_f32 v133, s79, v133\n\t"
    "v_permlane16_swap_b32 v124, v126\n\t"
    "v_permlane16_swap_b32 v125, v127\n\t"
    "v_mul_f32 v134, s79, v134\n\t"
    "v_mul_f32 v135, s79, v135\n\t"
    "v_mul_f32 v136, s79, v136\n\t"
    "v_mul_f32 v137, s79, v137\n\t"
    "v_mul_f32 v138, s79, v138\n\t"
    "v_mul_f32 v139, s79, v139\n\t"
    "v_cvt_pk_bf16_f32 v132, v132, v133\n\t"
    "v_cvt_pk_bf16_f32 v133, v134, v135\n\t"
    "v_cvt_pk_bf16_f32 v134, v136, v137\n\t"
    "v_cvt_pk_bf16_f32 v135, v138, v139\n\t"
    "v_mul_f32 v140, s79, v140\n\t"
    "v_permlane32_swap_b32 v132, v134\n\t"
    "v_permlane32_swap_b32 v133, v135\n\t"
    "v_mul_f32 v141, s79, v141\n\t"
    "v_permlane16_swap_b32 v132, v134\n\t"
    "v_permlane16_swap_b32 v133, v135\n\t"
    "v_mul_f32 v142, s79, v142\n\t"
    "v_mul_f32 v143, s79, v143\n\t"
    "v_mul_f32 v144, s79, v144\n\t"
    "v_mul_f32 v145, s79, v145\n\t"
    "v_mul_f32 v146, s79, v146\n\t"
    "v_mul_f32 v147, s79, v147\n\t"
    "v_cvt_pk_bf16_f32 v140, v140, v141\n\t"
    "v_cvt_pk_bf16_f32 v141, v142, v143\n\t"
    "v_cvt_pk_bf16_f32 v142, v144, v145\n\t"
    "v_cvt_pk_bf16_f32 v143, v146, v147\n\t"
    "v_mul_f32 v148, s79, v148\n\t"
    "v_permlane32_swap_b32 v140, v142\n\t"
    "v_permlane32_swap_b32 v141, v143\n\t"
    "v_mul_f32 v149, s79, v149\n\t"
    "v_permlane16_swap_b32 v140, v142\n\t"
    "v_permlane16_swap_b32 v141, v143\n\t"
    "v_mul_f32 v150, s79, v150\n\t"
    "v_mul_f32 v151, s79, v151\n\t"
    "v_mul_f32 v152, s79, v152\n\t"
    "v_mul_f32 v153, s79, v153\n\t"
    "v_mul_f32 v154, s79, v154\n\t"
    "v_mul_f32 v155, s79, v155\n\t"
    "v_cvt_pk_bf16_f32 v148, v148, v149\n\t"
    "v_cvt_pk_bf16_f32 v149, v150, v151\n\t"
    "v_cvt_pk_bf16_f32 v150, v152, v153\n\t"
    "v_cvt_pk_bf16_f32 v151, v154, v155\n\t"
    "v_mul_f32 v156, s79, v156\n\t"
    "v_permlane32_swap_b32 v148, v150\n\t"
    "v_permlane32_swap_b32 v149, v151\n\t"
    "v_mul_f32 v157, s79, v157\n\t"
    "v_permlane16_swap_b32 v148, v150\n\t"
    "v_permlane16_swap_b32 v149, v151\n\t"
    "v_mul_f32 v158, s79, v158\n\t"
    "v_mul_f32 v159, s79, v159\n\t"
    "v_mul_f32 v160, s79, v160\n\t"
    "v_mul_f32 v161, s79, v161\n\t"
    "v_mul_f32 v162, s79, v162\n\t"
    "v_mul_f32 v163, s79, v163\n\t"
    "v_cvt_pk_bf16_f32 v156, v156, v157\n\t"
    "v_cvt_pk_bf16_f32 v157, v158, v159\n\t"
    "v_cvt_pk_bf16_f32 v158, v160, v161\n\t"
    "v_cvt_pk_bf16_f32 v159, v162, v163\n\t"
    "v_mul_f32 v164, s79, v164\n\t"
    "v_permlane32_swap_b32 v156, v158\n\t"
    "v_permlane32_swap_b32 v157, v159\n\t"
    "v_mul_f32 v165, s79, v165\n\t"
    "v_permlane16_swap_b32 v156, v158\n\t"
    "v_permlane16_swap_b32 v157, v159\n\t"
    "v_mul_f32 v166, s79, v166\n\t"
    "v_mul_f32 v167, s79, v167\n\t"
    "v_mul_f32 v168, s79, v168\n\t"
    "v_mul_f32 v169, s79, v169\n\t"
    "v_mul_f32 v170, s79, v170\n\t"
    "v_mul_f32 v171, s79, v171\n\t"
    "v_cvt_pk_bf16_f32 v164, v164, v165\n\t"
    "v_cvt_pk_bf16_f32 v165, v166, v167\n\t"
    "v_cvt_pk_bf16_f32 v166, v168, v169\n\t"
    "v_cvt_pk_bf16_f32 v167, v170, v171\n\t"
    "v_mov_b32 v240, 0x3f803f80\n\t"
    "v_permlane32_swap_b32 v164, v166\n\t"
    "v_permlane32_swap_b32 v165, v167\n\t"
    "v_accvgpr_write_b32 a244, v240\n\t"
    "v_permlane16_swap_b32 v164, v166\n\t"
    "v_permlane16_swap_b32 v165, v167\n\t"
    "v_accvgpr_write_b32 a245, v240\n\t"
    "v_accvgpr_write_b32 a246, v240\n\t"
    "v_accvgpr_write_b32 a247, v240\n\t"
    "s_waitcnt vmcnt(16) lgkmcnt(0)\n\t"
    "s_barrier\n\t"
    "v_add_u32 v235, 0x14000, %6\n\t"
    "ds_read_b128 a[200:203], v235 offset:0\n\t"
    "v_add_u32 v236, 0x14000, v229\n\t"
    "ds_read_b128 a[204:207], v236 offset:0\n\t"
    "ds_read_b128 a[208:211], v235 offset:1024\n\t"
    "ds_read_b128 a[212:215], v236 offset:1024\n\t"
    "ds_read_b128 a[216:219], v235 offset:4096\n\t"
    "ds_read_b128 a[220:223], v236 offset:4096\n\t"
    "ds_read_b128 a[224:227], v235 offset:5120\n\t"
    "ds_read_b128 a[228:231], v236 offset:5120\n\t"
    "s_add_u32 s68, %20, 0x4000\n\t"
    "s_getreg_b32 s86, hwreg(HW_REG_XCC_ID)\n\t"
    "s_mov_b32 s65, 0x20000\n\t"
    "s_mov_b32 s94, 0\n\t"
    "s_mov_b32 s66, 0x8000\n\t"
    "s_add_u32 s67, %20, 0x8000\n\t"
    "s_mov_b32 s69, 7\n\t"
    "s_waitcnt lgkmcnt(7)\n\t"
    "v_mfma_f32_16x16x32_bf16 v[16:19], a[200:203], v[12:15], 0\n\t"
    "v_mfma_f32_16x16x32_bf16 v[24:27], a[200:203], v[44:47], 0\n\t"
    "v_mfma_f32_16x16x32_bf16 v[32:35], a[200:203], v[76:79], 0\n\t"
    "v_mfma_f32_16x16x32_bf16 v[40:43], a[200:203], v[108:111], 0\n\t"
    "v_mfma_f32_16x16x32_bf16 v[48:51], a[200:203], v[140:143], 0\n\t"
    "ds_read_b128 a[200:203], v235 offset:16384\n\t"
    "s_waitcnt lgkmcnt(1)\n\t"
    "v_mfma_f32_16x16x32_bf16 v[16:19], a[204:207], v[20:23], v[16:19]\n\t"
    "v_mfma_f32_16x16x32_bf16 v[24:27], a[204:207], v[52:55], v[24:27]\n\t"
    "v_mfma_f32_16x16x32_bf16 v[32:35], a[204:207], v[84:87], v[32:35]\n\t"
    "v_mfma_f32_16x16x32_bf16 v[40:43], a[204:207], v[116:119], v[40:43]\n\t"
    "v_mfma_f32_16x16x32_bf16 v[48:51], a[204:207], v[148:151], v[48:51]\n\t"
    "ds_read_b128 a[204:207], v236 offset:16384\n\t"
    "v_mfma_f32_16x16x32_bf16 v[16:19], a[208:211], v[28:31], v[16:19]\n\t"
    "v_mfma_f32_16x16x32_bf16 v[24:27], a[208:211], v[60:63], v[24:27]\n\t"
    "v_mfma_f32_16x16x32_bf16 v[32:35], a[208:211], v[92:95], v[32:35]\n\t"
    "v_mfma_f32_16x16x32_bf16 v[40:43], a[208:211], v[124:127], v[40:43]\n\t"
    "v_mfma_f32_16x16x32_bf16 v[48:51], a[208:211], v[156:159], v[48:51]\n\t"
    "ds_read_b128 a[208:211], v235 offset:17408\n\t"
    "v_mfma_f32_16x16x32_bf16 v[16:19], a[212:215], v[36:39], v[16:19]\n\t"
    "v_mfma_f32_16x16x32_bf16 v[24:27], a[212:215], v[68:71], v[24:27]\n\t"
    "v_mfma_f32_16x16x32_bf16 v[32:35], a[212:215], v[100:103], v[32:35]\n\t"
    "v_mfma_f32_16x16x32_bf16 v[40:43], a[212:215], v[132:135], v[40:43]\n\t"
    "s_nop 4\n\t"
    "v_max3_f32 v240, v16, v17, v18\n\t"
    "v_mfma_f32_16x16x32_bf16 v[48:51], a[212:215], v[164:167], v[48:51]\n\t"
    "v_max3_f32 v242, v24, v25, v26\n\t"
    "ds_read_b128 a[212:215], v236 offset:17408\n\t"
    "v_mfma_f32_16x16x32_bf16 v[56:59], a[216:219], v[12:15], 0\n\t"
    "v_max3_f32 v244, v32, v33, v34\n\t"
    "v_mfma_f32_16x16x32_bf16 v[64:67], a[216:219], v[44:47], 0\n\t"
    "v_max3_f32 v246, v40, v41, v42\n\t"
    "v_mfma_f32_16x16x32_bf16 v[72:75], a[216:219], v[76:79], 0\n\t"
    "s_nop 0\n\t"
    "v_max3_f32 v248, v48, v49, v50\n\t"
    "v_mfma_f32_16x16x32_bf16 v[80:83], a[216:219], v[108:111], 0\n\t"
    "v_mfma_f32_16x16x32_bf16 v[88:91], a[216:219], v[140:143], 0\n\t"
    "ds_read_b128 a[216:219], v235 offset:20480\n\t"
    "v_mfma_f32_16x16x32_bf16 v[56:59], a[220:223], v[20:23], v[56:59]\n\t"
    "v_mfma_f32_16x16x32_bf16 v[64:67], a[220:223], v[52:55], v[64:67]\n\t"
    "v_mfma_f32_16x16x32_bf16 v[72:75], a[220:223], v[84:87], v[72:75]\n\t"
    "v_mfma_f32_16x16x32_bf16 v[80:83], a[220:223], v[116:119], v[80:83]\n\t"
    "v_mfma_f32_16x16x32_bf16 v[88:91], a[220:223], v[148:151], v[88:91]\n\t"
    "ds_read_b128 a[220:223], v236 offset:20480\n\t"
    "v_mfma_f32_16x16x32_bf16 v[56:59], a[224:227], v[28:31], v[56:59]\n\t"
    "v_mfma_f32_16x16x32_bf16 v[64:67], a[224:227], v[60:63], v[64:67]\n\t"
    "v_mfma_f32_16x16x32_bf16 v[72:75], a[224:227], v[92:95], v[72:75]\n\t"
    "v_mfma_f32_16x16x32_bf16 v[80:83], a[224:227], v[124:127], v[80:83]\n\t"
    "v_mfma_f32_16x16x32_bf16 v[88:91], a[224:227], v[156:159], v[88:91]\n\t"
    "ds_read_b128 a[224:227], v235 offset:21504\n\t"
    "v_mfma_f32_16x16x32_bf16 v[56:59], a[228:231], v[36:39], v[56:59]\n\t"
    "v_mfma_f32_16x16x32_bf16 v[64:67], a[228:231], v[68:71], v[64:67]\n\t"
    "v_mfma_f32_16x16x32_bf16 v[72:75], a[228:231], v[100:103], v[72:75]\n\t"
    "v_mfma_f32_16x16x32_bf16 v[80:83], a[228:231], v[132:135], v[80:83]\n\t"
    "s_nop 4\n\t"
    "v_max3_f32 v240, v240, v19, v56\n\t"
    "v_max3_f32 v240, v240, v57, v58\n\t"
    "v_mfma_f32_16x16x32_bf16 v[88:91], a[228:231], v[164:167], v[88:91]\n\t"
    "v_max3_f32 v242, v242, v27, v64\n\t"
    "v_max3_f32 v242, v242, v65, v66\n\t"
    "v_max3_f32 v244, v244, v35, v72\n\t"
    "v_max3_f32 v244, v244, v73, v74\n\t"
    "v_max_f32 v240, v240, v59\n\t"
    "v_max_f32 v242, v242, v67\n\t"
    "v_max3_f32 v246, v246, v43, v80\n\t"
    "v_max3_f32 v246, v246, v81, v82\n\t"
    "v_max_f32 v244, v244, v75\n\t"
    "v_max_f32 v246, v246, v83\n\t"
    "v_max3_f32 v248, v248, v51, v88\n\t"
    "v_max3_f32 v248, v248, v89, v90\n\t"
    "v_max_f32 v248, v248, v91\n\t"
    "v_mov_b32 v241, v240\n\t"
    "v_mov_b32 v243, v242\n\t"
    "v_mov_b32 v245, v244\n\t"
    "v_mov_b32 v247, v246\n\t"
    "v_mov_b32 v249, v248\n\t"
    "v_permlane32_swap_b32 v240, v241\n\t"
    "v_permlane32_swap_b32 v242, v243\n\t"
    "v_permlane32_swap_b32 v244, v245\n\t"
    "v_permlane32_swap_b32 v246, v247\n\t"
    "v_permlane32_swap_b32 v248, v249\n\t"
    "v_max_f32 v240, v240, v241\n\t"
    "v_max_f32 v242, v242, v243\n\t"
    "v_max_f32 v244, v244, v245\n\t"
    "v_max_f32 v246, v246, v247\n\t"
    "v_max_f32 v248, v248, v249\n\t"
    "v_mov_b32 v241, v240\n\t"
    "v_mov_b32 v243, v242\n\t"
    "v_mov_b32 v245, v244\n\t"
    "v_mov_b32 v247, v246\n\t"
    "v_mov_b32 v249, v248\n\t"
    "v_permlane16_swap_b32 v240, v241\n\t"
    "v_permlane16_swap_b32 v242, v243\n\t"
    "v_permlane16_swap_b32 v244, v245\n\t"
    "v_permlane16_swap_b32 v246, v247\n\t"
    "v_permlane16_swap_b32 v248, v249\n\t"
    "v_max_f32 v240, v240, v241\n\t"
    "v_max_f32 v242, v242, v243\n\t"
    "v_max_f32 v244, v244, v245\n\t"
    "v_max_f32 v246, v246, v247\n\t"
    "v_max_f32 v248, v248, v249\n\t"
    "v_sub_f32 v241, 0, v240\n\t"
    "v_sub_f32 v243, 0, v242\n\t"
    "v_sub_f32 v245, 0, v244\n\t"
    "v_sub_f32 v247, 0, v246\n\t"
    "v_sub_f32 v249, 0, v248\n\t"
    "ds_read_b128 a[228:231], v236 offset:21504\n\t"
    "v_sub_f32 v16, v16, v240\n\t"
    "v_sub_f32 v17, v17, v240\n\t"
    "v_sub_f32 v18, v18, v240\n\t"
    "v_sub_f32 v19, v19, v240\n\t"
    "v_sub_f32 v56, v56, v240\n\t"
    "v_sub_f32 v57, v57, v240\n\t"
    "v_sub_f32 v58, v58, v240\n\t"
    "v_sub_f32 v59, v59, v240\n\t"
    "v_mov_b32 v192, v241\n\t"
    "v_mov_b32 v193, v241\n\t"
    "v_mov_b32 v194, v241\n\t"
    "v_mov_b32 v195, v241\n\t"
    "v_sub_f32 v24, v24, v242\n\t"
    "v_sub_f32 v25, v25, v242\n\t"
    "v_sub_f32 v26, v26, v242\n\t"
    "v_sub_f32 v27, v27, v242\n\t"
    "v_sub_f32 v64, v64, v242\n\t"
    "v_sub_f32 v65, v65, v242\n\t"
    "v_sub_f32 v66, v66, v242\n\t"
    "v_sub_f32 v67, v67, v242\n\t"
    "v_mov_b32 v196, v243\n\t"
    "v_mov_b32 v197, v243\n\t"
    "v_mov_b32 v198, v243\n\t"
    "v_mov_b32 v199, v243\n\t"
    "v_sub_f32 v32, v32, v244\n\t"
    "v_sub_f32 v33, v33, v244\n\t"
    "v_sub_f32 v34, v34, v244\n\t"
    "v_sub_f32 v35, v35, v244\n\t"
    "v_sub_f32 v72, v72, v244\n\t"
    "v_sub_f32 v73, v73, v244\n\t"
    "v_sub_f32 v74, v74, v244\n\t"
    "v_sub_f32 v75, v75, v244\n\t"
    "v_mov_b32 v200, v245\n\t"
    "v_mov_b32 v201, v245\n\t"
    "v_mov_b32 v202, v245\n\t"
    "v_mov_b32 v203, v245\n\t"
    "v_sub_f32 v40, v40, v246\n\t"
    "v_sub_f32 v41, v41, v246\n\t"
    "v_sub_f32 v42, v42, v246\n\t"
    "v_sub_f32 v43, v43, v246\n\t"
    "v_sub_f32 v80, v80, v246\n\t"
    "v_sub_f32 v81, v81, v246\n\t"
    "v_sub_f32 v82, v82, v246\n\t"
    "v_sub_f32 v83, v83, v246\n\t"
    "v_mov_b32 v204, v247\n\t"
    "v_mov_b32 v205, v247\n\t"
    "v_mov_b32 v206, v247\n\t"
    "v_mov_b32 v207, v247\n\t"
    "v_sub_f32 v48, v48, v248\n\t"
    "v_sub_f32 v49, v49, v248\n\t"
    "v_sub_f32 v50, v50, v248\n\t"
    "v_sub_f32 v51, v51, v248\n\t"
    "v_sub_f32 v88, v88, v248\n\t"
    "v_sub_f32 v89, v89, v248\n\t"
    "v_sub_f32 v90, v90, v248\n\t"
    "v_sub_f32 v91, v91, v248\n\t"
    "v_mov_b32 v208, v249\n\t"
    "v_mov_b32 v209, v249\n\t"
    "v_mov_b32 v210, v249\n\t"
    "v_mov_b32 v211, v249\n\t"
    "K1_ALOOP:\n\t"
    "s_waitcnt vmcnt(8) lgkmcnt(0)\n\t"
    "s_barrier\n\t"
    "v_mfma_f32_16x16x32_bf16 v[96:99], a[200:203], v[12:15], v[192:195]\n\t"
    "s_cmp_lt_i32 s63, 0\n\t"
    "s_cselect_b32 s64, 0x8000, s63\n\t"
    "v_mfma_f32_16x16x32_bf16 v[104:107], a[200:203], v[44:47], v[196:199]\n\t"
    "s_add_u32 m0, %17, 0x1c000\n\t"
    "s_add_u32 s63, s63, 0x1000\n\t"
    "v_mfma_f32_16x16x32_bf16 v[112:115], a[200:203], v[76:79], v[200:203]\n\t"
    "buffer_load_dwordx4 v219, s[36:39], s64 offen lds\n\t"
    "v_mfma_f32_16x16x32_bf16 v[120:123], a[200:203], v[108:111], v[204:207]\n\t"
    "s_bitcmp1_b32 s86, 0\n\t"
    "	s_cbranch_scc1 K1_TH1\n\t"
    "	s_waitcnt vmcnt(4)\n\t"
    "	K1_TH1:\n\t"
    "v_mfma_f32_16x16x32_bf16 v[128:131], a[200:203], v[140:143], v[208:211]\n\t"
    "s_add_u32 m0, s66, %17\n\t"
    "v_add_u32 v237, s65, %7\n\t"
    "v_mfma_f32_16x16x32_bf16 v[96:99], a[204:207], v[20:23], v[96:99]\n\t"
    "buffer_load_dwordx4 %5, s[48:51], s68 offen nt lds\n\t"
    "v_mfma_f32_16x16x32_bf16 v[104:107], a[204:207], v[52:55], v[104:107]\n\t"
    "ds_read_b64_tr_b16 a[180:181], v237 offset:0\n\t"
    "ds_read_b64_tr_b16 a[182:183], v237 offset:4096\n\t"
    "v_mfma_f32_16x16x32_bf16 v[112:115], a[204:207], v[84:87], v[112:115]\n\t"
    "buffer_load_dwordx4 %5, s[48:51], s68 offen offset:2048 nt lds\n\t"
    "v_mfma_f32_16x16x32_bf16 v[120:123], a[204:207], v[116:119], v[120:123]\n\t"
    "v_add_u32 v238, s65, v228\n\t"
    "ds_read_b64_tr_b16 a[184:185], v238 offset:0\n\t"
    "v_mfma_f32_16x16x32_bf16 v[128:131], a[204:207], v[148:151], v[128:131]\n\t"
    "ds_read_b64_tr_b16 a[186:187], v238 offset:4096\n\t"
    "ds_read_b64_tr_b16 a[188:189], v237 offset:512\n\t"
    "v_mfma_f32_16x16x32_bf16 v[96:99], a[208:211], v[28:31], v[96:99]\n\t"
    "ds_read_b64_tr_b16 a[190:191], v237 offset:4608\n\t"
    "ds_read_b64_tr_b16 a[192:193], v238 offset:512\n\t"
    "v_mfma_f32_16x16x32_bf16 v[104:107], a[208:211], v[60:63], v[104:107]\n\t"
    "ds_read_b64_tr_b16 a[194:195], v238 offset:4608\n\t"
    "ds_read_b64_tr_b16 a[196:197], v237 offset:1024\n\t"
    "v_mfma_f32_16x16x32_bf16 v[112:115], a[208:211], v[92:95], v[112:115]\n\t"
    "ds_read_b64_tr_b16 a[198:199], v237 offset:5120\n\t"
    "s_add_u32 m0, m0, 0x400\n\t"
    "v_mfma_f32_16x16x32_bf16 v[120:123], a[208:211], v[124:127], v[120:123]\n\t"
    "ds_read_b64_tr_b16 a[232:233], v238 offset:1024\n\t"
    "ds_read_b64_tr_b16 a[234:235], v238 offset:5120\n\t"
    "v_mfma_f32_16x16x32_bf16 v[128:131], a[208:211], v[156:159], v[128:131]\n\t"
    "ds_read_b64_tr_b16 a[236:237], v237 offset:1536\n\t"
    "ds_read_b64_tr_b16 a[238:239], v237 offset:5632\n\t"
    "v_mfma_f32_16x16x32_bf16 v[96:99], a[212:215], v[36:39], v[96:99]\n\t"
    "v_exp_f32 v16, v16\n\t"
    "v_mfma_f32_16x16x32_bf16 v[104:107], a[212:215], v[68:71], v[104:107]\n\t"
    "v_exp_f32 v17, v17\n\t"
    "v_mfma_f32_16x16x32_bf16 v[112:115], a[212:215], v[100:103], v[112:115]\n\t"
    "v_exp_f32 v18, v18\n\t"
    "v_mfma_f32_16x16x32_bf16 v[120:123], a[212:215], v[132:135], v[120:123]\n\t"
    "v_exp_f32 v19, v19\n\t"
    "v_mfma_f32_16x16x32_bf16 v[128:131], a[212:215], v[164:167], v[128:131]\n\t"
    "v_exp_f32 v56, v56\n\t"
    "v_mfma_f32_16x16x32_bf16 v[136:139], a[216:219], v[12:15], v[192:195]\n\t"
    "v_exp_f32 v57, v57\n\t"
    "v_mfma_f32_16x16x32_bf16 v[144:147], a[216:219], v[44:47], v[196:199]\n\t"
    "v_exp_f32 v58, v58\n\t"
    "v_mfma_f32_16x16x32_bf16 v[152:155], a[216:219], v[76:79], v[200:203]\n\t"
    "v_exp_f32 v59, v59\n\t"
    "v_mfma_f32_16x16x32_bf16 v[160:163], a[216:219], v[108:111], v[204:207]\n\t"
    "ds_read_b64_tr_b16 a[240:241], v238 offset:1536\n\t"
    "s_waitcnt lgkmcnt(3)\n\t"
    "ds_read_b64_tr_b16 a[242:243], v238 offset:5632\n\t"
    "v_mfma_f32_16x16x32_bf16 v[168:171], a[216:219], v[140:143], v[208:211]\n\t"
    "v_cvt_pk_bf16_f32 v172, v16, v17\n\t"
    "v_cvt_pk_bf16_f32 v173, v18, v19\n\t"
    "v_mfma_f32_16x16x32_bf16 v[136:139], a[220:223], v[20:23], v[136:139]\n\t"
    "v_cvt_pk_bf16_f32 v174, v56, v57\n\t"
    "v_cvt_pk_bf16_f32 v175, v58, v59\n\t"
    "v_mfma_f32_16x16x32_bf16 v[144:147], a[220:223], v[52:55], v[144:147]\n\t"
    "buffer_load_dwordx4 v231, s[48:51], s68 offen nt lds\n\t"
    "v_mfma_f32_16x16x32_bf16 v[152:155], a[220:223], v[84:87], v[152:155]\n\t"
    "buffer_load_dwordx4 v231, s[48:51], s68 offen offset:2048 nt lds\n\t"
    "v_mfma_f32_16x16x32_bf16 v[160:163], a[220:223], v[116:119], v[160:163]\n\t"
    "s_add_u32 m0, s66, %17\n\t"
    "v_add_u32 v235, s65, %6\n\t"
    "v_mfma_f32_16x16x32_bf16 v[168:171], a[220:223], v[148:151], v[168:171]\n\t"
    "v_exp_f32 v24, v24\n\t"
    "v_mfma_f32_16x16x32_bf16 v[136:139], a[224:227], v[28:31], v[136:139]\n\t"
    "v_exp_f32 v25, v25\n\t"
    "v_mfma_f32_16x16x32_bf16 v[144:147], a[224:227], v[60:63], v[144:147]\n\t"
    "v_exp_f32 v26, v26\n\t"
    "v_mfma_f32_16x16x32_bf16 v[152:155], a[224:227], v[92:95], v[152:155]\n\t"
    "v_exp_f32 v27, v27\n\t"
    "v_mfma_f32_16x16x32_bf16 v[160:163], a[224:227], v[124:127], v[160:163]\n\t"
    "v_exp_f32 v64, v64\n\t"
    "v_mfma_f32_16x16x32_bf16 v[168:171], a[224:227], v[156:159], v[168:171]\n\t"
    "v_exp_f32 v65, v65\n\t"
    "v_mfma_f32_16x16x32_bf16 v[136:139], a[228:231], v[36:39], v[136:139]\n\t"
    "v_exp_f32 v66, v66\n\t"
    "v_mfma_f32_16x16x32_bf16 v[144:147], a[228:231], v[68:71], v[144:147]\n\t"
    "v_exp_f32 v67, v67\n\t"
    "v_mfma_f32_16x16x32_bf16 v[152:155], a[228:231], v[100:103], v[152:155]\n\t"
    "v_cvt_pk_bf16_f32 v176, v24, v25\n\t"
    "v_cvt_pk_bf16_f32 v177, v26, v27\n\t"
    "v_mfma_f32_16x16x32_bf16 v[160:163], a[228:231], v[132:135], v[160:163]\n\t"
    "v_cvt_pk_bf16_f32 v178, v64, v65\n\t"
    "v_cvt_pk_bf16_f32 v179, v66, v67\n\t"
    "v_mfma_f32_16x16x32_bf16 v[168:171], a[228:231], v[164:167], v[168:171]\n\t"
    "ds_read_b128 a[200:203], v235 offset:0\n\t"
    "s_add_u32 m0, m0, 0x4000\n\t"
    "v_mfma_f32_16x16x32_bf16 a[0:3], a[180:183], v[172:175], a[0:3]\n\t"
    "v_add_u32 v236, s65, v229\n\t"
    "ds_read_b128 a[204:207], v236 offset:0\n\t"
    "v_mfma_f32_16x16x32_bf16 a[20:23], a[184:187], v[172:175], a[20:23]\n\t"
    "ds_read_b128 a[208:211], v235 offset:1024\n\t"
    "ds_read_b128 a[212:215], v236 offset:1024\n\t"
    "v_mfma_f32_16x16x32_bf16 a[40:43], a[188:191], v[172:175], a[40:43]\n\t"
    "buffer_load_dwordx4 %4, s[44:47], s67 offen nt lds\n\t"
    "v_mfma_f32_16x16x32_bf16 a[60:63], a[192:195], v[172:175], a[60:63]\n\t"
    "v_exp_f32 v32, v32\n\t"
    "v_mfma_f32_16x16x32_bf16 a[80:83], a[196:199], v[172:175], a[80:83]\n\t"
    "v_exp_f32 v33, v33\n\t"
    "v_mfma_f32_16x16x32_bf16 a[100:103], a[232:235], v[172:175], a[100:103]\n\t"
    "v_exp_f32 v34, v34\n\t"
    "s_waitcnt lgkmcnt(4)\n\t"
    "v_mfma_f32_16x16x32_bf16 a[120:123], a[236:239], v[172:175], a[120:123]\n\t"
    "v_exp_f32 v35, v35\n\t"
    "v_mfma_f32_16x16x32_bf16 a[140:143], a[240:243], v[172:175], a[140:143]\n\t"
    "v_exp_f32 v72, v72\n\t"
    "v_mfma_f32_16x16x32_bf16 a[160:163], a[244:247], v[172:175], a[160:163]\n\t"
    "v_exp_f32 v73, v73\n\t"
    "v_mfma_f32_16x16x32_bf16 a[4:7], a[180:183], v[176:179], a[4:7]\n\t"
    "v_exp_f32 v74, v74\n\t"
    "v_mfma_f32_16x16x32_bf16 a[24:27], a[184:187], v[176:179], a[24:27]\n\t"
    "v_exp_f32 v75, v75\n\t"
    "v_mfma_f32_16x16x32_bf16 a[44:47], a[188:191], v[176:179], a[44:47]\n\t"
    "v_cvt_pk_bf16_f32 v180, v32, v33\n\t"
    "v_cvt_pk_bf16_f32 v181, v34, v35\n\t"
    "v_mfma_f32_16x16x32_bf16 a[64:67], a[192:195], v[176:179], a[64:67]\n\t"
    "v_cvt_pk_bf16_f32 v182, v72, v73\n\t"
    "v_cvt_pk_bf16_f32 v183, v74, v75\n\t"
    "v_mfma_f32_16x16x32_bf16 a[84:87], a[196:199], v[176:179], a[84:87]\n\t"
    "ds_read_b128 a[216:219], v235 offset:4096\n\t"
    "ds_read_b128 a[220:223], v236 offset:4096\n\t"
    "v_mfma_f32_16x16x32_bf16 a[104:107], a[232:235], v[176:179], a[104:107]\n\t"
    "buffer_load_dwordx4 v232, s[44:47], s67 offen offset:2048 nt lds\n\t"
    "v_mfma_f32_16x16x32_bf16 a[124:127], a[236:239], v[176:179], a[124:127]\n\t"
    "ds_read_b128 a[224:227], v235 offset:5120\n\t"
    "s_add_u32 m0, m0, 0x400\n\t"
    "v_mfma_f32_16x16x32_bf16 a[144:147], a[240:243], v[176:179], a[144:147]\n\t"
    "ds_read_b128 a[228:231], v236 offset:5120\n\t"
    "v_max3_f32 v240, v96, v97, v98\n\t"
    "v_mfma_f32_16x16x32_bf16 a[164:167], a[244:247], v[176:179], a[164:167]\n\t"
    "v_exp_f32 v40, v40\n\t"
    "v_mfma_f32_16x16x32_bf16 a[8:11], a[180:183], v[180:183], a[8:11]\n\t"
    "v_exp_f32 v41, v41\n\t"
    "v_mfma_f32_16x16x32_bf16 a[28:31], a[184:187], v[180:183], a[28:31]\n\t"
    "v_exp_f32 v42, v42\n\t"
    "v_mfma_f32_16x16x32_bf16 a[48:51], a[188:191], v[180:183], a[48:51]\n\t"
    "v_exp_f32 v43, v43\n\t"
    "v_mfma_f32_16x16x32_bf16 a[68:71], a[192:195], v[180:183], a[68:71]\n\t"
    "v_exp_f32 v80, v80\n\t"
    "v_mfma_f32_16x16x32_bf16 a[88:91], a[196:199], v[180:183], a[88:91]\n\t"
    "v_exp_f32 v81, v81\n\t"
    "v_mfma_f32_16x16x32_bf16 a[108:111], a[232:235], v[180:183], a[108:111]\n\t"
    "v_exp_f32 v82, v82\n\t"
    "v_mfma_f32_16x16x32_bf16 a[128:131], a[236:239], v[180:183], a[128:131]\n\t"
    "v_exp_f32 v83, v83\n\t"
    "v_mfma_f32_16x16x32_bf16 a[148:151], a[240:243], v[180:183], a[148:151]\n\t"
    "buffer_load_dwordx4 v233, s[44:47], s67 offen nt lds\n\t"
    "v_mfma_f32_16x16x32_bf16 a[168:171], a[244:247], v[180:183], a[168:171]\n\t"
    "v_cvt_pk_bf16_f32 v184, v40, v41\n\t"
    "v_cvt_pk_bf16_f32 v185, v42, v43\n\t"
    "v_cvt_pk_bf16_f32 v186, v80, v81\n\t"
    "v_cvt_pk_bf16_f32 v187, v82, v83\n\t"
    "buffer_load_dwordx4 v234, s[44:47], s67 offen offset:2048 nt lds\n\t"
    "s_nop 0\n\t"
    "v_mfma_f32_16x16x32_bf16 a[12:15], a[180:183], v[184:187], a[12:15]\n\t"
    "v_exp_f32 v48, v48\n\t"
    "v_mfma_f32_16x16x32_bf16 a[32:35], a[184:187], v[184:187], a[32:35]\n\t"
    "v_exp_f32 v49, v49\n\t"
    "v_mfma_f32_16x16x32_bf16 a[52:55], a[188:191], v[184:187], a[52:55]\n\t"
    "v_exp_f32 v50, v50\n\t"
    "v_mfma_f32_16x16x32_bf16 a[72:75], a[192:195], v[184:187], a[72:75]\n\t"
    "v_exp_f32 v51, v51\n\t"
    "v_mfma_f32_16x16x32_bf16 a[92:95], a[196:199], v[184:187], a[92:95]\n\t"
    "v_exp_f32 v88, v88\n\t"
    "v_mfma_f32_16x16x32_bf16 a[112:115], a[232:235], v[184:187], a[112:115]\n\t"
    "v_exp_f32 v89, v89\n\t"
    "v_mfma_f32_16x16x32_bf16 a[132:135], a[236:239], v[184:187], a[132:135]\n\t"
    "v_exp_f32 v90, v90\n\t"
    "v_mfma_f32_16x16x32_bf16 a[152:155], a[240:243], v[184:187], a[152:155]\n\t"
    "v_exp_f32 v91, v91\n\t"
    "v_mfma_f32_16x16x32_bf16 a[172:175], a[244:247], v[184:187], a[172:175]\n\t"
    "v_cvt_pk_bf16_f32 v188, v48, v49\n\t"
    "v_cvt_pk_bf16_f32 v189, v50, v51\n\t"
    "v_cvt_pk_bf16_f32 v190, v88, v89\n\t"
    "v_cvt_pk_bf16_f32 v191, v90, v91\n\t"
    "v_max3_f32 v241, v136, v137, v138\n\t"
    "v_max3_f32 v240, v240, v99, v104\n\t"
    "v_mfma_f32_16x16x32_bf16 a[16:19], a[180:183], v[188:191], a[16:19]\n\t"
    "v_max3_f32 v241, v241, v139, v144\n\t"
    "v_max3_f32 v240, v240, v105, v106\n\t"
    "v_mfma_f32_16x16x32_bf16 a[36:39], a[184:187], v[188:191], a[36:39]\n\t"
    "v_max3_f32 v241, v241, v145, v146\n\t"
    "v_max3_f32 v240, v240, v107, v112\n\t"
    "v_mfma_f32_16x16x32_bf16 a[56:59], a[188:191], v[188:191], a[56:59]\n\t"
    "v_max3_f32 v241, v241, v147, v152\n\t"
    "v_max3_f32 v240, v240, v113, v114\n\t"
    "v_mfma_f32_16x16x32_bf16 a[76:79], a[192:195], v[188:191], a[76:79]\n\t"
    "v_max3_f32 v241, v241, v153, v154\n\t"
    "v_max3_f32 v240, v240, v115, v120\n\t"
    "v_mfma_f32_16x16x32_bf16 a[96:99], a[196:199], v[188:191], a[96:99]\n\t"
    "v_max3_f32 v241, v241, v155, v160\n\t"
    "s_mov_b32 s65, s94\n\t"
    "v_mfma_f32_16x16x32_bf16 a[116:119], a[232:235], v[188:191], a[116:119]\n\t"
    "v_max3_f32 v240, v240, v121, v122\n\t"
    "v_max3_f32 v241, v241, v161, v162\n\t"
    "v_mfma_f32_16x16x32_bf16 a[136:139], a[236:239], v[188:191], a[136:139]\n\t"
    "s_mov_b32 s94, s66\n\t"
    "v_max3_f32 v240, v240, v123, v128\n\t"
    "v_mfma_f32_16x16x32_bf16 a[156:159], a[240:243], v[188:191], a[156:159]\n\t"
    "v_max3_f32 v241, v241, v163, v168\n\t"
    "s_add_u32 s66, s66, 0x8000\n\t"
    "v_mfma_f32_16x16x32_bf16 a[176:179], a[244:247], v[188:191], a[176:179]\n\t"
    "v_max3_f32 v240, v240, v129, v130\n\t"
    "v_max3_f32 v241, v241, v169, v170\n\t"
    "s_cmp_eq_u32 s66, 0x18000\n\t"
    "v_max_f32 v240, v240, v131\n\t"
    "v_max_f32 v241, v241, v171\n\t"
    "s_cselect_b32 s66, 0, s66\n\t"
    "v_max_f32 v240, v240, v241\n\t"
    "s_add_u32 s68, s68, 0x2000\n\t"
    "v_cmp_lt_f32 vcc, 0x41000000, v240\n\t"
    "s_add_u32 s67, s67, 0x2000\n\t"
    "s_nop 2\n\t"
    "s_cbranch_vccz K1_NR0\n\t"
    "s_swappc_b64 s[74:75], s[72:73]\n\t"
    "K1_NR0:\n\t"
    "s_waitcnt vmcnt(8) lgkmcnt(0)\n\t"
    "s_barrier\n\t"
    "v_mfma_f32_16x16x32_bf16 v[16:19], a[200:203], v[12:15], v[192:195]\n\t"
    "s_cmp_lt_i32 s63, 0\n\t"
    "s_cselect_b32 s64, 0x8000, s63\n\t"
    "v_mfma_f32_16x16x32_bf16 v[24:27], a[200:203], v[44:47], v[196:199]\n\t"
    "s_add_u32 m0, %17, 0x1c000\n\t"
    "s_add_u32 s63, s63, 0x1000\n\t"
    "v_mfma_f32_16x16x32_bf16 v[32:35], a[200:203], v[76:79], v[200:203]\n\t"
    "buffer_load_dwordx4 v219, s[36:39], s64 offen lds\n\t"
    "v_mfma_f32_16x16x32_bf16 v[40:43], a[200:203], v[108:111], v[204:207]\n\t"
    "s_bitcmp1_b32 s86, 0\n\t"
    "	s_cbranch_scc1 K1_TH2\n\t"
    "	s_waitcnt vmcnt(4)\n\t"
    "	K1_TH2:\n\t"
    "v_mfma_f32_16x16x32_bf16 v[48:51], a[200:203], v[140:143], v[208:211]\n\t"
    "s_add_u32 m0, s66, %17\n\t"
    "v_add_u32 v237, s65, %7\n\t"
    "v_mfma_f32_16x16x32_bf16 v[16:19], a[204:207], v[20:23], v[16:19]\n\t"
    "buffer_load_dwordx4 %5, s[48:51], s68 offen nt lds\n\t"
    "v_mfma_f32_16x16x32_bf16 v[24:27], a[204:207], v[52:55], v[24:27]\n\t"
    "ds_read_b64_tr_b16 a[180:181], v237 offset:0\n\t"
    "ds_read_b64_tr_b16 a[182:183], v237 offset:4096\n\t"
    "v_mfma_f32_16x16x32_bf16 v[32:35], a[204:207], v[84:87], v[32:35]\n\t"
    "buffer_load_dwordx4 %5, s[48:51], s68 offen offset:2048 nt lds\n\t"
    "v_mfma_f32_16x16x32_bf16 v[40:43], a[204:207], v[116:119], v[40:43]\n\t"
    "v_add_u32 v238, s65, v228\n\t"
    "ds_read_b64_tr_b16 a[184:185], v238 offset:0\n\t"
    "v_mfma_f32_16x16x32_bf16 v[48:51], a[204:207], v[148:151], v[48:51]\n\t"
    "ds_read_b64_tr_b16 a[186:187], v238 offset:4096\n\t"
    "ds_read_b64_tr_b16 a[188:189], v237 offset:512\n\t"
    "v_mfma_f32_16x16x32_bf16 v[16:19], a[208:211], v[28:31], v[16:19]\n\t"
    "ds_read_b64_tr_b16 a[190:191], v237 offset:4608\n\t"
    "ds_read_b64_tr_b16 a[192:193], v238 offset:512\n\t"
    "v_mfma_f32_16x16x32_bf16 v[24:27], a[208:211], v[60:63], v[24:27]\n\t"
    "ds_read_b64_tr_b16 a[194:195], v238 offset:4608\n\t"
    "ds_read_b64_tr_b16 a[196:197], v237 offset:1024\n\t"
    "v_mfma_f32_16x16x32_bf16 v[32:35], a[208:211], v[92:95], v[32:35]\n\t"
    "ds_read_b64_tr_b16 a[198:199], v237 offset:5120\n\t"
    "s_add_u32 m0, m0, 0x400\n\t"
    "v_mfma_f32_16x16x32_bf16 v[40:43], a[208:211], v[124:127], v[40:43]\n\t"
    "ds_read_b64_tr_b16 a[232:233], v238 offset:1024\n\t"
    "ds_read_b64_tr_b16 a[234:235], v238 offset:5120\n\t"
    "v_mfma_f32_16x16x32_bf16 v[48:51], a[208:211], v[156:159], v[48:51]\n\t"
    "ds_read_b64_tr_b16 a[236:237], v237 offset:1536\n\t"
    "ds_read_b64_tr_b16 a[238:239], v237 offset:5632\n\t"
    "v_mfma_f32_16x16x32_bf16 v[16:19], a[212:215], v[36:39], v[16:19]\n\t"
    "v_exp_f32 v96, v96\n\t"
    "v_mfma_f32_16x16x32_bf16 v[24:27], a[212:215], v[68:71], v[24:27]\n\t"
    "v_exp_f32 v97, v97\n\t"
    "v_mfma_f32_16x16x32_bf16 v[32:35], a[212:215], v[100:103], v[32:35]\n\t"
    "v_exp_f32 v98, v98\n\t"
    "v_mfma_f32_16x16x32_bf16 v[40:43], a[212:215], v[132:135], v[40:43]\n\t"
    "v_exp_f32 v99, v99\n\t"
    "v_mfma_f32_16x16x32_bf16 v[48:51], a[212:215], v[164:167], v[48:51]\n\t"
    "v_exp_f32 v136, v136\n\t"
    "v_mfma_f32_16x16x32_bf16 v[56:59], a[216:219], v[12:15], v[192:195]\n\t"
    "v_exp_f32 v137, v137\n\t"
    "v_mfma_f32_16x16x32_bf16 v[64:67], a[216:219], v[44:47], v[196:199]\n\t"
    "v_exp_f32 v138, v138\n\t"
    "v_mfma_f32_16x16x32_bf16 v[72:75], a[216:219], v[76:79], v[200:203]\n\t"
    "v_exp_f32 v139, v139\n\t"
    "v_mfma_f32_16x16x32_bf16 v[80:83], a[216:219], v[108:111], v[204:207]\n\t"
    "ds_read_b64_tr_b16 a[240:241], v238 offset:1536\n\t"
    "s_waitcnt lgkmcnt(3)\n\t"
    "ds_read_b64_tr_b16 a[242:243], v238 offset:5632\n\t"
    "v_mfma_f32_16x16x32_bf16 v[88:91], a[216:219], v[140:143], v[208:211]\n\t"
    "v_cvt_pk_bf16_f32 v172, v96, v97\n\t"
    "v_cvt_pk_bf16_f32 v173, v98, v99\n\t"
    "v_mfma_f32_16x16x32_bf16 v[56:59], a[220:223], v[20:23], v[56:59]\n\t"
    "v_cvt_pk_bf16_f32 v174, v136, v137\n\t"
    "v_cvt_pk_bf16_f32 v175, v138, v139\n\t"
    "v_mfma_f32_16x16x32_bf16 v[64:67], a[220:223], v[52:55], v[64:67]\n\t"
    "buffer_load_dwordx4 v231, s[48:51], s68 offen nt lds\n\t"
    "v_mfma_f32_16x16x32_bf16 v[72:75], a[220:223], v[84:87], v[72:75]\n\t"
    "buffer_load_dwordx4 v231, s[48:51], s68 offen offset:2048 nt lds\n\t"
    "v_mfma_f32_16x16x32_bf16 v[80:83], a[220:223], v[116:119], v[80:83]\n\t"
    "s_add_u32 m0, s66, %17\n\t"
    "v_add_u32 v235, s65, %6\n\t"
    "v_mfma_f32_16x16x32_bf16 v[88:91], a[220:223], v[148:151], v[88:91]\n\t"
    "v_exp_f32 v104, v104\n\t"
    "v_mfma_f32_16x16x32_bf16 v[56:59], a[224:227], v[28:31], v[56:59]\n\t"
    "v_exp_f32 v105, v105\n\t"
    "v_mfma_f32_16x16x32_bf16 v[64:67], a[224:227], v[60:63], v[64:67]\n\t"
    "v_exp_f32 v106, v106\n\t"
    "v_mfma_f32_16x16x32_bf16 v[72:75], a[224:227], v[92:95], v[72:75]\n\t"
    "v_exp_f32 v107, v107\n\t"
    "v_mfma_f32_16x16x32_bf16 v[80:83], a[224:227], v[124:127], v[80:83]\n\t"
    "v_exp_f32 v144, v144\n\t"
    "v_mfma_f32_16x16x32_bf16 v[88:91], a[224:227], v[156:159], v[88:91]\n\t"
    "v_exp_f32 v145, v145\n\t"
    "v_mfma_f32_16x16x32_bf16 v[56:59], a[228:231], v[36:39], v[56:59]\n\t"
    "v_exp_f32 v146, v146\n\t"
    "v_mfma_f32_16x16x32_bf16 v[64:67], a[228:231], v[68:71], v[64:67]\n\t"
    "v_exp_f32 v147, v147\n\t"
    "v_mfma_f32_16x16x32_bf16 v[72:75], a[228:231], v[100:103], v[72:75]\n\t"
    "v_cvt_pk_bf16_f32 v176, v104, v105\n\t"
    "v_cvt_pk_bf16_f32 v177, v106, v107\n\t"
    "v_mfma_f32_16x16x32_bf16 v[80:83], a[228:231], v[132:135], v[80:83]\n\t"
    "v_cvt_pk_bf16_f32 v178, v144, v145\n\t"
    "v_cvt_pk_bf16_f32 v179, v146, v147\n\t"
    "v_mfma_f32_16x16x32_bf16 v[88:91], a[228:231], v[164:167], v[88:91]\n\t"
    "ds_read_b128 a[200:203], v235 offset:0\n\t"
    "s_add_u32 m0, m0, 0x4000\n\t"
    "v_mfma_f32_16x16x32_bf16 a[0:3], a[180:183], v[172:175], a[0:3]\n\t"
    "v_add_u32 v236, s65, v229\n\t"
    "ds_read_b128 a[204:207], v236 offset:0\n\t"
    "v_mfma_f32_16x16x32_bf16 a[20:23], a[184:187], v[172:175], a[20:23]\n\t"
    "ds_read_b128 a[208:211], v235 offset:1024\n\t"
    "ds_read_b128 a[212:215], v236 offset:1024\n\t"
    "v_mfma_f32_16x16x32_bf16 a[40:43], a[188:191], v[172:175], a[40:43]\n\t"
    "buffer_load_dwordx4 %4, s[44:47], s67 offen nt lds\n\t"
    "v_mfma_f32_16x16x32_bf16 a[60:63], a[192:195], v[172:175], a[60:63]\n\t"
    "v_exp_f32 v112, v112\n\t"
    "v_mfma_f32_16x16x32_bf16 a[80:83], a[196:199], v[172:175], a[80:83]\n\t"
    "v_exp_f32 v113, v113\n\t"
    "v_mfma_f32_16x16x32_bf16 a[100:103], a[232:235], v[172:175], a[100:103]\n\t"
    "v_exp_f32 v114, v114\n\t"
    "s_waitcnt lgkmcnt(4)\n\t"
    "v_mfma_f32_16x16x32_bf16 a[120:123], a[236:239], v[172:175], a[120:123]\n\t"
    "v_exp_f32 v115, v115\n\t"
    "v_mfma_f32_16x16x32_bf16 a[140:143], a[240:243], v[172:175], a[140:143]\n\t"
    "v_exp_f32 v152, v152\n\t"
    "v_mfma_f32_16x16x32_bf16 a[160:163], a[244:247], v[172:175], a[160:163]\n\t"
    "v_exp_f32 v153, v153\n\t"
    "v_mfma_f32_16x16x32_bf16 a[4:7], a[180:183], v[176:179], a[4:7]\n\t"
    "v_exp_f32 v154, v154\n\t"
    "v_mfma_f32_16x16x32_bf16 a[24:27], a[184:187], v[176:179], a[24:27]\n\t"
    "v_exp_f32 v155, v155\n\t"
    "v_mfma_f32_16x16x32_bf16 a[44:47], a[188:191], v[176:179], a[44:47]\n\t"
    "v_cvt_pk_bf16_f32 v180, v112, v113\n\t"
    "v_cvt_pk_bf16_f32 v181, v114, v115\n\t"
    "v_mfma_f32_16x16x32_bf16 a[64:67], a[192:195], v[176:179], a[64:67]\n\t"
    "v_cvt_pk_bf16_f32 v182, v152, v153\n\t"
    "v_cvt_pk_bf16_f32 v183, v154, v155\n\t"
    "v_mfma_f32_16x16x32_bf16 a[84:87], a[196:199], v[176:179], a[84:87]\n\t"
    "ds_read_b128 a[216:219], v235 offset:4096\n\t"
    "ds_read_b128 a[220:223], v236 offset:4096\n\t"
    "v_mfma_f32_16x16x32_bf16 a[104:107], a[232:235], v[176:179], a[104:107]\n\t"
    "buffer_load_dwordx4 v232, s[44:47], s67 offen offset:2048 nt lds\n\t"
    "v_mfma_f32_16x16x32_bf16 a[124:127], a[236:239], v[176:179], a[124:127]\n\t"
    "ds_read_b128 a[224:227], v235 offset:5120\n\t"
    "s_add_u32 m0, m0, 0x400\n\t"
    "v_mfma_f32_16x16x32_bf16 a[144:147], a[240:243], v[176:179], a[144:147]\n\t"
    "ds_read_b128 a[228:231], v236 offset:5120\n\t"
    "v_max3_f32 v240, v16, v17, v18\n\t"
    "v_mfma_f32_16x16x32_bf16 a[164:167], a[244:247], v[176:179], a[164:167]\n\t"
    "v_exp_f32 v120, v120\n\t"
    "v_mfma_f32_16x16x32_bf16 a[8:11], a[180:183], v[180:183], a[8:11]\n\t"
    "v_exp_f32 v121, v121\n\t"
    "v_mfma_f32_16x16x32_bf16 a[28:31], a[184:187], v[180:183], a[28:31]\n\t"
    "v_exp_f32 v122, v122\n\t"
    "v_mfma_f32_16x16x32_bf16 a[48:51], a[188:191], v[180:183], a[48:51]\n\t"
    "v_exp_f32 v123, v123\n\t"
    "v_mfma_f32_16x16x32_bf16 a[68:71], a[192:195], v[180:183], a[68:71]\n\t"
    "v_exp_f32 v160, v160\n\t"
    "v_mfma_f32_16x16x32_bf16 a[88:91], a[196:199], v[180:183], a[88:91]\n\t"
    "v_exp_f32 v161, v161\n\t"
    "v_mfma_f32_16x16x32_bf16 a[108:111], a[232:235], v[180:183], a[108:111]\n\t"
    "v_exp_f32 v162, v162\n\t"
    "v_mfma_f32_16x16x32_bf16 a[128:131], a[236:239], v[180:183], a[128:131]\n\t"
    "v_exp_f32 v163, v163\n\t"
    "v_mfma_f32_16x16x32_bf16 a[148:151], a[240:243], v[180:183], a[148:151]\n\t"
    "buffer_load_dwordx4 v233, s[44:47], s67 offen nt lds\n\t"
    "v_mfma_f32_16x16x32_bf16 a[168:171], a[244:247], v[180:183], a[168:171]\n\t"
    "v_cvt_pk_bf16_f32 v184, v120, v121\n\t"
    "v_cvt_pk_bf16_f32 v185, v122, v123\n\t"
    "v_cvt_pk_bf16_f32 v186, v160, v161\n\t"
    "v_cvt_pk_bf16_f32 v187, v162, v163\n\t"
    "buffer_load_dwordx4 v234, s[44:47], s67 offen offset:2048 nt lds\n\t"
    "s_nop 0\n\t"
    "v_mfma_f32_16x16x32_bf16 a[12:15], a[180:183], v[184:187], a[12:15]\n\t"
    "v_exp_f32 v128, v128\n\t"
    "v_mfma_f32_16x16x32_bf16 a[32:35], a[184:187], v[184:187], a[32:35]\n\t"
    "v_exp_f32 v129, v129\n\t"
    "v_mfma_f32_16x16x32_bf16 a[52:55], a[188:191], v[184:187], a[52:55]\n\t"
    "v_exp_f32 v130, v130\n\t"
    "v_mfma_f32_16x16x32_bf16 a[72:75], a[192:195], v[184:187], a[72:75]\n\t"
    "v_exp_f32 v131, v131\n\t"
    "v_mfma_f32_16x16x32_bf16 a[92:95], a[196:199], v[184:187], a[92:95]\n\t"
    "v_exp_f32 v168, v168\n\t"
    "v_mfma_f32_16x16x32_bf16 a[112:115], a[232:235], v[184:187], a[112:115]\n\t"
    "v_exp_f32 v169, v169\n\t"
    "v_mfma_f32_16x16x32_bf16 a[132:135], a[236:239], v[184:187], a[132:135]\n\t"
    "v_exp_f32 v170, v170\n\t"
    "v_mfma_f32_16x16x32_bf16 a[152:155], a[240:243], v[184:187], a[152:155]\n\t"
    "v_exp_f32 v171, v171\n\t"
    "v_mfma_f32_16x16x32_bf16 a[172:175], a[244:247], v[184:187], a[172:175]\n\t"
    "v_cvt_pk_bf16_f32 v188, v128, v129\n\t"
    "v_cvt_pk_bf16_f32 v189, v130, v131\n\t"
    "v_cvt_pk_bf16_f32 v190, v168, v169\n\t"
    "v_cvt_pk_bf16_f32 v191, v170, v171\n\t"
    "v_max3_f32 v241, v56, v57, v58\n\t"
    "v_max3_f32 v240, v240, v19, v24\n\t"
    "v_mfma_f32_16x16x32_bf16 a[16:19], a[180:183], v[188:191], a[16:19]\n\t"
    "v_max3_f32 v241, v241, v59, v64\n\t"
    "v_max3_f32 v240, v240, v25, v26\n\t"
    "v_mfma_f32_16x16x32_bf16 a[36:39], a[184:187], v[188:191], a[36:39]\n\t"
    "v_max3_f32 v241, v241, v65, v66\n\t"
    "v_max3_f32 v240, v240, v27, v32\n\t"
    "v_mfma_f32_16x16x32_bf16 a[56:59], a[188:191], v[188:191], a[56:59]\n\t"
    "v_max3_f32 v241, v241, v67, v72\n\t"
    "v_max3_f32 v240, v240, v33, v34\n\t"
    "v_mfma_f32_16x16x32_bf16 a[76:79], a[192:195], v[188:191], a[76:79]\n\t"
    "v_max3_f32 v241, v241, v73, v74\n\t"
    "v_max3_f32 v240, v240, v35, v40\n\t"
    "v_mfma_f32_16x16x32_bf16 a[96:99], a[196:199], v[188:191], a[96:99]\n\t"
    "v_max3_f32 v241, v241, v75, v80\n\t"
    "s_mov_b32 s65, s94\n\t"
    "v_mfma_f32_16x16x32_bf16 a[116:119], a[232:235], v[188:191], a[116:119]\n\t"
    "v_max3_f32 v240, v240, v41, v42\n\t"
    "v_max3_f32 v241, v241, v81, v82\n\t"
    "v_mfma_f32_16x16x32_bf16 a[136:139], a[236:239], v[188:191], a[136:139]\n\t"
    "s_mov_b32 s94, s66\n\t"
    "v_max3_f32 v240, v240, v43, v48\n\t"
    "v_mfma_f32_16x16x32_bf16 a[156:159], a[240:243], v[188:191], a[156:159]\n\t"
    "v_max3_f32 v241, v241, v83, v88\n\t"
    "s_add_u32 s66, s66, 0x8000\n\t"
    "v_mfma_f32_16x16x32_bf16 a[176:179], a[244:247], v[188:191], a[176:179]\n\t"
    "v_max3_f32 v240, v240, v49, v50\n\t"
    "v_max3_f32 v241, v241, v89, v90\n\t"
    "s_cmp_eq_u32 s66, 0x18000\n\t"
    "v_max_f32 v240, v240, v51\n\t"
    "v_max_f32 v241, v241, v91\n\t"
    "s_cselect_b32 s66, 0, s66\n\t"
    "v_max_f32 v240, v240, v241\n\t"
    "s_add_u32 s68, s68, 0x2000\n\t"
    "v_cmp_lt_f32 vcc, 0x41000000, v240\n\t"
    "s_add_u32 s67, s67, 0x2000\n\t"
    "s_nop 2\n\t"
    "s_cbranch_vccz K1_NR1\n\t"
    "s_swappc_b64 s[74:75], s[70:71]\n\t"
    "K1_NR1:\n\t"
    "s_sub_u32 s69, s69, 1\n\t"
    "s_cmp_lg_u32 s69, 0\n\t"
    "s_cbranch_scc1 K1_ALOOP\n\t"
    "s_waitcnt vmcnt(8) lgkmcnt(0)\n\t"
    "s_barrier\n\t"
    "v_mfma_f32_16x16x32_bf16 v[96:99], a[200:203], v[12:15], v[192:195]\n\t"
    "s_mov_b64 s[36:37], %15\n\t"
    "s_and_b32 s37, s37, 0xffff\n\t"
    "v_mfma_f32_16x16x32_bf16 v[104:107], a[200:203], v[44:47], v[196:199]\n\t"
    "s_bfe_u32 s77, %23, 0x20002\n\t"
    "s_bfe_u32 s64, %23, 0x10004\n\t"
    "v_mfma_f32_16x16x32_bf16 v[112:115], a[200:203], v[76:79], v[200:203]\n\t"
    "s_lshl_b32 s64, s64, 17\n\t"
    "s_bfe_u32 s69, %23, 0x10005\n\t"
    "v_mfma_f32_16x16x32_bf16 v[120:123], a[200:203], v[108:111], v[204:207]\n\t"
    "s_lshl_b32 s69, s69, 14\n\t"
    "s_add_u32 s78, s64, s69\n\t"
    "v_mfma_f32_16x16x32_bf16 v[128:131], a[200:203], v[140:143], v[208:211]\n\t"
    "s_add_u32 s60, s77, 0\n\t"
    "s_and_b32 s60, s60, 3\n\t"
    "v_mfma_f32_16x16x32_bf16 v[96:99], a[204:207], v[20:23], v[96:99]\n\t"
    "s_lshl_b32 s60, s60, 18\n\t"
    "s_add_u32 s60, s60, s78\n\t"
    "v_mfma_f32_16x16x32_bf16 v[104:107], a[204:207], v[52:55], v[104:107]\n\t"
    "s_add_u32 s61, s77, 1\n\t"
    "s_and_b32 s61, s61, 3\n\t"
    "v_mfma_f32_16x16x32_bf16 v[112:115], a[204:207], v[84:87], v[112:115]\n\t"
    "s_lshl_b32 s61, s61, 18\n\t"
    "s_add_u32 s61, s61, s78\n\t"
    "v_mfma_f32_16x16x32_bf16 v[120:123], a[204:207], v[116:119], v[120:123]\n\t"
    "s_add_u32 s62, s77, 2\n\t"
    "s_and_b32 s62, s62, 3\n\t"
    "v_mfma_f32_16x16x32_bf16 v[128:131], a[204:207], v[148:151], v[128:131]\n\t"
    "s_lshl_b32 s62, s62, 18\n\t"
    "s_add_u32 s62, s62, s78\n\t"
    "v_mfma_f32_16x16x32_bf16 v[96:99], a[208:211], v[28:31], v[96:99]\n\t"
    "s_add_u32 s63, s77, 3\n\t"
    "s_and_b32 s63, s63, 3\n\t"
    "v_mfma_f32_16x16x32_bf16 v[104:107], a[208:211], v[60:63], v[104:107]\n\t"
    "s_lshl_b32 s63, s63, 18\n\t"
    "s_add_u32 s63, s63, s78\n\t"
    "v_mfma_f32_16x16x32_bf16 v[112:115], a[208:211], v[92:95], v[112:115]\n\t"
    "s_add_u32 s64, s60, 0\n\t"
    "s_add_u32 m0, %17, 0x18000\n\t"
    "v_mfma_f32_16x16x32_bf16 v[120:123], a[208:211], v[124:127], v[120:123]\n\t"
    "s_mov_b32 s38, 0x100000\n\t"
    "s_mov_b32 s39, 0x20000\n\t"
    "v_mfma_f32_16x16x32_bf16 v[128:131], a[208:211], v[156:159], v[128:131]\n\t"
    "buffer_load_dwordx4 %9, s[36:39], s64 offen lds\n\t"
    "v_mfma_f32_16x16x32_bf16 v[96:99], a[212:215], v[36:39], v[96:99]\n\t"
    "s_add_u32 m0, m0, 0x400\n\t"
    "v_xor_b32 v230, 0x80, %9\n\t"
    "v_mfma_f32_16x16x32_bf16 v[104:107], a[212:215], v[68:71], v[104:107]\n\t"
    "buffer_load_dwordx4 v230, s[36:39], s64 offen lds\n\t"
    "v_mfma_f32_16x16x32_bf16 v[112:115], a[212:215], v[100:103], v[112:115]\n\t"
    "s_add_u32 m0, m0, 0x400\n\t"
    "v_add_u32 v219, 0x2000, %9\n\t"
    "v_mfma_f32_16x16x32_bf16 v[120:123], a[212:215], v[132:135], v[120:123]\n\t"
    "buffer_load_dwordx4 v219, s[36:39], s64 offen lds\n\t"
    "v_mfma_f32_16x16x32_bf16 v[128:131], a[212:215], v[164:167], v[128:131]\n\t"
    "s_add_u32 m0, m0, 0x400\n\t"
    "v_add_u32 v220, 0x2000, v230\n\t"
    "v_mfma_f32_16x16x32_bf16 v[136:139], a[216:219], v[12:15], v[192:195]\n\t"
    "buffer_load_dwordx4 v220, s[36:39], s64 offen lds\n\t"
    "v_mfma_f32_16x16x32_bf16 v[144:147], a[216:219], v[44:47], v[196:199]\n\t"
    "s_add_u32 s69, s60, 0x8000\n\t"
    "s_add_u32 m0, %17, 0x1c000\n\t"
    "v_mfma_f32_16x16x32_bf16 v[152:155], a[216:219], v[76:79], v[200:203]\n\t"
    "buffer_load_dwordx4 %9, s[36:39], s69 offen lds\n\t"
    "v_mfma_f32_16x16x32_bf16 v[160:163], a[216:219], v[108:111], v[204:207]\n\t"
    "s_add_u32 m0, m0, 0x400\n\t"
    "v_add_u32 v237, s65, %7\n\t"
    "v_mfma_f32_16x16x32_bf16 v[168:171], a[216:219], v[140:143], v[208:211]\n\t"
    "buffer_load_dwordx4 v230, s[36:39], s69 offen lds\n\t"
    "v_mfma_f32_16x16x32_bf16 v[136:139], a[220:223], v[20:23], v[136:139]\n\t"
    "ds_read_b64_tr_b16 a[180:181], v237 offset:0\n\t"
    "ds_read_b64_tr_b16 a[182:183], v237 offset:4096\n\t"
    "v_mfma_f32_16x16x32_bf16 v[144:147], a[220:223], v[52:55], v[144:147]\n\t"
    "v_add_u32 v238, s65, v228\n\t"
    "ds_read_b64_tr_b16 a[184:185], v238 offset:0\n\t"
    "v_mfma_f32_16x16x32_bf16 v[152:155], a[220:223], v[84:87], v[152:155]\n\t"
    "ds_read_b64_tr_b16 a[186:187], v238 offset:4096\n\t"
    "s_add_u32 m0, m0, 0x400\n\t"
    "v_mfma_f32_16x16x32_bf16 v[160:163], a[220:223], v[116:119], v[160:163]\n\t"
    "ds_read_b64_tr_b16 a[188:189], v237 offset:512\n\t"
    "ds_read_b64_tr_b16 a[190:191], v237 offset:4608\n\t"
    "v_mfma_f32_16x16x32_bf16 v[168:171], a[220:223], v[148:151], v[168:171]\n\t"
    "ds_read_b64_tr_b16 a[192:193], v238 offset:512\n\t"
    "ds_read_b64_tr_b16 a[194:195], v238 offset:4608\n\t"
    "v_mfma_f32_16x16x32_bf16 v[136:139], a[224:227], v[28:31], v[136:139]\n\t"
    "buffer_load_dwordx4 v219, s[36:39], s69 offen lds\n\t"
    "v_mfma_f32_16x16x32_bf16 v[144:147], a[224:227], v[60:63], v[144:147]\n\t"
    "ds_read_b64_tr_b16 a[196:197], v237 offset:1024\n\t"
    "ds_read_b64_tr_b16 a[198:199], v237 offset:5120\n\t"
    "v_mfma_f32_16x16x32_bf16 v[152:155], a[224:227], v[92:95], v[152:155]\n\t"
    "s_add_u32 m0, m0, 0x400\n\t"
    "ds_read_b64_tr_b16 a[232:233], v238 offset:1024\n\t"
    "v_mfma_f32_16x16x32_bf16 v[160:163], a[224:227], v[124:127], v[160:163]\n\t"
    "ds_read_b64_tr_b16 a[234:235], v238 offset:5120\n\t"
    "ds_read_b64_tr_b16 a[236:237], v237 offset:1536\n\t"
    "v_mfma_f32_16x16x32_bf16 v[168:171], a[224:227], v[156:159], v[168:171]\n\t"
    "ds_read_b64_tr_b16 a[238:239], v237 offset:5632\n\t"
    "ds_read_b64_tr_b16 a[240:241], v238 offset:1536\n\t"
    "v_mfma_f32_16x16x32_bf16 v[136:139], a[228:231], v[36:39], v[136:139]\n\t"
    "v_exp_f32 v16, v16\n\t"
    "v_mfma_f32_16x16x32_bf16 v[144:147], a[228:231], v[68:71], v[144:147]\n\t"
    "v_exp_f32 v17, v17\n\t"
    "v_mfma_f32_16x16x32_bf16 v[152:155], a[228:231], v[100:103], v[152:155]\n\t"
    "v_exp_f32 v18, v18\n\t"
    "v_mfma_f32_16x16x32_bf16 v[160:163], a[228:231], v[132:135], v[160:163]\n\t"
    "v_exp_f32 v19, v19\n\t"
    "v_mfma_f32_16x16x32_bf16 v[168:171], a[228:231], v[164:167], v[168:171]\n\t"
    "v_exp_f32 v56, v56\n\t"
    "v_exp_f32 v57, v57\n\t"
    "v_exp_f32 v58, v58\n\t"
    "v_exp_f32 v59, v59\n\t"
    "buffer_load_dwordx4 v220, s[36:39], s69 offen lds\n\t"
    "s_waitcnt lgkmcnt(5)\n\t"
    "ds_read_b64_tr_b16 a[242:243], v238 offset:5632\n\t"
    "v_cvt_pk_bf16_f32 v172, v16, v17\n\t"
    "v_cvt_pk_bf16_f32 v173, v18, v19\n\t"
    "v_cvt_pk_bf16_f32 v174, v56, v57\n\t"
    "v_cvt_pk_bf16_f32 v175, v58, v59\n\t"
    "s_add_u32 s64, s60, 0x10000\n\t"
    "s_add_u32 m0, %17, 0x20000\n\t"
    "v_mfma_f32_16x16x32_bf16 a[0:3], a[180:183], v[172:175], a[0:3]\n\t"
    "buffer_load_dwordx4 %9, s[36:39], s64 offen lds\n\t"
    "v_mfma_f32_16x16x32_bf16 a[20:23], a[184:187], v[172:175], a[20:23]\n\t"
    "s_add_u32 m0, m0, 0x400\n\t"
    "v_max3_f32 v240, v96, v97, v98\n\t"
    "v_mfma_f32_16x16x32_bf16 a[40:43], a[188:191], v[172:175], a[40:43]\n\t"
    "buffer_load_dwordx4 v230, s[36:39], s64 offen lds\n\t"
    "v_mfma_f32_16x16x32_bf16 a[60:63], a[192:195], v[172:175], a[60:63]\n\t"
    "v_exp_f32 v24, v24\n\t"
    "v_mfma_f32_16x16x32_bf16 a[80:83], a[196:199], v[172:175], a[80:83]\n\t"
    "v_exp_f32 v25, v25\n\t"
    "s_waitcnt lgkmcnt(1)\n\t"
    "v_mfma_f32_16x16x32_bf16 a[100:103], a[232:235], v[172:175], a[100:103]\n\t"
    "v_exp_f32 v26, v26\n\t"
    "v_mfma_f32_16x16x32_bf16 a[120:123], a[236:239], v[172:175], a[120:123]\n\t"
    "v_exp_f32 v27, v27\n\t"
    "s_waitcnt lgkmcnt(0)\n\t"
    "v_mfma_f32_16x16x32_bf16 a[140:143], a[240:243], v[172:175], a[140:143]\n\t"
    "v_exp_f32 v64, v64\n\t"
    "v_mfma_f32_16x16x32_bf16 a[160:163], a[244:247], v[172:175], a[160:163]\n\t"
    "v_exp_f32 v65, v65\n\t"
    "v_exp_f32 v66, v66\n\t"
    "v_exp_f32 v67, v67\n\t"
    "v_cvt_pk_bf16_f32 v176, v24, v25\n\t"
    "v_cvt_pk_bf16_f32 v177, v26, v27\n\t"
    "v_cvt_pk_bf16_f32 v178, v64, v65\n\t"
    "v_cvt_pk_bf16_f32 v179, v66, v67\n\t"
    "s_add_u32 m0, m0, 0x400\n\t"
    "v_exp_f32 v32, v32\n\t"
    "v_mfma_f32_16x16x32_bf16 a[4:7], a[180:183], v[176:179], a[4:7]\n\t"
    "buffer_load_dwordx4 v219, s[36:39], s64 offen lds\n\t"
    "v_mfma_f32_16x16x32_bf16 a[24:27], a[184:187], v[176:179], a[24:27]\n\t"
    "s_add_u32 m0, m0, 0x400\n\t"
    "v_max3_f32 v241, v136, v137, v138\n\t"
    "v_mfma_f32_16x16x32_bf16 a[44:47], a[188:191], v[176:179], a[44:47]\n\t"
    "buffer_load_dwordx4 v220, s[36:39], s64 offen lds\n\t"
    "v_mfma_f32_16x16x32_bf16 a[64:67], a[192:195], v[176:179], a[64:67]\n\t"
    "s_add_u32 s64, s61, 0\n\t"
    "s_add_u32 m0, %17, 0\n\t"
    "v_mfma_f32_16x16x32_bf16 a[84:87], a[196:199], v[176:179], a[84:87]\n\t"
    "v_exp_f32 v33, v33\n\t"
    "v_mfma_f32_16x16x32_bf16 a[104:107], a[232:235], v[176:179], a[104:107]\n\t"
    "v_exp_f32 v34, v34\n\t"
    "v_mfma_f32_16x16x32_bf16 a[124:127], a[236:239], v[176:179], a[124:127]\n\t"
    "v_exp_f32 v35, v35\n\t"
    "v_mfma_f32_16x16x32_bf16 a[144:147], a[240:243], v[176:179], a[144:147]\n\t"
    "v_exp_f32 v72, v72\n\t"
    "v_mfma_f32_16x16x32_bf16 a[164:167], a[244:247], v[176:179], a[164:167]\n\t"
    "v_exp_f32 v73, v73\n\t"
    "v_exp_f32 v74, v74\n\t"
    "v_exp_f32 v75, v75\n\t"
    "v_cvt_pk_bf16_f32 v180, v32, v33\n\t"
    "v_cvt_pk_bf16_f32 v181, v34, v35\n\t"
    "v_cvt_pk_bf16_f32 v182, v72, v73\n\t"
    "v_cvt_pk_bf16_f32 v183, v74, v75\n\t"
    "buffer_load_dwordx4 %9, s[36:39], s64 offen lds\n\t"
    "s_nop 0\n\t"
    "v_mfma_f32_16x16x32_bf16 a[8:11], a[180:183], v[180:183], a[8:11]\n\t"
    "s_add_u32 m0, m0, 0x400\n\t"
    "v_max3_f32 v240, v240, v99, v104\n\t"
    "v_mfma_f32_16x16x32_bf16 a[28:31], a[184:187], v[180:183], a[28:31]\n\t"
    "buffer_load_dwordx4 v230, s[36:39], s64 offen lds\n\t"
    "v_mfma_f32_16x16x32_bf16 a[48:51], a[188:191], v[180:183], a[48:51]\n\t"
    "s_add_u32 m0, m0, 0x400\n\t"
    "v_max3_f32 v241, v241, v139, v144\n\t"
    "v_mfma_f32_16x16x32_bf16 a[68:71], a[192:195], v[180:183], a[68:71]\n\t"
    "v_exp_f32 v40, v40\n\t"
    "v_mfma_f32_16x16x32_bf16 a[88:91], a[196:199], v[180:183], a[88:91]\n\t"
    "v_exp_f32 v41, v41\n\t"
    "v_mfma_f32_16x16x32_bf16 a[108:111], a[232:235], v[180:183], a[108:111]\n\t"
    "v_exp_f32 v42, v42\n\t"
    "v_mfma_f32_16x16x32_bf16 a[128:131], a[236:239], v[180:183], a[128:131]\n\t"
    "v_exp_f32 v43, v43\n\t"
    "v_mfma_f32_16x16x32_bf16 a[148:151], a[240:243], v[180:183], a[148:151]\n\t"
    "v_exp_f32 v80, v80\n\t"
    "v_mfma_f32_16x16x32_bf16 a[168:171], a[244:247], v[180:183], a[168:171]\n\t"
    "v_exp_f32 v81, v81\n\t"
    "v_exp_f32 v82, v82\n\t"
    "v_exp_f32 v83, v83\n\t"
    "buffer_load_dwordx4 v219, s[36:39], s64 offen lds\n\t"
    "v_cvt_pk_bf16_f32 v184, v40, v41\n\t"
    "v_cvt_pk_bf16_f32 v185, v42, v43\n\t"
    "v_cvt_pk_bf16_f32 v186, v80, v81\n\t"
    "v_cvt_pk_bf16_f32 v187, v82, v83\n\t"
    "s_add_u32 m0, m0, 0x400\n\t"
    "s_add_u32 s69, s61, 0x8000\n\t"
    "v_mfma_f32_16x16x32_bf16 a[12:15], a[180:183], v[184:187], a[12:15]\n\t"
    "buffer_load_dwordx4 v220, s[36:39], s64 offen lds\n\t"
    "v_mfma_f32_16x16x32_bf16 a[32:35], a[184:187], v[184:187], a[32:35]\n\t"
    "s_add_u32 m0, %17, 0x4000\n\t"
    "v_max3_f32 v240, v240, v105, v106\n\t"
    "v_mfma_f32_16x16x32_bf16 a[52:55], a[188:191], v[184:187], a[52:55]\n\t"
    "buffer_load_dwordx4 %9, s[36:39], s69 offen lds\n\t"
    "v_mfma_f32_16x16x32_bf16 a[72:75], a[192:195], v[184:187], a[72:75]\n\t"
    "v_exp_f32 v48, v48\n\t"
    "v_mfma_f32_16x16x32_bf16 a[92:95], a[196:199], v[184:187], a[92:95]\n\t"
    "v_exp_f32 v49, v49\n\t"
    "v_mfma_f32_16x16x32_bf16 a[112:115], a[232:235], v[184:187], a[112:115]\n\t"
    "v_exp_f32 v50, v50\n\t"
    "v_mfma_f32_16x16x32_bf16 a[132:135], a[236:239], v[184:187], a[132:135]\n\t"
    "v_exp_f32 v51, v51\n\t"
    "v_mfma_f32_16x16x32_bf16 a[152:155], a[240:243], v[184:187], a[152:155]\n\t"
    "v_exp_f32 v88, v88\n\t"
    "v_mfma_f32_16x16x32_bf16 a[172:175], a[244:247], v[184:187], a[172:175]\n\t"
    "v_exp_f32 v89, v89\n\t"
    "v_exp_f32 v90, v90\n\t"
    "v_exp_f32 v91, v91\n\t"
    "v_cvt_pk_bf16_f32 v188, v48, v49\n\t"
    "v_cvt_pk_bf16_f32 v189, v50, v51\n\t"
    "v_cvt_pk_bf16_f32 v190, v88, v89\n\t"
    "v_cvt_pk_bf16_f32 v191, v90, v91\n\t"
    "s_add_u32 m0, m0, 0x400\n\t"
    "v_max3_f32 v241, v241, v145, v146\n\t"
    "v_mfma_f32_16x16x32_bf16 a[16:19], a[180:183], v[188:191], a[16:19]\n\t"
    "buffer_load_dwordx4 v230, s[36:39], s69 offen lds\n\t"
    "v_mfma_f32_16x16x32_bf16 a[36:39], a[184:187], v[188:191], a[36:39]\n\t"
    "s_add_u32 m0, m0, 0x400\n\t"
    "v_max3_f32 v240, v240, v107, v112\n\t"
    "v_mfma_f32_16x16x32_bf16 a[56:59], a[188:191], v[188:191], a[56:59]\n\t"
    "buffer_load_dwordx4 v219, s[36:39], s69 offen lds\n\t"
    "v_mfma_f32_16x16x32_bf16 a[76:79], a[192:195], v[188:191], a[76:79]\n\t"
    "s_add_u32 m0, m0, 0x400\n\t"
    "v_max3_f32 v241, v241, v147, v152\n\t"
    "v_mfma_f32_16x16x32_bf16 a[96:99], a[196:199], v[188:191], a[96:99]\n\t"
    "buffer_load_dwordx4 v220, s[36:39], s69 offen lds\n\t"
    "v_mfma_f32_16x16x32_bf16 a[116:119], a[232:235], v[188:191], a[116:119]\n\t"
    "v_add_u32 v235, s65, %6\n\t"
    "v_add_u32 v236, s65, v229\n\t"
    "v_mfma_f32_16x16x32_bf16 a[136:139], a[236:239], v[188:191], a[136:139]\n\t"
    "v_max3_f32 v240, v240, v113, v114\n\t"
    "v_max3_f32 v241, v241, v153, v154\n\t"
    "v_mfma_f32_16x16x32_bf16 a[156:159], a[240:243], v[188:191], a[156:159]\n\t"
    "v_max3_f32 v240, v240, v115, v120\n\t"
    "v_max3_f32 v241, v241, v155, v160\n\t"
    "v_mfma_f32_16x16x32_bf16 a[176:179], a[244:247], v[188:191], a[176:179]\n\t"
    "s_mov_b32 s65, s94\n\t"
    "v_max3_f32 v240, v240, v121, v122\n\t"
    "v_max3_f32 v241, v241, v161, v162\n\t"
    "s_mov_b32 s94, s66\n\t"
    "v_max3_f32 v240, v240, v123, v128\n\t"
    "v_max3_f32 v241, v241, v163, v168\n\t"
    "s_add_u32 s66, s66, 0x8000\n\t"
    "v_max3_f32 v240, v240, v129, v130\n\t"
    "v_max3_f32 v241, v241, v169, v170\n\t"
    "s_cmp_eq_u32 s66, 0x18000\n\t"
    "v_max_f32 v240, v240, v131\n\t"
    "v_max_f32 v241, v241, v171\n\t"
    "s_cselect_b32 s66, 0, s66\n\t"
    "v_max_f32 v240, v240, v241\n\t"
    "s_add_u32 s68, s68, 0x2000\n\t"
    "v_cmp_lt_f32 vcc, 0x41000000, v240\n\t"
    "s_add_u32 s67, s67, 0x2000\n\t"
    "s_nop 2\n\t"
    "s_cbranch_vccz K1_NR2\n\t"
    "s_swappc_b64 s[74:75], s[72:73]\n\t"
    "K1_NR2:\n\t"
    "s_waitcnt vmcnt(20) lgkmcnt(0)\n\t"
    "s_barrier\n\t"
    "s_add_u32 s69, s60, 0x18000\n\t"
    "s_add_u32 m0, %17, 0x24000\n\t"
    "v_add_u32 v237, s65, %7\n\t"
    "buffer_load_dwordx4 %9, s[36:39], s69 offen lds\n\t"
    "s_add_u32 m0, m0, 0x400\n\t"
    "ds_read_b64_tr_b16 a[180:181], v237 offset:0\n\t"
    "buffer_load_dwordx4 v230, s[36:39], s69 offen lds\n\t"
    "s_add_u32 m0, m0, 0x400\n\t"
    "ds_read_b64_tr_b16 a[182:183], v237 offset:4096\n\t"
    "buffer_load_dwordx4 v219, s[36:39], s69 offen lds\n\t"
    "s_add_u32 m0, m0, 0x400\n\t"
    "s_add_u32 s64, s61, 0x10000\n\t"
    "buffer_load_dwordx4 v220, s[36:39], s69 offen lds\n\t"
    "s_add_u32 m0, %17, 0x8000\n\t"
    "v_add_u32 v238, s65, v228\n\t"
    "buffer_load_dwordx4 %9, s[36:39], s64 offen lds\n\t"
    "s_add_u32 m0, m0, 0x400\n\t"
    "ds_read_b64_tr_b16 a[184:185], v238 offset:0\n\t"
    "buffer_load_dwordx4 v230, s[36:39], s64 offen lds\n\t"
    "s_add_u32 m0, m0, 0x400\n\t"
    "ds_read_b64_tr_b16 a[186:187], v238 offset:4096\n\t"
    "buffer_load_dwordx4 v219, s[36:39], s64 offen lds\n\t"
    "s_add_u32 m0, m0, 0x400\n\t"
    "s_add_u32 s69, s61, 0x18000\n\t"
    "buffer_load_dwordx4 v220, s[36:39], s64 offen lds\n\t"
    "s_add_u32 m0, %17, 0xc000\n\t"
    "ds_read_b64_tr_b16 a[188:189], v237 offset:512\n\t"
    "ds_read_b64_tr_b16 a[190:191], v237 offset:4608\n\t"
    "buffer_load_dwordx4 %9, s[36:39], s69 offen lds\n\t"
    "s_add_u32 m0, m0, 0x400\n\t"
    "ds_read_b64_tr_b16 a[192:193], v238 offset:512\n\t"
    "ds_read_b64_tr_b16 a[194:195], v238 offset:4608\n\t"
    "buffer_load_dwordx4 v230, s[36:39], s69 offen lds\n\t"
    "s_add_u32 m0, m0, 0x400\n\t"
    "ds_read_b64_tr_b16 a[196:197], v237 offset:1024\n\t"
    "ds_read_b64_tr_b16 a[198:199], v237 offset:5120\n\t"
    "buffer_load_dwordx4 v219, s[36:39], s69 offen lds\n\t"
    "s_add_u32 m0, m0, 0x400\n\t"
    "ds_read_b64_tr_b16 a[232:233], v238 offset:1024\n\t"
    "ds_read_b64_tr_b16 a[234:235], v238 offset:5120\n\t"
    "s_bfe_u32 s79, %23, 0x20000\n\t"
    "s_bfe_u32 s78, %23, 0x10006\n\t"
    "s_bfe_u32 s74, %23, 0x30007\n\t"
    "s_lshl_b32 s74, s74, 2\n\t"
    "ds_read_b64_tr_b16 a[236:237], v237 offset:1536\n\t"
    "ds_read_b64_tr_b16 a[238:239], v237 offset:5632\n\t"
    "s_add_u32 s74, s74, s77\n\t"
    "s_bfe_u32 s75, %23, 0x10004\n\t"
    "v_exp_f32 v96, v96\n\t"
    "v_exp_f32 v97, v97\n\t"
    "v_exp_f32 v98, v98\n\t"
    "v_exp_f32 v99, v99\n\t"
    "v_exp_f32 v136, v136\n\t"
    "v_exp_f32 v137, v137\n\t"
    "v_exp_f32 v138, v138\n\t"
    "v_exp_f32 v139, v139\n\t"
    "s_lshl_b32 s75, s75, 1\n\t"
    "s_bfe_u32 s64, %23, 0x10005\n\t"
    "ds_read_b64_tr_b16 a[240:241], v238 offset:1536\n\t"
    "s_waitcnt lgkmcnt(9)\n\t"
    "ds_read_b64_tr_b16 a[242:243], v238 offset:5632\n\t"
    "s_add_u32 s75, s75, s64\n\t"
    "v_cvt_pk_bf16_f32 v172, v96, v97\n\t"
    "v_cvt_pk_bf16_f32 v173, v98, v99\n\t"
    "v_cvt_pk_bf16_f32 v174, v136, v137\n\t"
    "v_cvt_pk_bf16_f32 v175, v138, v139\n\t"
    "s_lshl_b32 s78, s78, 2\n\t"
    "s_lshl_b32 s74, s74, 3\n\t"
    "v_mfma_f32_16x16x32_bf16 a[0:3], a[180:183], v[172:175], a[0:3]\n\t"
    "s_add_u32 s74, s74, s78\n\t"
    "s_add_u32 s94, s77, 0\n\t"
    "v_mfma_f32_16x16x32_bf16 a[20:23], a[184:187], v[172:175], a[20:23]\n\t"
    "s_and_b32 s94, s94, 3\n\t"
    "s_mul_i32 s94, s94, 0x14000\n\t"
    "v_mfma_f32_16x16x32_bf16 a[40:43], a[188:191], v[172:175], a[40:43]\n\t"
    "s_add_u32 s94, s94, %26\n\t"
    "s_add_u32 s66, s77, 1\n\t"
    "s_waitcnt lgkmcnt(2)\n\t"
    "v_mfma_f32_16x16x32_bf16 a[60:63], a[192:195], v[172:175], a[60:63]\n\t"
    "s_and_b32 s66, s66, 3\n\t"
    "s_mul_i32 s66, s66, 0x14000\n\t"
    "v_mfma_f32_16x16x32_bf16 a[80:83], a[196:199], v[172:175], a[80:83]\n\t"
    "s_mul_i32 s75, s75, 0x1e00\n\t"
    "s_add_u32 s66, s66, %26\n\t"
    "v_mfma_f32_16x16x32_bf16 a[100:103], a[232:235], v[172:175], a[100:103]\n\t"
    "s_add_u32 s67, s77, 2\n\t"
    "s_and_b32 s67, s67, 3\n\t"
    "v_mfma_f32_16x16x32_bf16 a[120:123], a[236:239], v[172:175], a[120:123]\n\t"
    "s_mul_i32 s67, s67, 0x14000\n\t"
    "s_add_u32 s67, s67, %26\n\t"
    "s_waitcnt lgkmcnt(0)\n\t"
    "v_mfma_f32_16x16x32_bf16 a[140:143], a[240:243], v[172:175], a[140:143]\n\t"
    "s_add_u32 s68, s77, 3\n\t"
    "s_and_b32 s68, s68, 3\n\t"
    "v_mfma_f32_16x16x32_bf16 a[160:163], a[244:247], v[172:175], a[160:163]\n\t"
    "s_mul_i32 s68, s68, 0x14000\n\t"
    "s_add_u32 s68, s68, %26\n\t"
    "s_add_u32 s73, s79, 0\n\t"
    "s_and_b32 s73, s73, 3\n\t"
    "s_add_u32 s73, s73, s74\n\t"
    "s_mul_i32 s73, s73, 0x7800\n\t"
    "s_add_u32 s73, s73, s75\n\t"
    "s_add_u32 s70, s79, 1\n\t"
    "s_and_b32 s70, s70, 3\n\t"
    "s_add_u32 s70, s70, s74\n\t"
    "s_mul_i32 s70, s70, 0x7800\n\t"
    "s_add_u32 s70, s70, s75\n\t"
    "s_add_u32 s71, s79, 2\n\t"
    "s_and_b32 s71, s71, 3\n\t"
    "s_add_u32 s71, s71, s74\n\t"
    "v_exp_f32 v104, v104\n\t"
    "v_exp_f32 v105, v105\n\t"
    "v_exp_f32 v106, v106\n\t"
    "v_exp_f32 v107, v107\n\t"
    "v_exp_f32 v144, v144\n\t"
    "v_exp_f32 v145, v145\n\t"
    "v_exp_f32 v146, v146\n\t"
    "v_exp_f32 v147, v147\n\t"
    "s_mul_i32 s71, s71, 0x7800\n\t"
    "s_add_u32 s71, s71, s75\n\t"
    "s_add_u32 s71, s71, 0xa00\n\t"
    "v_cvt_pk_bf16_f32 v176, v104, v105\n\t"
    "v_cvt_pk_bf16_f32 v177, v106, v107\n\t"
    "v_cvt_pk_bf16_f32 v178, v144, v145\n\t"
    "v_cvt_pk_bf16_f32 v179, v146, v147\n\t"
    "s_add_u32 s72, s79, 3\n\t"
    "s_and_b32 s72, s72, 3\n\t"
    "v_mfma_f32_16x16x32_bf16 a[4:7], a[180:183], v[176:179], a[4:7]\n\t"
    "s_add_u32 s72, s72, s74\n\t"
    "s_mul_i32 s72, s72, 0x7800\n\t"
    "v_mfma_f32_16x16x32_bf16 a[24:27], a[184:187], v[176:179], a[24:27]\n\t"
    "s_add_u32 s72, s72, s75\n\t"
    "s_add_u32 s72, s72, 0x1400\n\t"
    "v_mfma_f32_16x16x32_bf16 a[44:47], a[188:191], v[176:179], a[44:47]\n\t"
    "s_bfe_u32 s64, %23, 0x10004\n\t"
    "s_lshl_b32 s64, s64, 5\n\t"
    "v_mfma_f32_16x16x32_bf16 a[64:67], a[192:195], v[176:179], a[64:67]\n\t"
    "v_xor_b32 v221, s64, %7\n\t"
    "v_xor_b32 v222, 32, v221\n\t"
    "v_mfma_f32_16x16x32_bf16 a[84:87], a[196:199], v[176:179], a[84:87]\n\t"
    "v_add_u32 v223, 0x10000, v221\n\t"
    "v_add_u32 v224, 0x10000, v222\n\t"
    "v_mfma_f32_16x16x32_bf16 a[104:107], a[232:235], v[176:179], a[104:107]\n\t"
    "v_add_u32 v225, 0x20000, v221\n\t"
    "v_add_u32 v226, 0x20000, v222\n\t"
    "v_mfma_f32_16x16x32_bf16 a[124:127], a[236:239], v[176:179], a[124:127]\n\t"
    "v_add_u32 v227, %24, v239\n\t"
    "v_and_b32 v231, 0xf0, v239\n\t"
    "v_mfma_f32_16x16x32_bf16 a[144:147], a[240:243], v[176:179], a[144:147]\n\t"
    "v_lshlrev_b32 v231, 7, v231\n\t"
    "v_lshrrev_b32 v232, 5, v239\n\t"
    "v_mfma_f32_16x16x32_bf16 a[164:167], a[244:247], v[176:179], a[164:167]\n\t"
    "v_and_b32 v232, 24, v232\n\t"
    "v_or_b32 v231, v231, v232\n\t"
    "s_mov_b64 s[40:41], %14\n\t"
    "s_add_u32 s40, s40, 0x1600000\n\t"
    "s_addc_u32 s41, s41, 0\n\t"
    "s_and_b32 s41, s41, 0xffff\n\t"
    "s_mov_b32 s42, 0x780000\n\t"
    "s_mov_b32 s43, 0x20000\n\t"
    "s_mov_b64 s[44:45], %16\n\t"
    "s_and_b32 s45, s45, 0xffff\n\t"
    "s_mov_b32 s46, 0x28000\n\t"
    "s_mov_b32 s47, 0x20000\n\t"
    "v_exp_f32 v112, v112\n\t"
    "v_exp_f32 v113, v113\n\t"
    "v_exp_f32 v114, v114\n\t"
    "v_exp_f32 v115, v115\n\t"
    "v_exp_f32 v152, v152\n\t"
    "v_exp_f32 v153, v153\n\t"
    "v_exp_f32 v154, v154\n\t"
    "v_exp_f32 v155, v155\n\t"
    "v_cvt_pk_bf16_f32 v180, v112, v113\n\t"
    "v_cvt_pk_bf16_f32 v181, v114, v115\n\t"
    "v_cvt_pk_bf16_f32 v182, v152, v153\n\t"
    "v_cvt_pk_bf16_f32 v183, v154, v155\n\t"
    "v_exp_f32 v120, v120\n\t"
    "s_nop 0\n\t"
    "v_mfma_f32_16x16x32_bf16 a[8:11], a[180:183], v[180:183], a[8:11]\n\t"
    "v_exp_f32 v121, v121\n\t"
    "v_mfma_f32_16x16x32_bf16 a[28:31], a[184:187], v[180:183], a[28:31]\n\t"
    "v_exp_f32 v122, v122\n\t"
    "v_mfma_f32_16x16x32_bf16 a[48:51], a[188:191], v[180:183], a[48:51]\n\t"
    "v_exp_f32 v123, v123\n\t"
    "v_mfma_f32_16x16x32_bf16 a[68:71], a[192:195], v[180:183], a[68:71]\n\t"
    "v_exp_f32 v160, v160\n\t"
    "v_mfma_f32_16x16x32_bf16 a[88:91], a[196:199], v[180:183], a[88:91]\n\t"
    "v_exp_f32 v161, v161\n\t"
    "v_mfma_f32_16x16x32_bf16 a[108:111], a[232:235], v[180:183], a[108:111]\n\t"
    "v_exp_f32 v162, v162\n\t"
    "v_mfma_f32_16x16x32_bf16 a[128:131], a[236:239], v[180:183], a[128:131]\n\t"
    "v_exp_f32 v163, v163\n\t"
    "v_mfma_f32_16x16x32_bf16 a[148:151], a[240:243], v[180:183], a[148:151]\n\t"
    "v_cvt_pk_bf16_f32 v184, v120, v121\n\t"
    "v_cvt_pk_bf16_f32 v185, v122, v123\n\t"
    "v_mfma_f32_16x16x32_bf16 a[168:171], a[244:247], v[180:183], a[168:171]\n\t"
    "v_cvt_pk_bf16_f32 v186, v160, v161\n\t"
    "v_cvt_pk_bf16_f32 v187, v162, v163\n\t"
    "buffer_load_dwordx4 v220, s[36:39], s69 offen lds\n\t"
    "s_nop 0\n\t"
    "v_mfma_f32_16x16x32_bf16 a[12:15], a[180:183], v[184:187], a[12:15]\n\t"
    "v_exp_f32 v128, v128\n\t"
    "v_mfma_f32_16x16x32_bf16 a[32:35], a[184:187], v[184:187], a[32:35]\n\t"
    "v_exp_f32 v129, v129\n\t"
    "v_mfma_f32_16x16x32_bf16 a[52:55], a[188:191], v[184:187], a[52:55]\n\t"
    "v_exp_f32 v130, v130\n\t"
    "v_mfma_f32_16x16x32_bf16 a[72:75], a[192:195], v[184:187], a[72:75]\n\t"
    "v_exp_f32 v131, v131\n\t"
    "v_mfma_f32_16x16x32_bf16 a[92:95], a[196:199], v[184:187], a[92:95]\n\t"
    "v_exp_f32 v168, v168\n\t"
    "v_mfma_f32_16x16x32_bf16 a[112:115], a[232:235], v[184:187], a[112:115]\n\t"
    "v_exp_f32 v169, v169\n\t"
    "v_mfma_f32_16x16x32_bf16 a[132:135], a[236:239], v[184:187], a[132:135]\n\t"
    "v_exp_f32 v170, v170\n\t"
    "v_mfma_f32_16x16x32_bf16 a[152:155], a[240:243], v[184:187], a[152:155]\n\t"
    "v_exp_f32 v171, v171\n\t"
    "v_mfma_f32_16x16x32_bf16 a[172:175], a[244:247], v[184:187], a[172:175]\n\t"
    "v_cvt_pk_bf16_f32 v188, v128, v129\n\t"
    "v_cvt_pk_bf16_f32 v189, v130, v131\n\t"
    "v_cvt_pk_bf16_f32 v190, v168, v169\n\t"
    "v_cvt_pk_bf16_f32 v191, v170, v171\n\t"
    "v_accvgpr_read_b32 v240, a160\n\t"
    "v_rcp_f32 v240, v240\n\t"
    "v_mfma_f32_16x16x32_bf16 a[16:19], a[180:183], v[188:191], a[16:19]\n\t"
    "v_accvgpr_read_b32 v12, a0\n\t"
    "v_accvgpr_read_b32 v13, a1\n\t"
    "v_mfma_f32_16x16x32_bf16 a[36:39], a[184:187], v[188:191], a[36:39]\n\t"
    "v_accvgpr_read_b32 v14, a2\n\t"
    "v_accvgpr_read_b32 v15, a3\n\t"
    "v_mfma_f32_16x16x32_bf16 a[56:59], a[188:191], v[188:191], a[56:59]\n\t"
    "v_accvgpr_read_b32 v16, a20\n\t"
    "v_accvgpr_read_b32 v17, a21\n\t"
    "v_mfma_f32_16x16x32_bf16 a[76:79], a[192:195], v[188:191], a[76:79]\n\t"
    "v_accvgpr_read_b32 v18, a22\n\t"
    "v_accvgpr_read_b32 v19, a23\n\t"
    "v_mfma_f32_16x16x32_bf16 a[96:99], a[196:199], v[188:191], a[96:99]\n\t"
    "v_mul_f32 v12, v12, v240\n\t"
    "v_mul_f32 v13, v13, v240\n\t"
    "v_mfma_f32_16x16x32_bf16 a[116:119], a[232:235], v[188:191], a[116:119]\n\t"
    "v_mul_f32 v14, v14, v240\n\t"
    "v_mul_f32 v15, v15, v240\n\t"
    "v_mfma_f32_16x16x32_bf16 a[136:139], a[236:239], v[188:191], a[136:139]\n\t"
    "v_mul_f32 v16, v16, v240\n\t"
    "v_mul_f32 v17, v17, v240\n\t"
    "v_mfma_f32_16x16x32_bf16 a[156:159], a[240:243], v[188:191], a[156:159]\n\t"
    "v_mul_f32 v18, v18, v240\n\t"
    "v_mul_f32 v19, v19, v240\n\t"
    "v_mfma_f32_16x16x32_bf16 a[176:179], a[244:247], v[188:191], a[176:179]\n\t"
    "v_accvgpr_read_b32 v20, a40\n\t"
    "v_accvgpr_read_b32 v21, a41\n\t"
    "v_accvgpr_read_b32 v22, a42\n\t"
    "v_accvgpr_read_b32 v23, a43\n\t"
    "v_accvgpr_read_b32 v24, a60\n\t"
    "v_accvgpr_read_b32 v25, a61\n\t"
    "v_accvgpr_read_b32 v26, a62\n\t"
    "v_accvgpr_read_b32 v27, a63\n\t"
    "v_cvt_pk_bf16_f32 v92, v12, v13\n\t"
    "v_cvt_pk_bf16_f32 v93, v14, v15\n\t"
    "v_cvt_pk_bf16_f32 v94, v16, v17\n\t"
    "v_cvt_pk_bf16_f32 v95, v18, v19\n\t"
    "s_add_u32 s80, s94, 0\n\t"
    "v_mul_f32 v20, v20, v240\n\t"
    "v_mul_f32 v21, v21, v240\n\t"
    "v_mul_f32 v22, v22, v240\n\t"
    "v_mul_f32 v23, v23, v240\n\t"
    "v_mul_f32 v24, v24, v240\n\t"
    "v_mul_f32 v25, v25, v240\n\t"
    "v_mul_f32 v26, v26, v240\n\t"
    "v_mul_f32 v27, v27, v240\n\t"
    "v_accvgpr_read_b32 v28, a80\n\t"
    "v_accvgpr_read_b32 v29, a81\n\t"
    "v_accvgpr_read_b32 v30, a82\n\t"
    "v_accvgpr_read_b32 v31, a83\n\t"
    "v_accvgpr_read_b32 v32, a100\n\t"
    "v_accvgpr_read_b32 v33, a101\n\t"
    "v_accvgpr_read_b32 v34, a102\n\t"
    "v_accvgpr_read_b32 v35, a103\n\t"
    "buffer_store_dwordx4 v[92:95], v239, s[52:55], s80 offen sc1\n\t"
    "v_cvt_pk_bf16_f32 v112, v20, v21\n\t"
    "v_cvt_pk_bf16_f32 v113, v22, v23\n\t"
    "v_cvt_pk_bf16_f32 v114, v24, v25\n\t"
    "v_cvt_pk_bf16_f32 v115, v26, v27\n\t"
    "v_accvgpr_read_b32 v241, a164\n\t"
    "v_mul_f32 v28, v28, v240\n\t"
    "v_mul_f32 v29, v29, v240\n\t"
    "v_mul_f32 v30, v30, v240\n\t"
    "v_mul_f32 v31, v31, v240\n\t"
    "v_mul_f32 v32, v32, v240\n\t"
    "v_mul_f32 v33, v33, v240\n\t"
    "v_mul_f32 v34, v34, v240\n\t"
    "v_mul_f32 v35, v35, v240\n\t"
    "v_accvgpr_read_b32 v36, a120\n\t"
    "v_accvgpr_read_b32 v37, a121\n\t"
    "v_accvgpr_read_b32 v38, a122\n\t"
    "v_accvgpr_read_b32 v39, a123\n\t"
    "v_accvgpr_read_b32 v40, a140\n\t"
    "v_accvgpr_read_b32 v41, a141\n\t"
    "v_accvgpr_read_b32 v42, a142\n\t"
    "v_accvgpr_read_b32 v43, a143\n\t"
    "v_rcp_f32 v241, v241\n\t"
    "buffer_store_dwordx4 v[112:115], v239, s[52:55], s80 offen offset:1024 sc1\n\t"
    "v_cvt_pk_bf16_f32 v132, v28, v29\n\t"
    "v_cvt_pk_bf16_f32 v133, v30, v31\n\t"
    "v_cvt_pk_bf16_f32 v134, v32, v33\n\t"
    "v_cvt_pk_bf16_f32 v135, v34, v35\n\t"
    "v_mul_f32 v36, v36, v240\n\t"
    "v_mul_f32 v37, v37, v240\n\t"
    "v_mul_f32 v38, v38, v240\n\t"
    "v_mul_f32 v39, v39, v240\n\t"
    "v_mul_f32 v40, v40, v240\n\t"
    "v_mul_f32 v41, v41, v240\n\t"
    "v_mul_f32 v42, v42, v240\n\t"
    "v_mul_f32 v43, v43, v240\n\t"
    "v_accvgpr_read_b32 v242, a168\n\t"
    "v_accvgpr_read_b32 v243, a172\n\t"
    "v_accvgpr_read_b32 v244, a176\n\t"
    "v_accvgpr_read_b32 v44, a4\n\t"
    "v_accvgpr_read_b32 v45, a5\n\t"
    "v_accvgpr_read_b32 v46, a6\n\t"
    "v_accvgpr_read_b32 v47, a7\n\t"
    "v_accvgpr_read_b32 v48, a24\n\t"
    "v_accvgpr_read_b32 v49, a25\n\t"
    "v_accvgpr_read_b32 v50, a26\n\t"
    "v_accvgpr_read_b32 v51, a27\n\t"
    "buffer_store_dwordx4 v[132:135], v239, s[52:55], s80 offen offset:2048 sc1\n\t"
    "v_cvt_pk_bf16_f32 v152, v36, v37\n\t"
    "v_cvt_pk_bf16_f32 v153, v38, v39\n\t"
    "v_cvt_pk_bf16_f32 v154, v40, v41\n\t"
    "v_cvt_pk_bf16_f32 v155, v42, v43\n\t"
    "v_rcp_f32 v242, v242\n\t"
    "v_rcp_f32 v243, v243\n\t"
    "v_rcp_f32 v244, v244\n\t"
    "v_mul_f32 v44, v44, v241\n\t"
    "v_mul_f32 v45, v45, v241\n\t"
    "v_mul_f32 v46, v46, v241\n\t"
    "v_mul_f32 v47, v47, v241\n\t"
    "v_mul_f32 v48, v48, v241\n\t"
    "v_mul_f32 v49, v49, v241\n\t"
    "v_mul_f32 v50, v50, v241\n\t"
    "v_mul_f32 v51, v51, v241\n\t"
    "v_accvgpr_read_b32 v52, a44\n\t"
    "v_accvgpr_read_b32 v53, a45\n\t"
    "v_accvgpr_read_b32 v54, a46\n\t"
    "v_accvgpr_read_b32 v55, a47\n\t"
    "v_accvgpr_read_b32 v56, a64\n\t"
    "v_accvgpr_read_b32 v57, a65\n\t"
    "v_accvgpr_read_b32 v58, a66\n\t"
    "v_accvgpr_read_b32 v59, a67\n\t"
    "buffer_store_dwordx4 v[152:155], v239, s[52:55], s80 offen offset:3072 sc1\n\t"
    "v_cvt_pk_bf16_f32 v96, v44, v45\n\t"
    "v_cvt_pk_bf16_f32 v97, v46, v47\n\t"
    "v_cvt_pk_bf16_f32 v98, v48, v49\n\t"
    "v_cvt_pk_bf16_f32 v99, v50, v51\n\t"
    "s_add_u32 s81, s94, 0x1000\n\t"
    "v_mul_f32 v52, v52, v241\n\t"
    "v_mul_f32 v53, v53, v241\n\t"
    "v_mul_f32 v54, v54, v241\n\t"
    "v_mul_f32 v55, v55, v241\n\t"
    "v_mul_f32 v56, v56, v241\n\t"
    "v_mul_f32 v57, v57, v241\n\t"
    "v_mul_f32 v58, v58, v241\n\t"
    "v_mul_f32 v59, v59, v241\n\t"
    "buffer_store_dwordx4 v[96:99], v239, s[52:55], s81 offen sc1\n\t"
    "v_cvt_pk_bf16_f32 v116, v52, v53\n\t"
    "v_cvt_pk_bf16_f32 v117, v54, v55\n\t"
    "v_cvt_pk_bf16_f32 v118, v56, v57\n\t"
    "v_cvt_pk_bf16_f32 v119, v58, v59\n\t"
    "v_accvgpr_read_b32 v60, a84\n\t"
    "v_accvgpr_read_b32 v61, a85\n\t"
    "v_accvgpr_read_b32 v62, a86\n\t"
    "v_accvgpr_read_b32 v63, a87\n\t"
    "v_accvgpr_read_b32 v64, a104\n\t"
    "v_accvgpr_read_b32 v65, a105\n\t"
    "v_accvgpr_read_b32 v66, a106\n\t"
    "v_accvgpr_read_b32 v67, a107\n\t"
    "v_accvgpr_read_b32 v68, a124\n\t"
    "v_accvgpr_read_b32 v69, a125\n\t"
    "v_accvgpr_read_b32 v70, a126\n\t"
    "v_accvgpr_read_b32 v71, a127\n\t"
    "v_accvgpr_read_b32 v72, a144\n\t"
    "v_accvgpr_read_b32 v73, a145\n\t"
    "v_accvgpr_read_b32 v74, a146\n\t"
    "v_accvgpr_read_b32 v75, a147\n\t"
    "v_mul_f32 v60, v60, v241\n\t"
    "v_mul_f32 v61, v61, v241\n\t"
    "v_mul_f32 v62, v62, v241\n\t"
    "v_mul_f32 v63, v63, v241\n\t"
    "v_mul_f32 v64, v64, v241\n\t"
    "v_mul_f32 v65, v65, v241\n\t"
    "v_mul_f32 v66, v66, v241\n\t"
    "v_mul_f32 v67, v67, v241\n\t"
    "buffer_store_dwordx4 v[116:119], v239, s[52:55], s81 offen offset:1024 sc1\n\t"
    "v_accvgpr_read_b32 v76, a8\n\t"
    "v_accvgpr_read_b32 v77, a9\n\t"
    "v_accvgpr_read_b32 v78, a10\n\t"
    "v_accvgpr_read_b32 v79, a11\n\t"
    "v_accvgpr_read_b32 v80, a28\n\t"
    "v_accvgpr_read_b32 v81, a29\n\t"
    "v_accvgpr_read_b32 v82, a30\n\t"
    "v_accvgpr_read_b32 v83, a31\n\t"
    "v_mul_f32 v68, v68, v241\n\t"
    "v_mul_f32 v69, v69, v241\n\t"
    "v_mul_f32 v70, v70, v241\n\t"
    "v_mul_f32 v71, v71, v241\n\t"
    "v_mul_f32 v72, v72, v241\n\t"
    "v_mul_f32 v73, v73, v241\n\t"
    "v_mul_f32 v74, v74, v241\n\t"
    "v_mul_f32 v75, v75, v241\n\t"
    "v_cvt_pk_bf16_f32 v136, v60, v61\n\t"
    "v_cvt_pk_bf16_f32 v137, v62, v63\n\t"
    "v_cvt_pk_bf16_f32 v138, v64, v65\n\t"
    "v_cvt_pk_bf16_f32 v139, v66, v67\n\t"
    "v_accvgpr_read_b32 v84, a48\n\t"
    "v_accvgpr_read_b32 v85, a49\n\t"
    "v_accvgpr_read_b32 v86, a50\n\t"
    "v_accvgpr_read_b32 v87, a51\n\t"
    "v_accvgpr_read_b32 v88, a68\n\t"
    "v_accvgpr_read_b32 v89, a69\n\t"
    "v_accvgpr_read_b32 v90, a70\n\t"
    "v_accvgpr_read_b32 v91, a71\n\t"
    "v_mul_f32 v76, v76, v242\n\t"
    "v_mul_f32 v77, v77, v242\n\t"
    "v_mul_f32 v78, v78, v242\n\t"
    "v_mul_f32 v79, v79, v242\n\t"
    "v_mul_f32 v80, v80, v242\n\t"
    "v_mul_f32 v81, v81, v242\n\t"
    "v_mul_f32 v82, v82, v242\n\t"
    "v_mul_f32 v83, v83, v242\n\t"
    "v_cvt_pk_bf16_f32 v156, v68, v69\n\t"
    "v_cvt_pk_bf16_f32 v157, v70, v71\n\t"
    "v_cvt_pk_bf16_f32 v158, v72, v73\n\t"
    "v_cvt_pk_bf16_f32 v159, v74, v75\n\t"
    "v_accvgpr_read_b32 v12, a88\n\t"
    "v_accvgpr_read_b32 v13, a89\n\t"
    "v_accvgpr_read_b32 v14, a90\n\t"
    "v_accvgpr_read_b32 v15, a91\n\t"
    "v_accvgpr_read_b32 v16, a108\n\t"
    "v_accvgpr_read_b32 v17, a109\n\t"
    "v_accvgpr_read_b32 v18, a110\n\t"
    "v_accvgpr_read_b32 v19, a111\n\t"
    "v_mul_f32 v84, v84, v242\n\t"
    "v_mul_f32 v85, v85, v242\n\t"
    "v_mul_f32 v86, v86, v242\n\t"
    "v_mul_f32 v87, v87, v242\n\t"
    "v_mul_f32 v88, v88, v242\n\t"
    "v_mul_f32 v89, v89, v242\n\t"
    "v_mul_f32 v90, v90, v242\n\t"
    "v_mul_f32 v91, v91, v242\n\t"
    "v_cvt_pk_bf16_f32 v100, v76, v77\n\t"
    "v_cvt_pk_bf16_f32 v101, v78, v79\n\t"
    "v_cvt_pk_bf16_f32 v102, v80, v81\n\t"
    "v_cvt_pk_bf16_f32 v103, v82, v83\n\t"
    "s_add_u32 s82, s94, 0x2000\n\t"
    "v_accvgpr_read_b32 v20, a128\n\t"
    "v_accvgpr_read_b32 v21, a129\n\t"
    "v_accvgpr_read_b32 v22, a130\n\t"
    "v_accvgpr_read_b32 v23, a131\n\t"
    "v_accvgpr_read_b32 v24, a148\n\t"
    "v_accvgpr_read_b32 v25, a149\n\t"
    "v_accvgpr_read_b32 v26, a150\n\t"
    "v_accvgpr_read_b32 v27, a151\n\t"
    "v_mul_f32 v12, v12, v242\n\t"
    "v_mul_f32 v13, v13, v242\n\t"
    "v_mul_f32 v14, v14, v242\n\t"
    "v_mul_f32 v15, v15, v242\n\t"
    "v_mul_f32 v16, v16, v242\n\t"
    "v_mul_f32 v17, v17, v242\n\t"
    "v_mul_f32 v18, v18, v242\n\t"
    "v_mul_f32 v19, v19, v242\n\t"
    "v_cvt_pk_bf16_f32 v120, v84, v85\n\t"
    "v_cvt_pk_bf16_f32 v121, v86, v87\n\t"
    "v_cvt_pk_bf16_f32 v122, v88, v89\n\t"
    "v_cvt_pk_bf16_f32 v123, v90, v91\n\t"
    "v_accvgpr_read_b32 v28, a12\n\t"
    "v_accvgpr_read_b32 v29, a13\n\t"
    "v_accvgpr_read_b32 v30, a14\n\t"
    "v_accvgpr_read_b32 v31, a15\n\t"
    "v_accvgpr_read_b32 v32, a32\n\t"
    "v_accvgpr_read_b32 v33, a33\n\t"
    "v_accvgpr_read_b32 v34, a34\n\t"
    "v_accvgpr_read_b32 v35, a35\n\t"
    "v_mul_f32 v20, v20, v242\n\t"
    "v_mul_f32 v21, v21, v242\n\t"
    "v_mul_f32 v22, v22, v242\n\t"
    "v_mul_f32 v23, v23, v242\n\t"
    "v_mul_f32 v24, v24, v242\n\t"
    "v_mul_f32 v25, v25, v242\n\t"
    "v_mul_f32 v26, v26, v242\n\t"
    "v_mul_f32 v27, v27, v242\n\t"
    "v_cvt_pk_bf16_f32 v140, v12, v13\n\t"
    "v_cvt_pk_bf16_f32 v141, v14, v15\n\t"
    "v_cvt_pk_bf16_f32 v142, v16, v17\n\t"
    "v_cvt_pk_bf16_f32 v143, v18, v19\n\t"
    "v_accvgpr_read_b32 v36, a52\n\t"
    "v_accvgpr_read_b32 v37, a53\n\t"
    "v_accvgpr_read_b32 v38, a54\n\t"
    "v_accvgpr_read_b32 v39, a55\n\t"
    "v_accvgpr_read_b32 v40, a72\n\t"
    "v_accvgpr_read_b32 v41, a73\n\t"
    "v_accvgpr_read_b32 v42, a74\n\t"
    "v_accvgpr_read_b32 v43, a75\n\t"
    "v_mul_f32 v28, v28, v243\n\t"
    "v_mul_f32 v29, v29, v243\n\t"
    "v_mul_f32 v30, v30, v243\n\t"
    "v_mul_f32 v31, v31, v243\n\t"
    "v_mul_f32 v32, v32, v243\n\t"
    "v_mul_f32 v33, v33, v243\n\t"
    "v_mul_f32 v34, v34, v243\n\t"
    "v_mul_f32 v35, v35, v243\n\t"
    "v_cvt_pk_bf16_f32 v160, v20, v21\n\t"
    "v_cvt_pk_bf16_f32 v161, v22, v23\n\t"
    "v_cvt_pk_bf16_f32 v162, v24, v25\n\t"
    "v_cvt_pk_bf16_f32 v163, v26, v27\n\t"
    "v_accvgpr_read_b32 v44, a92\n\t"
    "v_accvgpr_read_b32 v45, a93\n\t"
    "v_accvgpr_read_b32 v46, a94\n\t"
    "v_accvgpr_read_b32 v47, a95\n\t"
    "v_accvgpr_read_b32 v48, a112\n\t"
    "v_accvgpr_read_b32 v49, a113\n\t"
    "v_accvgpr_read_b32 v50, a114\n\t"
    "v_accvgpr_read_b32 v51, a115\n\t"
    "v_mul_f32 v36, v36, v243\n\t"
    "v_mul_f32 v37, v37, v243\n\t"
    "v_mul_f32 v38, v38, v243\n\t"
    "v_mul_f32 v39, v39, v243\n\t"
    "v_mul_f32 v40, v40, v243\n\t"
    "v_mul_f32 v41, v41, v243\n\t"
    "v_mul_f32 v42, v42, v243\n\t"
    "v_mul_f32 v43, v43, v243\n\t"
    "v_cvt_pk_bf16_f32 v104, v28, v29\n\t"
    "v_cvt_pk_bf16_f32 v105, v30, v31\n\t"
    "v_cvt_pk_bf16_f32 v106, v32, v33\n\t"
    "v_cvt_pk_bf16_f32 v107, v34, v35\n\t"
    "s_add_u32 s83, s94, 0x3000\n\t"
    "v_accvgpr_read_b32 v52, a132\n\t"
    "v_accvgpr_read_b32 v53, a133\n\t"
    "v_accvgpr_read_b32 v54, a134\n\t"
    "v_accvgpr_read_b32 v55, a135\n\t"
    "v_accvgpr_read_b32 v56, a152\n\t"
    "v_accvgpr_read_b32 v57, a153\n\t"
    "v_accvgpr_read_b32 v58, a154\n\t"
    "v_accvgpr_read_b32 v59, a155\n\t"
    "v_mul_f32 v44, v44, v243\n\t"
    "v_mul_f32 v45, v45, v243\n\t"
    "v_mul_f32 v46, v46, v243\n\t"
    "v_mul_f32 v47, v47, v243\n\t"
    "v_mul_f32 v48, v48, v243\n\t"
    "v_mul_f32 v49, v49, v243\n\t"
    "v_mul_f32 v50, v50, v243\n\t"
    "v_mul_f32 v51, v51, v243\n\t"
    "v_cvt_pk_bf16_f32 v124, v36, v37\n\t"
    "v_cvt_pk_bf16_f32 v125, v38, v39\n\t"
    "v_cvt_pk_bf16_f32 v126, v40, v41\n\t"
    "v_cvt_pk_bf16_f32 v127, v42, v43\n\t"
    "v_accvgpr_read_b32 v60, a16\n\t"
    "v_accvgpr_read_b32 v61, a17\n\t"
    "v_accvgpr_read_b32 v62, a18\n\t"
    "v_accvgpr_read_b32 v63, a19\n\t"
    "v_accvgpr_read_b32 v64, a36\n\t"
    "v_accvgpr_read_b32 v65, a37\n\t"
    "v_accvgpr_read_b32 v66, a38\n\t"
    "v_accvgpr_read_b32 v67, a39\n\t"
    "v_mul_f32 v52, v52, v243\n\t"
    "v_mul_f32 v53, v53, v243\n\t"
    "v_mul_f32 v54, v54, v243\n\t"
    "v_mul_f32 v55, v55, v243\n\t"
    "v_mul_f32 v56, v56, v243\n\t"
    "v_mul_f32 v57, v57, v243\n\t"
    "v_mul_f32 v58, v58, v243\n\t"
    "v_mul_f32 v59, v59, v243\n\t"
    "v_cvt_pk_bf16_f32 v144, v44, v45\n\t"
    "v_cvt_pk_bf16_f32 v145, v46, v47\n\t"
    "v_cvt_pk_bf16_f32 v146, v48, v49\n\t"
    "v_cvt_pk_bf16_f32 v147, v50, v51\n\t"
    "v_accvgpr_read_b32 v68, a56\n\t"
    "v_accvgpr_read_b32 v69, a57\n\t"
    "v_accvgpr_read_b32 v70, a58\n\t"
    "v_accvgpr_read_b32 v71, a59\n\t"
    "v_accvgpr_read_b32 v72, a76\n\t"
    "v_accvgpr_read_b32 v73, a77\n\t"
    "v_accvgpr_read_b32 v74, a78\n\t"
    "v_accvgpr_read_b32 v75, a79\n\t"
    "v_mul_f32 v60, v60, v244\n\t"
    "v_mul_f32 v61, v61, v244\n\t"
    "v_mul_f32 v62, v62, v244\n\t"
    "v_mul_f32 v63, v63, v244\n\t"
    "v_mul_f32 v64, v64, v244\n\t"
    "v_mul_f32 v65, v65, v244\n\t"
    "v_mul_f32 v66, v66, v244\n\t"
    "v_mul_f32 v67, v67, v244\n\t"
    "v_cvt_pk_bf16_f32 v164, v52, v53\n\t"
    "v_cvt_pk_bf16_f32 v165, v54, v55\n\t"
    "v_cvt_pk_bf16_f32 v166, v56, v57\n\t"
    "v_cvt_pk_bf16_f32 v167, v58, v59\n\t"
    "v_accvgpr_read_b32 v76, a96\n\t"
    "v_accvgpr_read_b32 v77, a97\n\t"
    "v_accvgpr_read_b32 v78, a98\n\t"
    "v_accvgpr_read_b32 v79, a99\n\t"
    "v_accvgpr_read_b32 v80, a116\n\t"
    "v_accvgpr_read_b32 v81, a117\n\t"
    "v_accvgpr_read_b32 v82, a118\n\t"
    "v_accvgpr_read_b32 v83, a119\n\t"
    "v_mul_f32 v68, v68, v244\n\t"
    "v_mul_f32 v69, v69, v244\n\t"
    "v_mul_f32 v70, v70, v244\n\t"
    "v_mul_f32 v71, v71, v244\n\t"
    "v_mul_f32 v72, v72, v244\n\t"
    "v_mul_f32 v73, v73, v244\n\t"
    "v_mul_f32 v74, v74, v244\n\t"
    "v_mul_f32 v75, v75, v244\n\t"
    "v_cvt_pk_bf16_f32 v108, v60, v61\n\t"
    "v_cvt_pk_bf16_f32 v109, v62, v63\n\t"
    "v_cvt_pk_bf16_f32 v110, v64, v65\n\t"
    "v_cvt_pk_bf16_f32 v111, v66, v67\n\t"
    "s_add_u32 s84, s94, 0x4000\n\t"
    "v_accvgpr_read_b32 v84, a136\n\t"
    "v_accvgpr_read_b32 v85, a137\n\t"
    "v_accvgpr_read_b32 v86, a138\n\t"
    "v_accvgpr_read_b32 v87, a139\n\t"
    "v_accvgpr_read_b32 v88, a156\n\t"
    "v_accvgpr_read_b32 v89, a157\n\t"
    "v_accvgpr_read_b32 v90, a158\n\t"
    "v_accvgpr_read_b32 v91, a159\n\t"
    "v_mul_f32 v76, v76, v244\n\t"
    "v_mul_f32 v77, v77, v244\n\t"
    "v_mul_f32 v78, v78, v244\n\t"
    "v_mul_f32 v79, v79, v244\n\t"
    "v_mul_f32 v80, v80, v244\n\t"
    "v_mul_f32 v81, v81, v244\n\t"
    "v_mul_f32 v82, v82, v244\n\t"
    "v_mul_f32 v83, v83, v244\n\t"
    "v_cvt_pk_bf16_f32 v128, v68, v69\n\t"
    "v_cvt_pk_bf16_f32 v129, v70, v71\n\t"
    "v_cvt_pk_bf16_f32 v130, v72, v73\n\t"
    "v_cvt_pk_bf16_f32 v131, v74, v75\n\t"
    "v_mul_f32 v84, v84, v244\n\t"
    "v_mul_f32 v85, v85, v244\n\t"
    "v_mul_f32 v86, v86, v244\n\t"
    "v_mul_f32 v87, v87, v244\n\t"
    "v_mul_f32 v88, v88, v244\n\t"
    "v_mul_f32 v89, v89, v244\n\t"
    "v_mul_f32 v90, v90, v244\n\t"
    "v_mul_f32 v91, v91, v244\n\t"
    "v_cvt_pk_bf16_f32 v148, v76, v77\n\t"
    "v_cvt_pk_bf16_f32 v149, v78, v79\n\t"
    "v_cvt_pk_bf16_f32 v150, v80, v81\n\t"
    "v_cvt_pk_bf16_f32 v151, v82, v83\n\t"
    "v_cvt_pk_bf16_f32 v168, v84, v85\n\t"
    "v_cvt_pk_bf16_f32 v169, v86, v87\n\t"
    "v_cvt_pk_bf16_f32 v170, v88, v89\n\t"
    "v_cvt_pk_bf16_f32 v171, v90, v91\n\t"
    "s_waitcnt vmcnt(14) lgkmcnt(0)\n\t"
    "s_barrier\n\t"
    "v_add_u32 v228, %25, v239\n\t"
    "v_lshrrev_b32 v233, 1, v239\n\t"
    "ds_read_b64_tr_b16 v[172:173], v223 offset:32768\n\t"
    "ds_read_b64_tr_b16 v[174:175], v223 offset:36864\n\t"
    "ds_read_b64_tr_b16 v[176:177], v223 offset:33280\n\t"
    "ds_read_b64_tr_b16 v[178:179], v223 offset:37376\n\t"
    "ds_read_b64_tr_b16 v[180:181], v223 offset:33792\n\t"
    "ds_read_b64_tr_b16 v[182:183], v223 offset:37888\n\t"
    "ds_read_b64_tr_b16 v[184:185], v223 offset:34304\n\t"
    "ds_read_b64_tr_b16 v[186:187], v223 offset:38400\n\t"
    "ds_read_b64_tr_b16 v[188:189], v224 offset:32768\n\t"
    "ds_read_b64_tr_b16 v[190:191], v224 offset:36864\n\t"
    "ds_read_b64_tr_b16 v[192:193], v224 offset:33280\n\t"
    "ds_read_b64_tr_b16 v[194:195], v224 offset:37376\n\t"
    "ds_read_b64_tr_b16 v[196:197], v224 offset:33792\n\t"
    "ds_read_b64_tr_b16 v[198:199], v224 offset:37888\n\t"
    "ds_read_b64_tr_b16 v[200:201], v224 offset:34304\n\t"
    "s_waitcnt lgkmcnt(14)\n\t"
    "ds_read_b64_tr_b16 v[202:203], v224 offset:38400\n\t"
    "s_waitcnt lgkmcnt(14)\n\t"
    "v_mfma_f32_16x16x32_bf16 v[12:15], v[172:175], v[92:95], 0\n\t"
    "s_add_u32 s64, s62, 0\n\t"
    "s_add_u32 m0, %17, 0x10000\n\t"
    "v_mfma_f32_16x16x32_bf16 v[16:19], v[172:175], v[96:99], 0\n\t"
    "buffer_load_dwordx4 %9, s[36:39], s64 offen lds\n\t"
    "v_mfma_f32_16x16x32_bf16 v[20:23], v[172:175], v[100:103], 0\n\t"
    "s_add_u32 m0, m0, 0x400\n\t"
    "ds_read_b64_tr_b16 v[204:205], v224 offset:50176\n\t"
    "v_mfma_f32_16x16x32_bf16 v[24:27], v[172:175], v[104:107], 0\n\t"
    "buffer_load_dwordx4 v230, s[36:39], s64 offen lds\n\t"
    "v_mfma_f32_16x16x32_bf16 v[28:31], v[172:175], v[108:111], 0\n\t"
    "s_add_u32 m0, m0, 0x400\n\t"
    "s_waitcnt lgkmcnt(14)\n\t"
    "ds_read_b64_tr_b16 v[172:173], v223 offset:49152\n\t"
    "s_waitcnt lgkmcnt(14)\n\t"
    "v_mfma_f32_16x16x32_bf16 v[32:35], v[176:179], v[92:95], 0\n\t"
    "buffer_load_dwordx4 v219, s[36:39], s64 offen lds\n\t"
    "v_mfma_f32_16x16x32_bf16 v[36:39], v[176:179], v[96:99], 0\n\t"
    "s_add_u32 m0, m0, 0x400\n\t"
    "s_add_u32 s69, s62, 0x8000\n\t"
    "v_mfma_f32_16x16x32_bf16 v[40:43], v[176:179], v[100:103], 0\n\t"
    "buffer_load_dwordx4 v220, s[36:39], s64 offen lds\n\t"
    "v_mfma_f32_16x16x32_bf16 v[44:47], v[176:179], v[104:107], 0\n\t"
    "s_add_u32 m0, %17, 0x14000\n\t"
    "ds_read_b64_tr_b16 v[174:175], v223 offset:53248\n\t"
    "v_mfma_f32_16x16x32_bf16 v[48:51], v[176:179], v[108:111], 0\n\t"
    "buffer_load_dwordx4 %9, s[36:39], s69 offen lds\n\t"
    "s_waitcnt lgkmcnt(13)\n\t"
    "v_mfma_f32_16x16x32_bf16 v[52:55], v[180:183], v[92:95], 0\n\t"
    "s_add_u32 m0, m0, 0x400\n\t"
    "ds_read_b64_tr_b16 v[176:177], v223 offset:49664\n\t"
    "v_mfma_f32_16x16x32_bf16 v[56:59], v[180:183], v[96:99], 0\n\t"
    "buffer_load_dwordx4 v230, s[36:39], s69 offen lds\n\t"
    "v_mfma_f32_16x16x32_bf16 v[60:63], v[180:183], v[100:103], 0\n\t"
    "s_add_u32 m0, m0, 0x400\n\t"
    "ds_read_b64_tr_b16 v[178:179], v223 offset:53760\n\t"
    "v_mfma_f32_16x16x32_bf16 v[64:67], v[180:183], v[104:107], 0\n\t"
    "buffer_load_dwordx4 v219, s[36:39], s69 offen lds\n\t"
    "v_mfma_f32_16x16x32_bf16 v[68:71], v[180:183], v[108:111], 0\n\t"
    "s_add_u32 m0, m0, 0x400\n\t"
    "s_waitcnt lgkmcnt(13)\n\t"
    "ds_read_b64_tr_b16 v[180:181], v223 offset:50176\n\t"
    "v_mfma_f32_16x16x32_bf16 v[72:75], v[184:187], v[92:95], 0\n\t"
    "buffer_load_dwordx4 v220, s[36:39], s69 offen lds\n\t"
    "v_mfma_f32_16x16x32_bf16 v[76:79], v[184:187], v[96:99], 0\n\t"
    "buffer_store_dwordx4 v[136:139], v239, s[52:55], s81 offen offset:2048 sc1\n\t"
    "v_mfma_f32_16x16x32_bf16 v[80:83], v[184:187], v[100:103], 0\n\t"
    "buffer_store_dwordx4 v[156:159], v239, s[52:55], s81 offen offset:3072 sc1\n\t"
    "v_mfma_f32_16x16x32_bf16 v[84:87], v[184:187], v[104:107], 0\n\t"
    "ds_read_b64_tr_b16 v[182:183], v223 offset:54272\n\t"
    "s_waitcnt lgkmcnt(12)\n\t"
    "ds_read_b64_tr_b16 v[206:207], v224 offset:54272\n\t"
    "v_mfma_f32_16x16x32_bf16 v[88:91], v[184:187], v[108:111], 0\n\t"
    "buffer_store_dwordx4 v[100:103], v239, s[52:55], s82 offen sc1\n\t"
    "v_mfma_f32_16x16x32_bf16 a[160:163], v[188:191], v[92:95], 0\n\t"
    "ds_read_b64_tr_b16 v[184:185], v223 offset:50688\n\t"
    "ds_read_b64_tr_b16 v[186:187], v223 offset:54784\n\t"
    "v_mfma_f32_16x16x32_bf16 a[164:167], v[188:191], v[96:99], 0\n\t"
    "buffer_store_dwordx4 v[120:123], v239, s[52:55], s82 offen offset:1024 sc1\n\t"
    "v_mfma_f32_16x16x32_bf16 a[168:171], v[188:191], v[100:103], 0\n\t"
    "buffer_store_dwordx4 v[140:143], v239, s[52:55], s82 offen offset:2048 sc1\n\t"
    "v_mfma_f32_16x16x32_bf16 a[172:175], v[188:191], v[104:107], 0\n\t"
    "s_waitcnt lgkmcnt(10)\n\t"
    "ds_read_b64_tr_b16 v[208:209], v224 offset:50688\n\t"
    "ds_read_b64_tr_b16 v[210:211], v224 offset:54784\n\t"
    "v_mfma_f32_16x16x32_bf16 a[176:179], v[188:191], v[108:111], 0\n\t"
    "ds_read_b64_tr_b16 v[188:189], v224 offset:49152\n\t"
    "ds_read_b64_tr_b16 v[190:191], v224 offset:53248\n\t"
    "v_mfma_f32_16x16x32_bf16 a[180:183], v[192:195], v[92:95], 0\n\t"
    "buffer_store_dwordx4 v[160:163], v239, s[52:55], s82 offen offset:3072 sc1\n\t"
    "v_mfma_f32_16x16x32_bf16 a[184:187], v[192:195], v[96:99], 0\n\t"
    "buffer_store_dwordx4 v[104:107], v239, s[52:55], s83 offen sc1\n\t"
    "v_mfma_f32_16x16x32_bf16 a[188:191], v[192:195], v[100:103], 0\n\t"
    "buffer_store_dwordx4 v[124:127], v239, s[52:55], s83 offen offset:1024 sc1\n\t"
    "v_mfma_f32_16x16x32_bf16 a[192:195], v[192:195], v[104:107], 0\n\t"
    "buffer_store_dwordx4 v[144:147], v239, s[52:55], s83 offen offset:2048 sc1\n\t"
    "v_mfma_f32_16x16x32_bf16 a[196:199], v[192:195], v[108:111], 0\n\t"
    "ds_read_b64_tr_b16 v[192:193], v224 offset:49664\n\t"
    "s_waitcnt lgkmcnt(7)\n\t"
    "ds_read_b64_tr_b16 v[194:195], v224 offset:53760\n\t"
    "v_mfma_f32_16x16x32_bf16 a[200:203], v[196:199], v[92:95], 0\n\t"
    "buffer_store_dwordx4 v[164:167], v239, s[52:55], s83 offen offset:3072 sc1\n\t"
    "v_mfma_f32_16x16x32_bf16 a[204:207], v[196:199], v[96:99], 0\n\t"
    "buffer_store_dwordx4 v[108:111], v239, s[52:55], s84 offen sc1\n\t"
    "v_mfma_f32_16x16x32_bf16 a[208:211], v[196:199], v[100:103], 0\n\t"
    "buffer_store_dwordx4 v[128:131], v239, s[52:55], s84 offen offset:1024 sc1\n\t"
    "v_mfma_f32_16x16x32_bf16 a[212:215], v[196:199], v[104:107], 0\n\t"
    "buffer_store_dwordx4 v[148:151], v239, s[52:55], s84 offen offset:2048 sc1\n\t"
    "v_mfma_f32_16x16x32_bf16 a[216:219], v[196:199], v[108:111], 0\n\t"
    "ds_read_b64_tr_b16 v[196:197], v226 offset:1024\n\t"
    "ds_read_b64_tr_b16 v[198:199], v226 offset:5120\n\t"
    "v_mfma_f32_16x16x32_bf16 a[220:223], v[200:203], v[92:95], 0\n\t"
    "buffer_store_dwordx4 v[168:171], v239, s[52:55], s84 offen offset:3072 sc1\n\t"
    "v_mfma_f32_16x16x32_bf16 a[224:227], v[200:203], v[96:99], 0\n\t"
    "v_mfma_f32_16x16x32_bf16 a[228:231], v[200:203], v[100:103], 0\n\t"
    "v_mfma_f32_16x16x32_bf16 a[232:235], v[200:203], v[104:107], 0\n\t"
    "v_mfma_f32_16x16x32_bf16 a[236:239], v[200:203], v[108:111], 0\n\t"
    "ds_read_b64_tr_b16 v[200:201], v226 offset:1536\n\t"
    "ds_read_b64_tr_b16 v[202:203], v226 offset:5632\n\t"
    "v_mfma_f32_16x16x32_bf16 v[12:15], v[172:175], v[112:115], v[12:15]\n\t"
    "v_mfma_f32_16x16x32_bf16 v[16:19], v[172:175], v[116:119], v[16:19]\n\t"
    "v_mfma_f32_16x16x32_bf16 v[20:23], v[172:175], v[120:123], v[20:23]\n\t"
    "v_mfma_f32_16x16x32_bf16 v[24:27], v[172:175], v[124:127], v[24:27]\n\t"
    "v_mfma_f32_16x16x32_bf16 v[28:31], v[172:175], v[128:131], v[28:31]\n\t"
    "ds_read_b64_tr_b16 v[172:173], v225 offset:0\n\t"
    "ds_read_b64_tr_b16 v[174:175], v225 offset:4096\n\t"
    "v_mfma_f32_16x16x32_bf16 v[32:35], v[176:179], v[112:115], v[32:35]\n\t"
    "v_mfma_f32_16x16x32_bf16 v[36:39], v[176:179], v[116:119], v[36:39]\n\t"
    "v_mfma_f32_16x16x32_bf16 v[40:43], v[176:179], v[120:123], v[40:43]\n\t"
    "v_mfma_f32_16x16x32_bf16 v[44:47], v[176:179], v[124:127], v[44:47]\n\t"
    "v_mfma_f32_16x16x32_bf16 v[48:51], v[176:179], v[128:131], v[48:51]\n\t"
    "ds_read_b64_tr_b16 v[176:177], v225 offset:512\n\t"
    "s_waitcnt lgkmcnt(3)\n\t"
    "ds_read_b64_tr_b16 v[178:179], v225 offset:4608\n\t"
    "v_mfma_f32_16x16x32_bf16 v[52:55], v[180:183], v[112:115], v[52:55]\n\t"
    "v_mfma_f32_16x16x32_bf16 v[56:59], v[180:183], v[116:119], v[56:59]\n\t"
    "v_mfma_f32_16x16x32_bf16 v[60:63], v[180:183], v[120:123], v[60:63]\n\t"
    "v_mfma_f32_16x16x32_bf16 v[64:67], v[180:183], v[124:127], v[64:67]\n\t"
    "v_mfma_f32_16x16x32_bf16 v[68:71], v[180:183], v[128:131], v[68:71]\n\t"
    "ds_read_b64_tr_b16 v[180:181], v225 offset:1024\n\t"
    "ds_read_b64_tr_b16 v[182:183], v225 offset:5120\n\t"
    "v_mfma_f32_16x16x32_bf16 v[72:75], v[184:187], v[112:115], v[72:75]\n\t"
    "v_mfma_f32_16x16x32_bf16 v[76:79], v[184:187], v[116:119], v[76:79]\n\t"
    "v_mfma_f32_16x16x32_bf16 v[80:83], v[184:187], v[120:123], v[80:83]\n\t"
    "v_mfma_f32_16x16x32_bf16 v[84:87], v[184:187], v[124:127], v[84:87]\n\t"
    "v_mfma_f32_16x16x32_bf16 v[88:91], v[184:187], v[128:131], v[88:91]\n\t"
    "ds_read_b64_tr_b16 v[184:185], v225 offset:1536\n\t"
    "ds_read_b64_tr_b16 v[186:187], v225 offset:5632\n\t"
    "v_mfma_f32_16x16x32_bf16 a[160:163], v[188:191], v[112:115], a[160:163]\n\t"
    "v_mfma_f32_16x16x32_bf16 a[164:167], v[188:191], v[116:119], a[164:167]\n\t"
    "v_mfma_f32_16x16x32_bf16 a[168:171], v[188:191], v[120:123], a[168:171]\n\t"
    "v_mfma_f32_16x16x32_bf16 a[172:175], v[188:191], v[124:127], a[172:175]\n\t"
    "v_mfma_f32_16x16x32_bf16 a[176:179], v[188:191], v[128:131], a[176:179]\n\t"
    "ds_read_b64_tr_b16 v[188:189], v226 offset:0\n\t"
    "ds_read_b64_tr_b16 v[190:191], v226 offset:4096\n\t"
    "v_mfma_f32_16x16x32_bf16 a[180:183], v[192:195], v[112:115], a[180:183]\n\t"
    "v_mfma_f32_16x16x32_bf16 a[184:187], v[192:195], v[116:119], a[184:187]\n\t"
    "v_mfma_f32_16x16x32_bf16 a[188:191], v[192:195], v[120:123], a[188:191]\n\t"
    "v_mfma_f32_16x16x32_bf16 a[192:195], v[192:195], v[124:127], a[192:195]\n\t"
    "v_mfma_f32_16x16x32_bf16 a[196:199], v[192:195], v[128:131], a[196:199]\n\t"
    "ds_read_b64_tr_b16 v[192:193], v226 offset:512\n\t"
    "ds_read_b64_tr_b16 v[194:195], v226 offset:4608\n\t"
    "v_mfma_f32_16x16x32_bf16 a[200:203], v[204:207], v[112:115], a[200:203]\n\t"
    "v_mfma_f32_16x16x32_bf16 a[204:207], v[204:207], v[116:119], a[204:207]\n\t"
    "v_mfma_f32_16x16x32_bf16 a[208:211], v[204:207], v[120:123], a[208:211]\n\t"
    "v_mfma_f32_16x16x32_bf16 a[212:215], v[204:207], v[124:127], a[212:215]\n\t"
    "v_mfma_f32_16x16x32_bf16 a[216:219], v[204:207], v[128:131], a[216:219]\n\t"
    "ds_read_b64_tr_b16 v[204:205], v226 offset:17408\n\t"
    "ds_read_b64_tr_b16 v[206:207], v226 offset:21504\n\t"
    "v_mfma_f32_16x16x32_bf16 a[220:223], v[208:211], v[112:115], a[220:223]\n\t"
    "v_mfma_f32_16x16x32_bf16 a[224:227], v[208:211], v[116:119], a[224:227]\n\t"
    "v_mfma_f32_16x16x32_bf16 a[228:231], v[208:211], v[120:123], a[228:231]\n\t"
    "v_mfma_f32_16x16x32_bf16 a[232:235], v[208:211], v[124:127], a[232:235]\n\t"
    "v_mfma_f32_16x16x32_bf16 a[236:239], v[208:211], v[128:131], a[236:239]\n\t"
    "ds_read_b64_tr_b16 v[208:209], v226 offset:17920\n\t"
    "s_waitcnt lgkmcnt(3)\n\t"
    "ds_read_b64_tr_b16 v[210:211], v226 offset:22016\n\t"
    "v_mfma_f32_16x16x32_bf16 v[12:15], v[172:175], v[132:135], v[12:15]\n\t"
    "v_mfma_f32_16x16x32_bf16 v[16:19], v[172:175], v[136:139], v[16:19]\n\t"
    "v_mfma_f32_16x16x32_bf16 v[20:23], v[172:175], v[140:143], v[20:23]\n\t"
    "v_mfma_f32_16x16x32_bf16 v[24:27], v[172:175], v[144:147], v[24:27]\n\t"
    "v_mfma_f32_16x16x32_bf16 v[28:31], v[172:175], v[148:151], v[28:31]\n\t"
    "ds_read_b64_tr_b16 v[172:173], v225 offset:16384\n\t"
    "ds_read_b64_tr_b16 v[174:175], v225 offset:20480\n\t"
    "v_mfma_f32_16x16x32_bf16 v[32:35], v[176:179], v[132:135], v[32:35]\n\t"
    "v_mfma_f32_16x16x32_bf16 v[36:39], v[176:179], v[136:139], v[36:39]\n\t"
    "v_mfma_f32_16x16x32_bf16 v[40:43], v[176:179], v[140:143], v[40:43]\n\t"
    "v_mfma_f32_16x16x32_bf16 v[44:47], v[176:179], v[144:147], v[44:47]\n\t"
    "v_mfma_f32_16x16x32_bf16 v[48:51], v[176:179], v[148:151], v[48:51]\n\t"
    "ds_read_b64_tr_b16 v[176:177], v225 offset:16896\n\t"
    "ds_read_b64_tr_b16 v[178:179], v225 offset:20992\n\t"
    "v_mfma_f32_16x16x32_bf16 v[52:55], v[180:183], v[132:135], v[52:55]\n\t"
    "v_mfma_f32_16x16x32_bf16 v[56:59], v[180:183], v[136:139], v[56:59]\n\t"
    "v_mfma_f32_16x16x32_bf16 v[60:63], v[180:183], v[140:143], v[60:63]\n\t"
    "v_mfma_f32_16x16x32_bf16 v[64:67], v[180:183], v[144:147], v[64:67]\n\t"
    "v_mfma_f32_16x16x32_bf16 v[68:71], v[180:183], v[148:151], v[68:71]\n\t"
    "ds_read_b64_tr_b16 v[180:181], v225 offset:17408\n\t"
    "ds_read_b64_tr_b16 v[182:183], v225 offset:21504\n\t"
    "v_mfma_f32_16x16x32_bf16 v[72:75], v[184:187], v[132:135], v[72:75]\n\t"
    "v_mfma_f32_16x16x32_bf16 v[76:79], v[184:187], v[136:139], v[76:79]\n\t"
    "v_mfma_f32_16x16x32_bf16 v[80:83], v[184:187], v[140:143], v[80:83]\n\t"
    "v_mfma_f32_16x16x32_bf16 v[84:87], v[184:187], v[144:147], v[84:87]\n\t"
    "v_mfma_f32_16x16x32_bf16 v[88:91], v[184:187], v[148:151], v[88:91]\n\t"
    "ds_read_b64_tr_b16 v[184:185], v225 offset:17920\n\t"
    "ds_read_b64_tr_b16 v[186:187], v225 offset:22016\n\t"
    "v_mfma_f32_16x16x32_bf16 a[160:163], v[188:191], v[132:135], a[160:163]\n\t"
    "v_mfma_f32_16x16x32_bf16 a[164:167], v[188:191], v[136:139], a[164:167]\n\t"
    "v_mfma_f32_16x16x32_bf16 a[168:171], v[188:191], v[140:143], a[168:171]\n\t"
    "v_mfma_f32_16x16x32_bf16 a[172:175], v[188:191], v[144:147], a[172:175]\n\t"
    "v_mfma_f32_16x16x32_bf16 a[176:179], v[188:191], v[148:151], a[176:179]\n\t"
    "ds_read_b64_tr_b16 v[188:189], v226 offset:16384\n\t"
    "ds_read_b64_tr_b16 v[190:191], v226 offset:20480\n\t"
    "v_mfma_f32_16x16x32_bf16 a[180:183], v[192:195], v[132:135], a[180:183]\n\t"
    "v_mfma_f32_16x16x32_bf16 a[184:187], v[192:195], v[136:139], a[184:187]\n\t"
    "v_mfma_f32_16x16x32_bf16 a[188:191], v[192:195], v[140:143], a[188:191]\n\t"
    "v_mfma_f32_16x16x32_bf16 a[192:195], v[192:195], v[144:147], a[192:195]\n\t"
    "v_mfma_f32_16x16x32_bf16 a[196:199], v[192:195], v[148:151], a[196:199]\n\t"
    "ds_read_b64_tr_b16 v[192:193], v226 offset:16896\n\t"
    "s_waitcnt lgkmcnt(3)\n\t"
    "ds_read_b64_tr_b16 v[194:195], v226 offset:20992\n\t"
    "v_mfma_f32_16x16x32_bf16 a[200:203], v[196:199], v[132:135], a[200:203]\n\t"
    "v_mfma_f32_16x16x32_bf16 a[204:207], v[196:199], v[136:139], a[204:207]\n\t"
    "v_mfma_f32_16x16x32_bf16 a[208:211], v[196:199], v[140:143], a[208:211]\n\t"
    "v_mfma_f32_16x16x32_bf16 a[212:215], v[196:199], v[144:147], a[212:215]\n\t"
    "v_mfma_f32_16x16x32_bf16 a[216:219], v[196:199], v[148:151], a[216:219]\n\t"
    "ds_read_b64_tr_b16 v[196:197], v222 offset:1024\n\t"
    "ds_read_b64_tr_b16 v[198:199], v222 offset:5120\n\t"
    "v_mfma_f32_16x16x32_bf16 a[220:223], v[200:203], v[132:135], a[220:223]\n\t"
    "v_mfma_f32_16x16x32_bf16 a[224:227], v[200:203], v[136:139], a[224:227]\n\t"
    "v_mfma_f32_16x16x32_bf16 a[228:231], v[200:203], v[140:143], a[228:231]\n\t"
    "v_mfma_f32_16x16x32_bf16 a[232:235], v[200:203], v[144:147], a[232:235]\n\t"
    "v_mfma_f32_16x16x32_bf16 a[236:239], v[200:203], v[148:151], a[236:239]\n\t"
    "ds_read_b64_tr_b16 v[200:201], v222 offset:1536\n\t"
    "ds_read_b64_tr_b16 v[202:203], v222 offset:5632\n\t"
    "v_mfma_f32_16x16x32_bf16 v[12:15], v[172:175], v[152:155], v[12:15]\n\t"
    "v_mfma_f32_16x16x32_bf16 v[16:19], v[172:175], v[156:159], v[16:19]\n\t"
    "v_mfma_f32_16x16x32_bf16 v[20:23], v[172:175], v[160:163], v[20:23]\n\t"
    "v_mfma_f32_16x16x32_bf16 v[24:27], v[172:175], v[164:167], v[24:27]\n\t"
    "v_mfma_f32_16x16x32_bf16 v[28:31], v[172:175], v[168:171], v[28:31]\n\t"
    "ds_read_b64_tr_b16 v[172:173], v221 offset:0\n\t"
    "ds_read_b64_tr_b16 v[174:175], v221 offset:4096\n\t"
    "v_mfma_f32_16x16x32_bf16 v[32:35], v[176:179], v[152:155], v[32:35]\n\t"
    "v_mfma_f32_16x16x32_bf16 v[36:39], v[176:179], v[156:159], v[36:39]\n\t"
    "v_mfma_f32_16x16x32_bf16 v[40:43], v[176:179], v[160:163], v[40:43]\n\t"
    "v_mfma_f32_16x16x32_bf16 v[44:47], v[176:179], v[164:167], v[44:47]\n\t"
    "v_mfma_f32_16x16x32_bf16 v[48:51], v[176:179], v[168:171], v[48:51]\n\t"
    "ds_read_b64_tr_b16 v[176:177], v221 offset:512\n\t"
    "ds_read_b64_tr_b16 v[178:179], v221 offset:4608\n\t"
    "v_mfma_f32_16x16x32_bf16 v[52:55], v[180:183], v[152:155], v[52:55]\n\t"
    "v_mfma_f32_16x16x32_bf16 v[56:59], v[180:183], v[156:159], v[56:59]\n\t"
    "v_mfma_f32_16x16x32_bf16 v[60:63], v[180:183], v[160:163], v[60:63]\n\t"
    "v_mfma_f32_16x16x32_bf16 v[64:67], v[180:183], v[164:167], v[64:67]\n\t"
    "v_mfma_f32_16x16x32_bf16 v[68:71], v[180:183], v[168:171], v[68:71]\n\t"
    "ds_read_b64_tr_b16 v[180:181], v221 offset:1024\n\t"
    "ds_read_b64_tr_b16 v[182:183], v221 offset:5120\n\t"
    "v_mfma_f32_16x16x32_bf16 v[72:75], v[184:187], v[152:155], v[72:75]\n\t"
    "v_mfma_f32_16x16x32_bf16 v[76:79], v[184:187], v[156:159], v[76:79]\n\t"
    "v_mfma_f32_16x16x32_bf16 v[80:83], v[184:187], v[160:163], v[80:83]\n\t"
    "v_mfma_f32_16x16x32_bf16 v[84:87], v[184:187], v[164:167], v[84:87]\n\t"
    "v_mfma_f32_16x16x32_bf16 v[88:91], v[184:187], v[168:171], v[88:91]\n\t"
    "ds_read_b64_tr_b16 v[184:185], v221 offset:1536\n\t"
    "s_waitcnt lgkmcnt(3)\n\t"
    "ds_read_b64_tr_b16 v[186:187], v221 offset:5632\n\t"
    "v_mfma_f32_16x16x32_bf16 a[160:163], v[188:191], v[152:155], a[160:163]\n\t"
    "v_mfma_f32_16x16x32_bf16 a[164:167], v[188:191], v[156:159], a[164:167]\n\t"
    "v_mfma_f32_16x16x32_bf16 a[168:171], v[188:191], v[160:163], a[168:171]\n\t"
    "v_mfma_f32_16x16x32_bf16 a[172:175], v[188:191], v[164:167], a[172:175]\n\t"
    "v_mfma_f32_16x16x32_bf16 a[176:179], v[188:191], v[168:171], a[176:179]\n\t"
    "ds_read_b64_tr_b16 v[188:189], v222 offset:0\n\t"
    "ds_read_b64_tr_b16 v[190:191], v222 offset:4096\n\t"
    "v_mfma_f32_16x16x32_bf16 a[180:183], v[192:195], v[152:155], a[180:183]\n\t"
    "v_mfma_f32_16x16x32_bf16 a[184:187], v[192:195], v[156:159], a[184:187]\n\t"
    "v_mfma_f32_16x16x32_bf16 a[188:191], v[192:195], v[160:163], a[188:191]\n\t"
    "v_mfma_f32_16x16x32_bf16 a[192:195], v[192:195], v[164:167], a[192:195]\n\t"
    "v_mfma_f32_16x16x32_bf16 a[196:199], v[192:195], v[168:171], a[196:199]\n\t"
    "ds_read_b64_tr_b16 v[192:193], v222 offset:512\n\t"
    "ds_read_b64_tr_b16 v[194:195], v222 offset:4608\n\t"
    "v_mfma_f32_16x16x32_bf16 a[200:203], v[204:207], v[152:155], a[200:203]\n\t"
    "v_mfma_f32_16x16x32_bf16 a[204:207], v[204:207], v[156:159], a[204:207]\n\t"
    "v_mfma_f32_16x16x32_bf16 a[208:211], v[204:207], v[160:163], a[208:211]\n\t"
    "v_mfma_f32_16x16x32_bf16 a[212:215], v[204:207], v[164:167], a[212:215]\n\t"
    "v_mfma_f32_16x16x32_bf16 a[216:219], v[204:207], v[168:171], a[216:219]\n\t"
    "v_mfma_f32_16x16x32_bf16 a[220:223], v[208:211], v[152:155], a[220:223]\n\t"
    "v_mfma_f32_16x16x32_bf16 a[224:227], v[208:211], v[156:159], a[224:227]\n\t"
    "v_mfma_f32_16x16x32_bf16 a[228:231], v[208:211], v[160:163], a[228:231]\n\t"
    "v_mfma_f32_16x16x32_bf16 a[232:235], v[208:211], v[164:167], a[232:235]\n\t"
    "v_mfma_f32_16x16x32_bf16 a[236:239], v[208:211], v[168:171], a[236:239]\n\t"
    "s_waitcnt vmcnt(0)\n\t"
    "s_mov_b64 exec, 1\n\t"
    "buffer_store_dwordx2 v[212:213], v216, s[56:59], 0 offen sc1\n\t"
    "s_mov_b64 exec, -1\n\t"
    "s_waitcnt lgkmcnt(0)\n\t"
    "s_barrier\n\t"
    "s_add_u32 s64, s62, 0x10000\n\t"
    "s_add_u32 m0, %17, 0x18000\n\t"
    "s_nop 0\n\t"
    "buffer_load_dwordx4 %9, s[36:39], s64 offen lds\n\t"
    "s_add_u32 m0, m0, 0x400\n\t"
    "s_nop 0\n\t"
    "buffer_load_dwordx4 v230, s[36:39], s64 offen lds\n\t"
    "s_add_u32 m0, m0, 0x400\n\t"
    "s_nop 0\n\t"
    "buffer_load_dwordx4 v219, s[36:39], s64 offen lds\n\t"
    "s_add_u32 m0, m0, 0x400\n\t"
    "s_nop 0\n\t"
    "buffer_load_dwordx4 v220, s[36:39], s64 offen lds\n\t"
    "s_add_u32 s69, s62, 0x18000\n\t"
    "s_add_u32 m0, %17, 0x1c000\n\t"
    "s_nop 0\n\t"
    "buffer_load_dwordx4 %9, s[36:39], s69 offen lds\n\t"
    "s_add_u32 m0, m0, 0x400\n\t"
    "s_nop 0\n\t"
    "buffer_load_dwordx4 v230, s[36:39], s69 offen lds\n\t"
    "s_add_u32 m0, m0, 0x400\n\t"
    "s_nop 0\n\t"
    "buffer_load_dwordx4 v219, s[36:39], s69 offen lds\n\t"
    "s_add_u32 m0, m0, 0x400\n\t"
    "s_nop 0\n\t"
    "buffer_load_dwordx4 v220, s[36:39], s69 offen lds\n\t"
    "s_add_u32 s64, s63, 0\n\t"
    "s_add_u32 m0, %17, 0x20000\n\t"
    "s_nop 0\n\t"
    "buffer_load_dwordx4 %9, s[36:39], s64 offen lds\n\t"
    "s_add_u32 m0, m0, 0x400\n\t"
    "s_nop 0\n\t"
    "buffer_load_dwordx4 v230, s[36:39], s64 offen lds\n\t"
    "s_add_u32 m0, m0, 0x400\n\t"
    "s_nop 0\n\t"
    "buffer_load_dwordx4 v219, s[36:39], s64 offen lds\n\t"
    "s_add_u32 m0, m0, 0x400\n\t"
    "s_nop 0\n\t"
    "buffer_load_dwordx4 v220, s[36:39], s64 offen lds\n\t"
    "s_add_u32 s69, s63, 0x8000\n\t"
    "s_add_u32 m0, %17, 0x24000\n\t"
    "s_nop 0\n\t"
    "buffer_load_dwordx4 %9, s[36:39], s69 offen lds\n\t"
    "s_add_u32 m0, m0, 0x400\n\t"
    "s_nop 0\n\t"
    "buffer_load_dwordx4 v230, s[36:39], s69 offen lds\n\t"
    "s_add_u32 m0, m0, 0x400\n\t"
    "s_nop 0\n\t"
    "buffer_load_dwordx4 v219, s[36:39], s69 offen lds\n\t"
    "s_add_u32 m0, m0, 0x400\n\t"
    "s_nop 0\n\t"
    "buffer_load_dwordx4 v220, s[36:39], s69 offen lds\n\t"
    "s_mov_b32 s64, 0x10000\n\t"
    "K1_POLL1:\n\t"
    "buffer_load_dwordx2 v[214:215], v216, s[56:59], 0 offen sc1\n\t"
    "s_waitcnt vmcnt(0)\n\t"
    "v_cmp_eq_u32 vcc, v214, v212\n\t"
    "v_cmp_eq_u32 s[76:77], v215, v213\n\t"
    "s_nop 3\n\t"
    "s_and_b64 vcc, vcc, s[76:77]\n\t"
    "s_and_b32 vcc_lo, vcc_lo, 14\n\t"
    "s_cmp_eq_u32 vcc_lo, 14\n\t"
    "s_cbranch_scc1 K1_POLL1_OK\n\t"
    "s_sleep 1\n\t"
    "s_sub_u32 s64, s64, 1\n\t"
    "s_cmp_lg_u32 s64, 0\n\t"
    "s_cbranch_scc1 K1_POLL1\n\t"
    "K1_POLL1_OK:\n\t"
    "; vm reset\n\t"
    "s_add_u32 s74, s66, 0\n\t"
    "buffer_load_dwordx4 a[0:3], v239, s[52:55], s74 offen sc1\n\t"
    "s_add_u32 s75, s66, 0x1000\n\t"
    "buffer_load_dwordx4 a[4:7], v239, s[52:55], s75 offen sc1\n\t"
    "s_add_u32 s74, s66, 0x2000\n\t"
    "buffer_load_dwordx4 a[8:11], v239, s[52:55], s74 offen sc1\n\t"
    "s_add_u32 s75, s66, 0x3000\n\t"
    "buffer_load_dwordx4 a[12:15], v239, s[52:55], s75 offen sc1\n\t"
    "s_add_u32 s74, s66, 0x4000\n\t"
    "buffer_load_dwordx4 a[16:19], v239, s[52:55], s74 offen sc1\n\t"
    "s_add_u32 s74, s66, 0\n\t"
    "buffer_load_dwordx4 a[20:23], v239, s[52:55], s74 offen offset:1024 sc1\n\t"
    "s_add_u32 s75, s66, 0x1000\n\t"
    "buffer_load_dwordx4 a[24:27], v239, s[52:55], s75 offen offset:1024 sc1\n\t"
    "s_add_u32 s74, s66, 0x2000\n\t"
    "buffer_load_dwordx4 a[28:31], v239, s[52:55], s74 offen offset:1024 sc1\n\t"
    "s_add_u32 s75, s66, 0x3000\n\t"
    "buffer_load_dwordx4 a[32:35], v239, s[52:55], s75 offen offset:1024 sc1\n\t"
    "s_add_u32 s74, s66, 0x4000\n\t"
    "buffer_load_dwordx4 a[36:39], v239, s[52:55], s74 offen offset:1024 sc1\n\t"
    "s_waitcnt vmcnt(5) lgkmcnt(0)\n\t"
    "s_barrier\n\t"
    "v_mfma_f32_16x16x32_bf16 v[12:15], v[172:175], a[0:3], v[12:15]\n\t"
    "s_add_u32 s64, s63, 0x10000\n\t"
    "s_add_u32 m0, %17, 0\n\t"
    "v_mfma_f32_16x16x32_bf16 v[16:19], v[172:175], a[4:7], v[16:19]\n\t"
    "buffer_load_dwordx4 %9, s[36:39], s64 offen lds\n\t"
    "v_mfma_f32_16x16x32_bf16 v[20:23], v[172:175], a[8:11], v[20:23]\n\t"
    "s_add_u32 m0, m0, 0x400\n\t"
    "ds_read_b64_tr_b16 v[204:205], v222 offset:17408\n\t"
    "v_mfma_f32_16x16x32_bf16 v[24:27], v[172:175], a[12:15], v[24:27]\n\t"
    "buffer_load_dwordx4 v230, s[36:39], s64 offen lds\n\t"
    "v_mfma_f32_16x16x32_bf16 v[28:31], v[172:175], a[16:19], v[28:31]\n\t"
    "s_add_u32 m0, m0, 0x400\n\t"
    "ds_read_b64_tr_b16 v[172:173], v221 offset:16384\n\t"
    "v_mfma_f32_16x16x32_bf16 v[32:35], v[176:179], a[0:3], v[32:35]\n\t"
    "buffer_load_dwordx4 v219, s[36:39], s64 offen lds\n\t"
    "v_mfma_f32_16x16x32_bf16 v[36:39], v[176:179], a[4:7], v[36:39]\n\t"
    "s_add_u32 m0, m0, 0x400\n\t"
    "s_add_u32 s74, s66, 0\n\t"
    "v_mfma_f32_16x16x32_bf16 v[40:43], v[176:179], a[8:11], v[40:43]\n\t"
    "buffer_load_dwordx4 v220, s[36:39], s64 offen lds\n\t"
    "v_mfma_f32_16x16x32_bf16 v[44:47], v[176:179], a[12:15], v[44:47]\n\t"
    "buffer_load_dwordx4 a[40:43], v239, s[52:55], s74 offen offset:2048 sc1\n\t"
    "v_mfma_f32_16x16x32_bf16 v[48:51], v[176:179], a[16:19], v[48:51]\n\t"
    "s_add_u32 s75, s66, 0x1000\n\t"
    "s_add_u32 s74, s66, 0x2000\n\t"
    "v_mfma_f32_16x16x32_bf16 v[52:55], v[180:183], a[0:3], v[52:55]\n\t"
    "buffer_load_dwordx4 a[44:47], v239, s[52:55], s75 offen offset:2048 sc1\n\t"
    "v_mfma_f32_16x16x32_bf16 v[56:59], v[180:183], a[4:7], v[56:59]\n\t"
    "buffer_load_dwordx4 a[48:51], v239, s[52:55], s74 offen offset:2048 sc1\n\t"
    "v_mfma_f32_16x16x32_bf16 v[60:63], v[180:183], a[8:11], v[60:63]\n\t"
    "s_add_u32 s75, s66, 0x3000\n\t"
    "s_add_u32 s74, s66, 0x4000\n\t"
    "v_mfma_f32_16x16x32_bf16 v[64:67], v[180:183], a[12:15], v[64:67]\n\t"
    "buffer_load_dwordx4 a[52:55], v239, s[52:55], s75 offen offset:2048 sc1\n\t"
    "v_mfma_f32_16x16x32_bf16 v[68:71], v[180:183], a[16:19], v[68:71]\n\t"
    "buffer_load_dwordx4 a[56:59], v239, s[52:55], s74 offen offset:2048 sc1\n\t"
    "v_mfma_f32_16x16x32_bf16 v[72:75], v[184:187], a[0:3], v[72:75]\n\t"
    "ds_read_b64_tr_b16 v[174:175], v221 offset:20480\n\t"
    "ds_read_b64_tr_b16 v[176:177], v221 offset:16896\n\t"
    "v_mfma_f32_16x16x32_bf16 v[76:79], v[184:187], a[4:7], v[76:79]\n\t"
    "ds_read_b64_tr_b16 v[178:179], v221 offset:20992\n\t"
    "ds_read_b64_tr_b16 v[180:181], v221 offset:17408\n\t"
    "v_mfma_f32_16x16x32_bf16 v[80:83], v[184:187], a[8:11], v[80:83]\n\t"
    "ds_read_b64_tr_b16 v[182:183], v221 offset:21504\n\t"
    "ds_read_b64_tr_b16 v[206:207], v222 offset:21504\n\t"
    "v_mfma_f32_16x16x32_bf16 v[84:87], v[184:187], a[12:15], v[84:87]\n\t"
    "ds_read_b64_tr_b16 v[208:209], v222 offset:17920\n\t"
    "ds_read_b64_tr_b16 v[210:211], v222 offset:22016\n\t"
    "v_mfma_f32_16x16x32_bf16 v[88:91], v[184:187], a[16:19], v[88:91]\n\t"
    "ds_read_b64_tr_b16 v[184:185], v221 offset:17920\n\t"
    "ds_read_b64_tr_b16 v[186:187], v221 offset:22016\n\t"
    "v_mfma_f32_16x16x32_bf16 a[160:163], v[188:191], a[0:3], a[160:163]\n\t"
    "v_mfma_f32_16x16x32_bf16 a[164:167], v[188:191], a[4:7], a[164:167]\n\t"
    "v_mfma_f32_16x16x32_bf16 a[168:171], v[188:191], a[8:11], a[168:171]\n\t"
    "v_mfma_f32_16x16x32_bf16 a[172:175], v[188:191], a[12:15], a[172:175]\n\t"
    "v_mfma_f32_16x16x32_bf16 a[176:179], v[188:191], a[16:19], a[176:179]\n\t"
    "ds_read_b64_tr_b16 v[188:189], v222 offset:16384\n\t"
    "ds_read_b64_tr_b16 v[190:191], v222 offset:20480\n\t"
    "v_mfma_f32_16x16x32_bf16 a[180:183], v[192:195], a[0:3], a[180:183]\n\t"
    "v_mfma_f32_16x16x32_bf16 a[184:187], v[192:195], a[4:7], a[184:187]\n\t"
    "v_mfma_f32_16x16x32_bf16 a[188:191], v[192:195], a[8:11], a[188:191]\n\t"
    "v_mfma_f32_16x16x32_bf16 a[192:195], v[192:195], a[12:15], a[192:195]\n\t"
    "v_mfma_f32_16x16x32_bf16 a[196:199], v[192:195], a[16:19], a[196:199]\n\t"
    "ds_read_b64_tr_b16 v[192:193], v222 offset:16896\n\t"
    "s_waitcnt lgkmcnt(3)\n\t"
    "ds_read_b64_tr_b16 v[194:195], v222 offset:20992\n\t"
    "v_mfma_f32_16x16x32_bf16 a[200:203], v[196:199], a[0:3], a[200:203]\n\t"
    "v_mfma_f32_16x16x32_bf16 a[204:207], v[196:199], a[4:7], a[204:207]\n\t"
    "v_mfma_f32_16x16x32_bf16 a[208:211], v[196:199], a[8:11], a[208:211]\n\t"
    "v_mfma_f32_16x16x32_bf16 a[212:215], v[196:199], a[12:15], a[212:215]\n\t"
    "v_mfma_f32_16x16x32_bf16 a[216:219], v[196:199], a[16:19], a[216:219]\n\t"
    "v_mfma_f32_16x16x32_bf16 a[220:223], v[200:203], a[0:3], a[220:223]\n\t"
    "v_mfma_f32_16x16x32_bf16 a[224:227], v[200:203], a[4:7], a[224:227]\n\t"
    "v_mfma_f32_16x16x32_bf16 a[228:231], v[200:203], a[8:11], a[228:231]\n\t"
    "v_mfma_f32_16x16x32_bf16 a[232:235], v[200:203], a[12:15], a[232:235]\n\t"
    "v_mfma_f32_16x16x32_bf16 a[236:239], v[200:203], a[16:19], a[236:239]\n\t"
    "s_waitcnt vmcnt(9) lgkmcnt(0)\n\t"
    "s_barrier\n\t"
    "v_mfma_f32_16x16x32_bf16 v[12:15], v[172:175], a[20:23], v[12:15]\n\t"
    "s_add_u32 s69, s63, 0x18000\n\t"
    "s_add_u32 m0, %17, 0x4000\n\t"
    "v_mfma_f32_16x16x32_bf16 v[16:19], v[172:175], a[24:27], v[16:19]\n\t"
    "buffer_load_dwordx4 %9, s[36:39], s69 offen lds\n\t"
    "v_mfma_f32_16x16x32_bf16 v[20:23], v[172:175], a[28:31], v[20:23]\n\t"
    "s_add_u32 m0, m0, 0x400\n\t"
    "ds_read_b64_tr_b16 v[196:197], v222 offset:33792\n\t"
    "v_mfma_f32_16x16x32_bf16 v[24:27], v[172:175], a[32:35], v[24:27]\n\t"
    "buffer_load_dwordx4 v230, s[36:39], s69 offen lds\n\t"
    "v_mfma_f32_16x16x32_bf16 v[28:31], v[172:175], a[36:39], v[28:31]\n\t"
    "s_add_u32 m0, m0, 0x400\n\t"
    "ds_read_b64_tr_b16 v[172:173], v221 offset:32768\n\t"
    "v_mfma_f32_16x16x32_bf16 v[32:35], v[176:179], a[20:23], v[32:35]\n\t"
    "buffer_load_dwordx4 v219, s[36:39], s69 offen lds\n\t"
    "v_mfma_f32_16x16x32_bf16 v[36:39], v[176:179], a[24:27], v[36:39]\n\t"
    "s_add_u32 m0, m0, 0x400\n\t"
    "s_add_u32 s74, s66, 0\n\t"
    "v_mfma_f32_16x16x32_bf16 v[40:43], v[176:179], a[28:31], v[40:43]\n\t"
    "buffer_load_dwordx4 v220, s[36:39], s69 offen lds\n\t"
    "v_mfma_f32_16x16x32_bf16 v[44:47], v[176:179], a[32:35], v[44:47]\n\t"
    "buffer_load_dwordx4 a[60:63], v239, s[52:55], s74 offen offset:3072 sc1\n\t"
    "v_mfma_f32_16x16x32_bf16 v[48:51], v[176:179], a[36:39], v[48:51]\n\t"
    "s_add_u32 s75, s66, 0x1000\n\t"
    "s_add_u32 s74, s66, 0x2000\n\t"
    "v_mfma_f32_16x16x32_bf16 v[52:55], v[180:183], a[20:23], v[52:55]\n\t"
    "buffer_load_dwordx4 a[64:67], v239, s[52:55], s75 offen offset:3072 sc1\n\t"
    "v_mfma_f32_16x16x32_bf16 v[56:59], v[180:183], a[24:27], v[56:59]\n\t"
    "buffer_load_dwordx4 a[68:71], v239, s[52:55], s74 offen offset:3072 sc1\n\t"
    "v_mfma_f32_16x16x32_bf16 v[60:63], v[180:183], a[28:31], v[60:63]\n\t"
    "s_add_u32 s75, s66, 0x3000\n\t"
    "s_add_u32 s74, s66, 0x4000\n\t"
    "v_mfma_f32_16x16x32_bf16 v[64:67], v[180:183], a[32:35], v[64:67]\n\t"
    "buffer_load_dwordx4 a[72:75], v239, s[52:55], s75 offen offset:3072 sc1\n\t"
    "v_mfma_f32_16x16x32_bf16 v[68:71], v[180:183], a[36:39], v[68:71]\n\t"
    "buffer_load_dwordx4 a[76:79], v239, s[52:55], s74 offen offset:3072 sc1\n\t"
    "v_mfma_f32_16x16x32_bf16 v[72:75], v[184:187], a[20:23], v[72:75]\n\t"
    "ds_read_b64_tr_b16 v[174:175], v221 offset:36864\n\t"
    "ds_read_b64_tr_b16 v[176:177], v221 offset:33280\n\t"
    "v_mfma_f32_16x16x32_bf16 v[76:79], v[184:187], a[24:27], v[76:79]\n\t"
    "ds_read_b64_tr_b16 v[178:179], v221 offset:37376\n\t"
    "ds_read_b64_tr_b16 v[180:181], v221 offset:33792\n\t"
    "v_mfma_f32_16x16x32_bf16 v[80:83], v[184:187], a[28:31], v[80:83]\n\t"
    "ds_read_b64_tr_b16 v[182:183], v221 offset:37888\n\t"
    "ds_read_b64_tr_b16 v[198:199], v222 offset:37888\n\t"
    "v_mfma_f32_16x16x32_bf16 v[84:87], v[184:187], a[32:35], v[84:87]\n\t"
    "ds_read_b64_tr_b16 v[200:201], v222 offset:34304\n\t"
    "ds_read_b64_tr_b16 v[202:203], v222 offset:38400\n\t"
    "v_mfma_f32_16x16x32_bf16 v[88:91], v[184:187], a[36:39], v[88:91]\n\t"
    "ds_read_b64_tr_b16 v[184:185], v221 offset:34304\n\t"
    "ds_read_b64_tr_b16 v[186:187], v221 offset:38400\n\t"
    "v_mfma_f32_16x16x32_bf16 a[160:163], v[188:191], a[20:23], a[160:163]\n\t"
    "v_mfma_f32_16x16x32_bf16 a[164:167], v[188:191], a[24:27], a[164:167]\n\t"
    "v_mfma_f32_16x16x32_bf16 a[168:171], v[188:191], a[28:31], a[168:171]\n\t"
    "v_mfma_f32_16x16x32_bf16 a[172:175], v[188:191], a[32:35], a[172:175]\n\t"
    "v_mfma_f32_16x16x32_bf16 a[176:179], v[188:191], a[36:39], a[176:179]\n\t"
    "ds_read_b64_tr_b16 v[188:189], v222 offset:32768\n\t"
    "ds_read_b64_tr_b16 v[190:191], v222 offset:36864\n\t"
    "v_mfma_f32_16x16x32_bf16 a[180:183], v[192:195], a[20:23], a[180:183]\n\t"
    "v_mfma_f32_16x16x32_bf16 a[184:187], v[192:195], a[24:27], a[184:187]\n\t"
    "v_mfma_f32_16x16x32_bf16 a[188:191], v[192:195], a[28:31], a[188:191]\n\t"
    "v_mfma_f32_16x16x32_bf16 a[192:195], v[192:195], a[32:35], a[192:195]\n\t"
    "v_mfma_f32_16x16x32_bf16 a[196:199], v[192:195], a[36:39], a[196:199]\n\t"
    "ds_read_b64_tr_b16 v[192:193], v222 offset:33280\n\t"
    "s_waitcnt lgkmcnt(3)\n\t"
    "ds_read_b64_tr_b16 v[194:195], v222 offset:37376\n\t"
    "v_mfma_f32_16x16x32_bf16 a[200:203], v[204:207], a[20:23], a[200:203]\n\t"
    "v_mfma_f32_16x16x32_bf16 a[204:207], v[204:207], a[24:27], a[204:207]\n\t"
    "v_mfma_f32_16x16x32_bf16 a[208:211], v[204:207], a[28:31], a[208:211]\n\t"
    "v_mfma_f32_16x16x32_bf16 a[212:215], v[204:207], a[32:35], a[212:215]\n\t"
    "v_mfma_f32_16x16x32_bf16 a[216:219], v[204:207], a[36:39], a[216:219]\n\t"
    "v_mfma_f32_16x16x32_bf16 a[220:223], v[208:211], a[20:23], a[220:223]\n\t"
    "v_mfma_f32_16x16x32_bf16 a[224:227], v[208:211], a[24:27], a[224:227]\n\t"
    "v_mfma_f32_16x16x32_bf16 a[228:231], v[208:211], a[28:31], a[228:231]\n\t"
    "v_mfma_f32_16x16x32_bf16 a[232:235], v[208:211], a[32:35], a[232:235]\n\t"
    "v_mfma_f32_16x16x32_bf16 a[236:239], v[208:211], a[36:39], a[236:239]\n\t"
    "s_waitcnt vmcnt(9)\n\t"
    "v_mfma_f32_16x16x32_bf16 v[12:15], v[172:175], a[40:43], v[12:15]\n\t"
    "s_add_u32 s74, s67, 0\n\t"
    "s_add_u32 s75, s67, 0x1000\n\t"
    "v_mfma_f32_16x16x32_bf16 v[16:19], v[172:175], a[44:47], v[16:19]\n\t"
    "buffer_load_dwordx4 a[80:83], v239, s[52:55], s74 offen sc1\n\t"
    "v_mfma_f32_16x16x32_bf16 v[20:23], v[172:175], a[48:51], v[20:23]\n\t"
    "buffer_load_dwordx4 a[84:87], v239, s[52:55], s75 offen sc1\n\t"
    "v_mfma_f32_16x16x32_bf16 v[24:27], v[172:175], a[52:55], v[24:27]\n\t"
    "s_add_u32 s74, s67, 0x2000\n\t"
    "s_add_u32 s75, s67, 0x3000\n\t"
    "v_mfma_f32_16x16x32_bf16 v[28:31], v[172:175], a[56:59], v[28:31]\n\t"
    "buffer_load_dwordx4 a[88:91], v239, s[52:55], s74 offen sc1\n\t"
    "v_mfma_f32_16x16x32_bf16 v[32:35], v[176:179], a[40:43], v[32:35]\n\t"
    "buffer_load_dwordx4 a[92:95], v239, s[52:55], s75 offen sc1\n\t"
    "v_mfma_f32_16x16x32_bf16 v[36:39], v[176:179], a[44:47], v[36:39]\n\t"
    "s_add_u32 s74, s67, 0x4000\n\t"
    "ds_read_b64_tr_b16 v[172:173], v221 offset:49152\n\t"
    "v_mfma_f32_16x16x32_bf16 v[40:43], v[176:179], a[48:51], v[40:43]\n\t"
    "buffer_load_dwordx4 a[96:99], v239, s[52:55], s74 offen sc1\n\t"
    "v_mfma_f32_16x16x32_bf16 v[44:47], v[176:179], a[52:55], v[44:47]\n\t"
    "ds_read_b64_tr_b16 v[174:175], v221 offset:53248\n\t"
    "ds_read_b64_tr_b16 v[204:205], v222 offset:50176\n\t"
    "v_mfma_f32_16x16x32_bf16 v[48:51], v[176:179], a[56:59], v[48:51]\n\t"
    "ds_read_b64_tr_b16 v[176:177], v221 offset:49664\n\t"
    "ds_read_b64_tr_b16 v[178:179], v221 offset:53760\n\t"
    "v_mfma_f32_16x16x32_bf16 v[52:55], v[180:183], a[40:43], v[52:55]\n\t"
    "ds_read_b64_tr_b16 v[206:207], v222 offset:54272\n\t"
    "ds_read_b64_tr_b16 v[208:209], v222 offset:50688\n\t"
    "v_mfma_f32_16x16x32_bf16 v[56:59], v[180:183], a[44:47], v[56:59]\n\t"
    "ds_read_b64_tr_b16 v[210:211], v222 offset:54784\n\t"
    "v_mfma_f32_16x16x32_bf16 v[60:63], v[180:183], a[48:51], v[60:63]\n\t"
    "v_mfma_f32_16x16x32_bf16 v[64:67], v[180:183], a[52:55], v[64:67]\n\t"
    "v_mfma_f32_16x16x32_bf16 v[68:71], v[180:183], a[56:59], v[68:71]\n\t"
    "ds_read_b64_tr_b16 v[180:181], v221 offset:50176\n\t"
    "ds_read_b64_tr_b16 v[182:183], v221 offset:54272\n\t"
    "v_mfma_f32_16x16x32_bf16 v[72:75], v[184:187], a[40:43], v[72:75]\n\t"
    "v_mfma_f32_16x16x32_bf16 v[76:79], v[184:187], a[44:47], v[76:79]\n\t"
    "v_mfma_f32_16x16x32_bf16 v[80:83], v[184:187], a[48:51], v[80:83]\n\t"
    "v_mfma_f32_16x16x32_bf16 v[84:87], v[184:187], a[52:55], v[84:87]\n\t"
    "v_mfma_f32_16x16x32_bf16 v[88:91], v[184:187], a[56:59], v[88:91]\n\t"
    "ds_read_b64_tr_b16 v[184:185], v221 offset:50688\n\t"
    "s_waitcnt lgkmcnt(6)\n\t"
    "ds_read_b64_tr_b16 v[186:187], v221 offset:54784\n\t"
    "v_mfma_f32_16x16x32_bf16 a[160:163], v[188:191], a[40:43], a[160:163]\n\t"
    "v_mfma_f32_16x16x32_bf16 a[164:167], v[188:191], a[44:47], a[164:167]\n\t"
    "v_mfma_f32_16x16x32_bf16 a[168:171], v[188:191], a[48:51], a[168:171]\n\t"
    "v_mfma_f32_16x16x32_bf16 a[172:175], v[188:191], a[52:55], a[172:175]\n\t"
    "v_mfma_f32_16x16x32_bf16 a[176:179], v[188:191], a[56:59], a[176:179]\n\t"
    "ds_read_b64_tr_b16 v[188:189], v222 offset:49152\n\t"
    "ds_read_b64_tr_b16 v[190:191], v222 offset:53248\n\t"
    "v_mfma_f32_16x16x32_bf16 a[180:183], v[192:195], a[40:43], a[180:183]\n\t"
    "v_mfma_f32_16x16x32_bf16 a[184:187], v[192:195], a[44:47], a[184:187]\n\t"
    "v_mfma_f32_16x16x32_bf16 a[188:191], v[192:195], a[48:51], a[188:191]\n\t"
    "v_mfma_f32_16x16x32_bf16 a[192:195], v[192:195], a[52:55], a[192:195]\n\t"
    "v_mfma_f32_16x16x32_bf16 a[196:199], v[192:195], a[56:59], a[196:199]\n\t"
    "ds_read_b64_tr_b16 v[192:193], v222 offset:49664\n\t"
    "ds_read_b64_tr_b16 v[194:195], v222 offset:53760\n\t"
    "v_mfma_f32_16x16x32_bf16 a[200:203], v[196:199], a[40:43], a[200:203]\n\t"
    "v_mfma_f32_16x16x32_bf16 a[204:207], v[196:199], a[44:47], a[204:207]\n\t"
    "v_mfma_f32_16x16x32_bf16 a[208:211], v[196:199], a[48:51], a[208:211]\n\t"
    "v_mfma_f32_16x16x32_bf16 a[212:215], v[196:199], a[52:55], a[212:215]\n\t"
    "v_mfma_f32_16x16x32_bf16 a[216:219], v[196:199], a[56:59], a[216:219]\n\t"
    "v_mfma_f32_16x16x32_bf16 a[220:223], v[200:203], a[40:43], a[220:223]\n\t"
    "v_mfma_f32_16x16x32_bf16 a[224:227], v[200:203], a[44:47], a[224:227]\n\t"
    "v_mfma_f32_16x16x32_bf16 a[228:231], v[200:203], a[48:51], a[228:231]\n\t"
    "v_mfma_f32_16x16x32_bf16 a[232:235], v[200:203], a[52:55], a[232:235]\n\t"
    "v_mfma_f32_16x16x32_bf16 a[236:239], v[200:203], a[56:59], a[236:239]\n\t"
    "s_waitcnt vmcnt(5)\n\t"
    "v_mfma_f32_16x16x32_bf16 v[12:15], v[172:175], a[60:63], v[12:15]\n\t"
    "s_add_u32 s74, s67, 0\n\t"
    "s_add_u32 s75, s67, 0x1000\n\t"
    "v_mfma_f32_16x16x32_bf16 v[16:19], v[172:175], a[64:67], v[16:19]\n\t"
    "buffer_load_dwordx4 a[100:103], v239, s[52:55], s74 offen offset:1024 sc1\n\t"
    "v_mfma_f32_16x16x32_bf16 v[20:23], v[172:175], a[68:71], v[20:23]\n\t"
    "buffer_load_dwordx4 a[104:107], v239, s[52:55], s75 offen offset:1024 sc1\n\t"
    "v_mfma_f32_16x16x32_bf16 v[24:27], v[172:175], a[72:75], v[24:27]\n\t"
    "s_add_u32 s74, s67, 0x2000\n\t"
    "s_add_u32 s75, s67, 0x3000\n\t"
    "v_mfma_f32_16x16x32_bf16 v[28:31], v[172:175], a[76:79], v[28:31]\n\t"
    "buffer_load_dwordx4 a[108:111], v239, s[52:55], s74 offen offset:1024 sc1\n\t"
    "v_mfma_f32_16x16x32_bf16 v[32:35], v[176:179], a[60:63], v[32:35]\n\t"
    "buffer_load_dwordx4 a[112:115], v239, s[52:55], s75 offen offset:1024 sc1\n\t"
    "v_mfma_f32_16x16x32_bf16 v[36:39], v[176:179], a[64:67], v[36:39]\n\t"
    "s_add_u32 s74, s67, 0x4000\n\t"
    "ds_read_b64_tr_b16 v[172:173], v223 offset:0\n\t"
    "v_mfma_f32_16x16x32_bf16 v[40:43], v[176:179], a[68:71], v[40:43]\n\t"
    "buffer_load_dwordx4 a[116:119], v239, s[52:55], s74 offen offset:1024 sc1\n\t"
    "v_mfma_f32_16x16x32_bf16 v[44:47], v[176:179], a[72:75], v[44:47]\n\t"
    "ds_read_b64_tr_b16 v[174:175], v223 offset:4096\n\t"
    "ds_read_b64_tr_b16 v[196:197], v224 offset:1024\n\t"
    "v_mfma_f32_16x16x32_bf16 v[48:51], v[176:179], a[76:79], v[48:51]\n\t"
    "ds_read_b64_tr_b16 v[176:177], v223 offset:512\n\t"
    "s_waitcnt lgkmcnt(4)\n\t"
    "ds_read_b64_tr_b16 v[178:179], v223 offset:4608\n\t"
    "v_mfma_f32_16x16x32_bf16 v[52:55], v[180:183], a[60:63], v[52:55]\n\t"
    "ds_read_b64_tr_b16 v[198:199], v224 offset:5120\n\t"
    "ds_read_b64_tr_b16 v[200:201], v224 offset:1536\n\t"
    "v_mfma_f32_16x16x32_bf16 v[56:59], v[180:183], a[64:67], v[56:59]\n\t"
    "ds_read_b64_tr_b16 v[202:203], v224 offset:5632\n\t"
    "v_mfma_f32_16x16x32_bf16 v[60:63], v[180:183], a[68:71], v[60:63]\n\t"
    "v_mfma_f32_16x16x32_bf16 v[64:67], v[180:183], a[72:75], v[64:67]\n\t"
    "v_mfma_f32_16x16x32_bf16 v[68:71], v[180:183], a[76:79], v[68:71]\n\t"
    "ds_read_b64_tr_b16 v[180:181], v223 offset:1024\n\t"
    "ds_read_b64_tr_b16 v[182:183], v223 offset:5120\n\t"
    "v_mfma_f32_16x16x32_bf16 v[72:75], v[184:187], a[60:63], v[72:75]\n\t"
    "v_mfma_f32_16x16x32_bf16 v[76:79], v[184:187], a[64:67], v[76:79]\n\t"
    "v_mfma_f32_16x16x32_bf16 v[80:83], v[184:187], a[68:71], v[80:83]\n\t"
    "v_mfma_f32_16x16x32_bf16 v[84:87], v[184:187], a[72:75], v[84:87]\n\t"
    "v_mfma_f32_16x16x32_bf16 v[88:91], v[184:187], a[76:79], v[88:91]\n\t"
    "ds_read_b64_tr_b16 v[184:185], v223 offset:1536\n\t"
    "ds_read_b64_tr_b16 v[186:187], v223 offset:5632\n\t"
    "v_mfma_f32_16x16x32_bf16 a[160:163], v[188:191], a[60:63], a[160:163]\n\t"
    "v_mfma_f32_16x16x32_bf16 a[164:167], v[188:191], a[64:67], a[164:167]\n\t"
    "v_mfma_f32_16x16x32_bf16 a[168:171], v[188:191], a[68:71], a[168:171]\n\t"
    "v_mfma_f32_16x16x32_bf16 a[172:175], v[188:191], a[72:75], a[172:175]\n\t"
    "v_mfma_f32_16x16x32_bf16 a[176:179], v[188:191], a[76:79], a[176:179]\n\t"
    "ds_read_b64_tr_b16 v[188:189], v224 offset:0\n\t"
    "ds_read_b64_tr_b16 v[190:191], v224 offset:4096\n\t"
    "v_mfma_f32_16x16x32_bf16 a[180:183], v[192:195], a[60:63], a[180:183]\n\t"
    "v_mfma_f32_16x16x32_bf16 a[184:187], v[192:195], a[64:67], a[184:187]\n\t"
    "v_mfma_f32_16x16x32_bf16 a[188:191], v[192:195], a[68:71], a[188:191]\n\t"
    "v_mfma_f32_16x16x32_bf16 a[192:195], v[192:195], a[72:75], a[192:195]\n\t"
    "v_mfma_f32_16x16x32_bf16 a[196:199], v[192:195], a[76:79], a[196:199]\n\t"
    "ds_read_b64_tr_b16 v[192:193], v224 offset:512\n\t"
    "s_waitcnt lgkmcnt(3)\n\t"
    "ds_read_b64_tr_b16 v[194:195], v224 offset:4608\n\t"
    "v_mfma_f32_16x16x32_bf16 a[200:203], v[204:207], a[60:63], a[200:203]\n\t"
    "v_mfma_f32_16x16x32_bf16 a[204:207], v[204:207], a[64:67], a[204:207]\n\t"
    "v_mfma_f32_16x16x32_bf16 a[208:211], v[204:207], a[68:71], a[208:211]\n\t"
    "v_mfma_f32_16x16x32_bf16 a[212:215], v[204:207], a[72:75], a[212:215]\n\t"
    "v_mfma_f32_16x16x32_bf16 a[216:219], v[204:207], a[76:79], a[216:219]\n\t"
    "v_mfma_f32_16x16x32_bf16 a[220:223], v[208:211], a[60:63], a[220:223]\n\t"
    "v_mfma_f32_16x16x32_bf16 a[224:227], v[208:211], a[64:67], a[224:227]\n\t"
    "v_mfma_f32_16x16x32_bf16 a[228:231], v[208:211], a[68:71], a[228:231]\n\t"
    "v_mfma_f32_16x16x32_bf16 a[232:235], v[208:211], a[72:75], a[232:235]\n\t"
    "v_mfma_f32_16x16x32_bf16 a[236:239], v[208:211], a[76:79], a[236:239]\n\t"
    "s_waitcnt vmcnt(5)\n\t"
    "v_mfma_f32_16x16x32_bf16 v[12:15], v[172:175], a[80:83], v[12:15]\n\t"
    "s_add_u32 s74, s67, 0\n\t"
    "s_add_u32 s75, s67, 0x1000\n\t"
    "v_mfma_f32_16x16x32_bf16 v[16:19], v[172:175], a[84:87], v[16:19]\n\t"
    "buffer_load_dwordx4 a[120:123], v239, s[52:55], s74 offen offset:2048 sc1\n\t"
    "v_mfma_f32_16x16x32_bf16 v[20:23], v[172:175], a[88:91], v[20:23]\n\t"
    "buffer_load_dwordx4 a[124:127], v239, s[52:55], s75 offen offset:2048 sc1\n\t"
    "v_mfma_f32_16x16x32_bf16 v[24:27], v[172:175], a[92:95], v[24:27]\n\t"
    "s_add_u32 s74, s67, 0x2000\n\t"
    "s_add_u32 s75, s67, 0x3000\n\t"
    "v_mfma_f32_16x16x32_bf16 v[28:31], v[172:175], a[96:99], v[28:31]\n\t"
    "buffer_load_dwordx4 a[128:131], v239, s[52:55], s74 offen offset:2048 sc1\n\t"
    "v_mfma_f32_16x16x32_bf16 v[32:35], v[176:179], a[80:83], v[32:35]\n\t"
    "buffer_load_dwordx4 a[132:135], v239, s[52:55], s75 offen offset:2048 sc1\n\t"
    "v_mfma_f32_16x16x32_bf16 v[36:39], v[176:179], a[84:87], v[36:39]\n\t"
    "s_add_u32 s74, s67, 0x4000\n\t"
    "ds_read_b64_tr_b16 v[172:173], v223 offset:16384\n\t"
    "v_mfma_f32_16x16x32_bf16 v[40:43], v[176:179], a[88:91], v[40:43]\n\t"
    "buffer_load_dwordx4 a[136:139], v239, s[52:55], s74 offen offset:2048 sc1\n\t"
    "v_mfma_f32_16x16x32_bf16 v[44:47], v[176:179], a[92:95], v[44:47]\n\t"
    "ds_read_b64_tr_b16 v[174:175], v223 offset:20480\n\t"
    "ds_read_b64_tr_b16 v[204:205], v224 offset:17408\n\t"
    "v_mfma_f32_16x16x32_bf16 v[48:51], v[176:179], a[96:99], v[48:51]\n\t"
    "ds_read_b64_tr_b16 v[176:177], v223 offset:16896\n\t"
    "ds_read_b64_tr_b16 v[178:179], v223 offset:20992\n\t"
    "v_mfma_f32_16x16x32_bf16 v[52:55], v[180:183], a[80:83], v[52:55]\n\t"
    "ds_read_b64_tr_b16 v[206:207], v224 offset:21504\n\t"
    "ds_read_b64_tr_b16 v[208:209], v224 offset:17920\n\t"
    "v_mfma_f32_16x16x32_bf16 v[56:59], v[180:183], a[84:87], v[56:59]\n\t"
    "ds_read_b64_tr_b16 v[210:211], v224 offset:22016\n\t"
    "v_mfma_f32_16x16x32_bf16 v[60:63], v[180:183], a[88:91], v[60:63]\n\t"
    "v_mfma_f32_16x16x32_bf16 v[64:67], v[180:183], a[92:95], v[64:67]\n\t"
    "v_mfma_f32_16x16x32_bf16 v[68:71], v[180:183], a[96:99], v[68:71]\n\t"
    "ds_read_b64_tr_b16 v[180:181], v223 offset:17408\n\t"
    "ds_read_b64_tr_b16 v[182:183], v223 offset:21504\n\t"
    "v_mfma_f32_16x16x32_bf16 v[72:75], v[184:187], a[80:83], v[72:75]\n\t"
    "v_mfma_f32_16x16x32_bf16 v[76:79], v[184:187], a[84:87], v[76:79]\n\t"
    "v_mfma_f32_16x16x32_bf16 v[80:83], v[184:187], a[88:91], v[80:83]\n\t"
    "v_mfma_f32_16x16x32_bf16 v[84:87], v[184:187], a[92:95], v[84:87]\n\t"
    "v_mfma_f32_16x16x32_bf16 v[88:91], v[184:187], a[96:99], v[88:91]\n\t"
    "ds_read_b64_tr_b16 v[184:185], v223 offset:17920\n\t"
    "s_waitcnt lgkmcnt(6)\n\t"
    "ds_read_b64_tr_b16 v[186:187], v223 offset:22016\n\t"
    "v_mfma_f32_16x16x32_bf16 a[160:163], v[188:191], a[80:83], a[160:163]\n\t"
    "v_mfma_f32_16x16x32_bf16 a[164:167], v[188:191], a[84:87], a[164:167]\n\t"
    "v_mfma_f32_16x16x32_bf16 a[168:171], v[188:191], a[88:91], a[168:171]\n\t"
    "v_mfma_f32_16x16x32_bf16 a[172:175], v[188:191], a[92:95], a[172:175]\n\t"
    "v_mfma_f32_16x16x32_bf16 a[176:179], v[188:191], a[96:99], a[176:179]\n\t"
    "ds_read_b64_tr_b16 v[188:189], v224 offset:16384\n\t"
    "ds_read_b64_tr_b16 v[190:191], v224 offset:20480\n\t"
    "v_mfma_f32_16x16x32_bf16 a[180:183], v[192:195], a[80:83], a[180:183]\n\t"
    "v_mfma_f32_16x16x32_bf16 a[184:187], v[192:195], a[84:87], a[184:187]\n\t"
    "v_mfma_f32_16x16x32_bf16 a[188:191], v[192:195], a[88:91], a[188:191]\n\t"
    "v_mfma_f32_16x16x32_bf16 a[192:195], v[192:195], a[92:95], a[192:195]\n\t"
    "v_mfma_f32_16x16x32_bf16 a[196:199], v[192:195], a[96:99], a[196:199]\n\t"
    "ds_read_b64_tr_b16 v[192:193], v224 offset:16896\n\t"
    "ds_read_b64_tr_b16 v[194:195], v224 offset:20992\n\t"
    "v_mfma_f32_16x16x32_bf16 a[200:203], v[196:199], a[80:83], a[200:203]\n\t"
    "v_mfma_f32_16x16x32_bf16 a[204:207], v[196:199], a[84:87], a[204:207]\n\t"
    "v_mfma_f32_16x16x32_bf16 a[208:211], v[196:199], a[88:91], a[208:211]\n\t"
    "v_mfma_f32_16x16x32_bf16 a[212:215], v[196:199], a[92:95], a[212:215]\n\t"
    "v_mfma_f32_16x16x32_bf16 a[216:219], v[196:199], a[96:99], a[216:219]\n\t"
    "v_mfma_f32_16x16x32_bf16 a[220:223], v[200:203], a[80:83], a[220:223]\n\t"
    "v_mfma_f32_16x16x32_bf16 a[224:227], v[200:203], a[84:87], a[224:227]\n\t"
    "v_mfma_f32_16x16x32_bf16 a[228:231], v[200:203], a[88:91], a[228:231]\n\t"
    "v_mfma_f32_16x16x32_bf16 a[232:235], v[200:203], a[92:95], a[232:235]\n\t"
    "v_mfma_f32_16x16x32_bf16 a[236:239], v[200:203], a[96:99], a[236:239]\n\t"
    "s_waitcnt vmcnt(5)\n\t"
    "v_mfma_f32_16x16x32_bf16 v[12:15], v[172:175], a[100:103], v[12:15]\n\t"
    "s_add_u32 s74, s67, 0\n\t"
    "s_add_u32 s75, s67, 0x1000\n\t"
    "v_mfma_f32_16x16x32_bf16 v[16:19], v[172:175], a[104:107], v[16:19]\n\t"
    "buffer_load_dwordx4 a[140:143], v239, s[52:55], s74 offen offset:3072 sc1\n\t"
    "v_mfma_f32_16x16x32_bf16 v[20:23], v[172:175], a[108:111], v[20:23]\n\t"
    "buffer_load_dwordx4 a[144:147], v239, s[52:55], s75 offen offset:3072 sc1\n\t"
    "v_mfma_f32_16x16x32_bf16 v[24:27], v[172:175], a[112:115], v[24:27]\n\t"
    "s_add_u32 s74, s67, 0x2000\n\t"
    "s_add_u32 s75, s67, 0x3000\n\t"
    "v_mfma_f32_16x16x32_bf16 v[28:31], v[172:175], a[116:119], v[28:31]\n\t"
    "buffer_load_dwordx4 a[148:151], v239, s[52:55], s74 offen offset:3072 sc1\n\t"
    "v_mfma_f32_16x16x32_bf16 v[32:35], v[176:179], a[100:103], v[32:35]\n\t"
    "buffer_load_dwordx4 a[152:155], v239, s[52:55], s75 offen offset:3072 sc1\n\t"
    "v_mfma_f32_16x16x32_bf16 v[36:39], v[176:179], a[104:107], v[36:39]\n\t"
    "s_add_u32 s74, s67, 0x4000\n\t"
    "ds_read_b64_tr_b16 v[172:173], v223 offset:32768\n\t"
    "v_mfma_f32_16x16x32_bf16 v[40:43], v[176:179], a[108:111], v[40:43]\n\t"
    "buffer_load_dwordx4 a[156:159], v239, s[52:55], s74 offen offset:3072 sc1\n\t"
    "v_mfma_f32_16x16x32_bf16 v[44:47], v[176:179], a[112:115], v[44:47]\n\t"
    "ds_read_b64_tr_b16 v[174:175], v223 offset:36864\n\t"
    "ds_read_b64_tr_b16 v[196:197], v224 offset:33792\n\t"
    "v_mfma_f32_16x16x32_bf16 v[48:51], v[176:179], a[116:119], v[48:51]\n\t"
    "ds_read_b64_tr_b16 v[176:177], v223 offset:33280\n\t"
    "s_waitcnt lgkmcnt(4)\n\t"
    "ds_read_b64_tr_b16 v[178:179], v223 offset:37376\n\t"
    "v_mfma_f32_16x16x32_bf16 v[52:55], v[180:183], a[100:103], v[52:55]\n\t"
    "ds_read_b64_tr_b16 v[198:199], v224 offset:37888\n\t"
    "ds_read_b64_tr_b16 v[200:201], v224 offset:34304\n\t"
    "v_mfma_f32_16x16x32_bf16 v[56:59], v[180:183], a[104:107], v[56:59]\n\t"
    "ds_read_b64_tr_b16 v[202:203], v224 offset:38400\n\t"
    "v_mfma_f32_16x16x32_bf16 v[60:63], v[180:183], a[108:111], v[60:63]\n\t"
    "v_mfma_f32_16x16x32_bf16 v[64:67], v[180:183], a[112:115], v[64:67]\n\t"
    "v_mfma_f32_16x16x32_bf16 v[68:71], v[180:183], a[116:119], v[68:71]\n\t"
    "ds_read_b64_tr_b16 v[180:181], v223 offset:33792\n\t"
    "ds_read_b64_tr_b16 v[182:183], v223 offset:37888\n\t"
    "v_mfma_f32_16x16x32_bf16 v[72:75], v[184:187], a[100:103], v[72:75]\n\t"
    "v_mfma_f32_16x16x32_bf16 v[76:79], v[184:187], a[104:107], v[76:79]\n\t"
    "v_mfma_f32_16x16x32_bf16 v[80:83], v[184:187], a[108:111], v[80:83]\n\t"
    "v_mfma_f32_16x16x32_bf16 v[84:87], v[184:187], a[112:115], v[84:87]\n\t"
    "v_mfma_f32_16x16x32_bf16 v[88:91], v[184:187], a[116:119], v[88:91]\n\t"
    "ds_read_b64_tr_b16 v[184:185], v223 offset:34304\n\t"
    "ds_read_b64_tr_b16 v[186:187], v223 offset:38400\n\t"
    "v_mfma_f32_16x16x32_bf16 a[160:163], v[188:191], a[100:103], a[160:163]\n\t"
    "v_mfma_f32_16x16x32_bf16 a[164:167], v[188:191], a[104:107], a[164:167]\n\t"
    "v_mfma_f32_16x16x32_bf16 a[168:171], v[188:191], a[108:111], a[168:171]\n\t"
    "v_mfma_f32_16x16x32_bf16 a[172:175], v[188:191], a[112:115], a[172:175]\n\t"
    "v_mfma_f32_16x16x32_bf16 a[176:179], v[188:191], a[116:119], a[176:179]\n\t"
    "ds_read_b64_tr_b16 v[188:189], v224 offset:32768\n\t"
    "ds_read_b64_tr_b16 v[190:191], v224 offset:36864\n\t"
    "v_mfma_f32_16x16x32_bf16 a[180:183], v[192:195], a[100:103], a[180:183]\n\t"
    "v_mfma_f32_16x16x32_bf16 a[184:187], v[192:195], a[104:107], a[184:187]\n\t"
    "v_mfma_f32_16x16x32_bf16 a[188:191], v[192:195], a[108:111], a[188:191]\n\t"
    "v_mfma_f32_16x16x32_bf16 a[192:195], v[192:195], a[112:115], a[192:195]\n\t"
    "v_mfma_f32_16x16x32_bf16 a[196:199], v[192:195], a[116:119], a[196:199]\n\t"
    "ds_read_b64_tr_b16 v[192:193], v224 offset:33280\n\t"
    "s_waitcnt lgkmcnt(3)\n\t"
    "ds_read_b64_tr_b16 v[194:195], v224 offset:37376\n\t"
    "v_mfma_f32_16x16x32_bf16 a[200:203], v[204:207], a[100:103], a[200:203]\n\t"
    "v_mfma_f32_16x16x32_bf16 a[204:207], v[204:207], a[104:107], a[204:207]\n\t"
    "v_mfma_f32_16x16x32_bf16 a[208:211], v[204:207], a[108:111], a[208:211]\n\t"
    "v_mfma_f32_16x16x32_bf16 a[212:215], v[204:207], a[112:115], a[212:215]\n\t"
    "v_mfma_f32_16x16x32_bf16 a[216:219], v[204:207], a[116:119], a[216:219]\n\t"
    "v_mfma_f32_16x16x32_bf16 a[220:223], v[208:211], a[100:103], a[220:223]\n\t"
    "v_mfma_f32_16x16x32_bf16 a[224:227], v[208:211], a[104:107], a[224:227]\n\t"
    "v_mfma_f32_16x16x32_bf16 a[228:231], v[208:211], a[108:111], a[228:231]\n\t"
    "v_mfma_f32_16x16x32_bf16 a[232:235], v[208:211], a[112:115], a[232:235]\n\t"
    "v_mfma_f32_16x16x32_bf16 a[236:239], v[208:211], a[116:119], a[236:239]\n\t"
    "s_waitcnt vmcnt(5)\n\t"
    "v_mfma_f32_16x16x32_bf16 v[12:15], v[172:175], a[120:123], v[12:15]\n\t"
    "s_add_u32 s74, s68, 0\n\t"
    "s_add_u32 s75, s68, 0x1000\n\t"
    "v_mfma_f32_16x16x32_bf16 v[16:19], v[172:175], a[124:127], v[16:19]\n\t"
    "buffer_load_dwordx4 v[92:95], v239, s[52:55], s74 offen sc1\n\t"
    "v_mfma_f32_16x16x32_bf16 v[20:23], v[172:175], a[128:131], v[20:23]\n\t"
    "buffer_load_dwordx4 v[96:99], v239, s[52:55], s75 offen sc1\n\t"
    "v_mfma_f32_16x16x32_bf16 v[24:27], v[172:175], a[132:135], v[24:27]\n\t"
    "s_add_u32 s74, s68, 0x2000\n\t"
    "s_add_u32 s75, s68, 0x3000\n\t"
    "v_mfma_f32_16x16x32_bf16 v[28:31], v[172:175], a[136:139], v[28:31]\n\t"
    "buffer_load_dwordx4 v[100:103], v239, s[52:55], s74 offen sc1\n\t"
    "v_mfma_f32_16x16x32_bf16 v[32:35], v[176:179], a[120:123], v[32:35]\n\t"
    "buffer_load_dwordx4 v[104:107], v239, s[52:55], s75 offen sc1\n\t"
    "v_mfma_f32_16x16x32_bf16 v[36:39], v[176:179], a[124:127], v[36:39]\n\t"
    "s_add_u32 s74, s68, 0x4000\n\t"
    "ds_read_b64_tr_b16 v[172:173], v223 offset:49152\n\t"
    "v_mfma_f32_16x16x32_bf16 v[40:43], v[176:179], a[128:131], v[40:43]\n\t"
    "buffer_load_dwordx4 v[108:111], v239, s[52:55], s74 offen sc1\n\t"
    "v_mfma_f32_16x16x32_bf16 v[44:47], v[176:179], a[132:135], v[44:47]\n\t"
    "ds_read_b64_tr_b16 v[174:175], v223 offset:53248\n\t"
    "ds_read_b64_tr_b16 v[204:205], v224 offset:50176\n\t"
    "v_mfma_f32_16x16x32_bf16 v[48:51], v[176:179], a[136:139], v[48:51]\n\t"
    "ds_read_b64_tr_b16 v[176:177], v223 offset:49664\n\t"
    "ds_read_b64_tr_b16 v[178:179], v223 offset:53760\n\t"
    "v_mfma_f32_16x16x32_bf16 v[52:55], v[180:183], a[120:123], v[52:55]\n\t"
    "ds_read_b64_tr_b16 v[206:207], v224 offset:54272\n\t"
    "ds_read_b64_tr_b16 v[208:209], v224 offset:50688\n\t"
    "v_mfma_f32_16x16x32_bf16 v[56:59], v[180:183], a[124:127], v[56:59]\n\t"
    "ds_read_b64_tr_b16 v[210:211], v224 offset:54784\n\t"
    "v_mfma_f32_16x16x32_bf16 v[60:63], v[180:183], a[128:131], v[60:63]\n\t"
    "v_mfma_f32_16x16x32_bf16 v[64:67], v[180:183], a[132:135], v[64:67]\n\t"
    "v_mfma_f32_16x16x32_bf16 v[68:71], v[180:183], a[136:139], v[68:71]\n\t"
    "ds_read_b64_tr_b16 v[180:181], v223 offset:50176\n\t"
    "ds_read_b64_tr_b16 v[182:183], v223 offset:54272\n\t"
    "v_mfma_f32_16x16x32_bf16 v[72:75], v[184:187], a[120:123], v[72:75]\n\t"
    "v_mfma_f32_16x16x32_bf16 v[76:79], v[184:187], a[124:127], v[76:79]\n\t"
    "v_mfma_f32_16x16x32_bf16 v[80:83], v[184:187], a[128:131], v[80:83]\n\t"
    "v_mfma_f32_16x16x32_bf16 v[84:87], v[184:187], a[132:135], v[84:87]\n\t"
    "v_mfma_f32_16x16x32_bf16 v[88:91], v[184:187], a[136:139], v[88:91]\n\t"
    "ds_read_b64_tr_b16 v[184:185], v223 offset:50688\n\t"
    "s_waitcnt lgkmcnt(6)\n\t"
    "ds_read_b64_tr_b16 v[186:187], v223 offset:54784\n\t"
    "v_mfma_f32_16x16x32_bf16 a[160:163], v[188:191], a[120:123], a[160:163]\n\t"
    "v_mfma_f32_16x16x32_bf16 a[164:167], v[188:191], a[124:127], a[164:167]\n\t"
    "v_mfma_f32_16x16x32_bf16 a[168:171], v[188:191], a[128:131], a[168:171]\n\t"
    "v_mfma_f32_16x16x32_bf16 a[172:175], v[188:191], a[132:135], a[172:175]\n\t"
    "v_mfma_f32_16x16x32_bf16 a[176:179], v[188:191], a[136:139], a[176:179]\n\t"
    "ds_read_b64_tr_b16 v[188:189], v224 offset:49152\n\t"
    "ds_read_b64_tr_b16 v[190:191], v224 offset:53248\n\t"
    "v_mfma_f32_16x16x32_bf16 a[180:183], v[192:195], a[120:123], a[180:183]\n\t"
    "v_mfma_f32_16x16x32_bf16 a[184:187], v[192:195], a[124:127], a[184:187]\n\t"
    "v_mfma_f32_16x16x32_bf16 a[188:191], v[192:195], a[128:131], a[188:191]\n\t"
    "v_mfma_f32_16x16x32_bf16 a[192:195], v[192:195], a[132:135], a[192:195]\n\t"
    "v_mfma_f32_16x16x32_bf16 a[196:199], v[192:195], a[136:139], a[196:199]\n\t"
    "ds_read_b64_tr_b16 v[192:193], v224 offset:49664\n\t"
    "ds_read_b64_tr_b16 v[194:195], v224 offset:53760\n\t"
    "v_mfma_f32_16x16x32_bf16 a[200:203], v[196:199], a[120:123], a[200:203]\n\t"
    "v_mfma_f32_16x16x32_bf16 a[204:207], v[196:199], a[124:127], a[204:207]\n\t"
    "v_mfma_f32_16x16x32_bf16 a[208:211], v[196:199], a[128:131], a[208:211]\n\t"
    "v_mfma_f32_16x16x32_bf16 a[212:215], v[196:199], a[132:135], a[212:215]\n\t"
    "v_mfma_f32_16x16x32_bf16 a[216:219], v[196:199], a[136:139], a[216:219]\n\t"
    "v_mfma_f32_16x16x32_bf16 a[220:223], v[200:203], a[120:123], a[220:223]\n\t"
    "v_mfma_f32_16x16x32_bf16 a[224:227], v[200:203], a[124:127], a[224:227]\n\t"
    "v_mfma_f32_16x16x32_bf16 a[228:231], v[200:203], a[128:131], a[228:231]\n\t"
    "v_mfma_f32_16x16x32_bf16 a[232:235], v[200:203], a[132:135], a[232:235]\n\t"
    "v_mfma_f32_16x16x32_bf16 a[236:239], v[200:203], a[136:139], a[236:239]\n\t"
    "s_waitcnt vmcnt(5)\n\t"
    "v_mfma_f32_16x16x32_bf16 v[12:15], v[172:175], a[140:143], v[12:15]\n\t"
    "s_add_u32 s74, s68, 0\n\t"
    "s_add_u32 s75, s68, 0x1000\n\t"
    "v_mfma_f32_16x16x32_bf16 v[16:19], v[172:175], a[144:147], v[16:19]\n\t"
    "buffer_load_dwordx4 v[112:115], v239, s[52:55], s74 offen offset:1024 sc1\n\t"
    "v_mfma_f32_16x16x32_bf16 v[20:23], v[172:175], a[148:151], v[20:23]\n\t"
    "buffer_load_dwordx4 v[116:119], v239, s[52:55], s75 offen offset:1024 sc1\n\t"
    "v_mfma_f32_16x16x32_bf16 v[24:27], v[172:175], a[152:155], v[24:27]\n\t"
    "s_add_u32 s74, s68, 0x2000\n\t"
    "s_add_u32 s75, s68, 0x3000\n\t"
    "v_mfma_f32_16x16x32_bf16 v[28:31], v[172:175], a[156:159], v[28:31]\n\t"
    "buffer_load_dwordx4 v[120:123], v239, s[52:55], s74 offen offset:1024 sc1\n\t"
    "v_mfma_f32_16x16x32_bf16 v[32:35], v[176:179], a[140:143], v[32:35]\n\t"
    "buffer_load_dwordx4 v[124:127], v239, s[52:55], s75 offen offset:1024 sc1\n\t"
    "v_mfma_f32_16x16x32_bf16 v[36:39], v[176:179], a[144:147], v[36:39]\n\t"
    "s_add_u32 s74, s68, 0x4000\n\t"
    "ds_read_b64_tr_b16 v[172:173], v225 offset:0\n\t"
    "v_mfma_f32_16x16x32_bf16 v[40:43], v[176:179], a[148:151], v[40:43]\n\t"
    "buffer_load_dwordx4 v[128:131], v239, s[52:55], s74 offen offset:1024 sc1\n\t"
    "v_mfma_f32_16x16x32_bf16 v[44:47], v[176:179], a[152:155], v[44:47]\n\t"
    "ds_read_b64_tr_b16 v[174:175], v225 offset:4096\n\t"
    "ds_read_b64_tr_b16 v[196:197], v226 offset:1024\n\t"
    "v_mfma_f32_16x16x32_bf16 v[48:51], v[176:179], a[156:159], v[48:51]\n\t"
    "ds_read_b64_tr_b16 v[176:177], v225 offset:512\n\t"
    "s_waitcnt lgkmcnt(4)\n\t"
    "ds_read_b64_tr_b16 v[178:179], v225 offset:4608\n\t"
    "v_mfma_f32_16x16x32_bf16 v[52:55], v[180:183], a[140:143], v[52:55]\n\t"
    "ds_read_b64_tr_b16 v[198:199], v226 offset:5120\n\t"
    "ds_read_b64_tr_b16 v[200:201], v226 offset:1536\n\t"
    "v_mfma_f32_16x16x32_bf16 v[56:59], v[180:183], a[144:147], v[56:59]\n\t"
    "ds_read_b64_tr_b16 v[202:203], v226 offset:5632\n\t"
    "v_mfma_f32_16x16x32_bf16 v[60:63], v[180:183], a[148:151], v[60:63]\n\t"
    "v_mfma_f32_16x16x32_bf16 v[64:67], v[180:183], a[152:155], v[64:67]\n\t"
    "v_mfma_f32_16x16x32_bf16 v[68:71], v[180:183], a[156:159], v[68:71]\n\t"
    "ds_read_b64_tr_b16 v[180:181], v225 offset:1024\n\t"
    "ds_read_b64_tr_b16 v[182:183], v225 offset:5120\n\t"
    "v_mfma_f32_16x16x32_bf16 v[72:75], v[184:187], a[140:143], v[72:75]\n\t"
    "v_mfma_f32_16x16x32_bf16 v[76:79], v[184:187], a[144:147], v[76:79]\n\t"
    "v_mfma_f32_16x16x32_bf16 v[80:83], v[184:187], a[148:151], v[80:83]\n\t"
    "v_mfma_f32_16x16x32_bf16 v[84:87], v[184:187], a[152:155], v[84:87]\n\t"
    "v_mfma_f32_16x16x32_bf16 v[88:91], v[184:187], a[156:159], v[88:91]\n\t"
    "ds_read_b64_tr_b16 v[184:185], v225 offset:1536\n\t"
    "ds_read_b64_tr_b16 v[186:187], v225 offset:5632\n\t"
    "v_mfma_f32_16x16x32_bf16 a[160:163], v[188:191], a[140:143], a[160:163]\n\t"
    "v_mfma_f32_16x16x32_bf16 a[164:167], v[188:191], a[144:147], a[164:167]\n\t"
    "v_mfma_f32_16x16x32_bf16 a[168:171], v[188:191], a[148:151], a[168:171]\n\t"
    "v_mfma_f32_16x16x32_bf16 a[172:175], v[188:191], a[152:155], a[172:175]\n\t"
    "v_mfma_f32_16x16x32_bf16 a[176:179], v[188:191], a[156:159], a[176:179]\n\t"
    "ds_read_b64_tr_b16 v[188:189], v226 offset:0\n\t"
    "ds_read_b64_tr_b16 v[190:191], v226 offset:4096\n\t"
    "v_mfma_f32_16x16x32_bf16 a[180:183], v[192:195], a[140:143], a[180:183]\n\t"
    "v_mfma_f32_16x16x32_bf16 a[184:187], v[192:195], a[144:147], a[184:187]\n\t"
    "v_mfma_f32_16x16x32_bf16 a[188:191], v[192:195], a[148:151], a[188:191]\n\t"
    "v_mfma_f32_16x16x32_bf16 a[192:195], v[192:195], a[152:155], a[192:195]\n\t"
    "v_mfma_f32_16x16x32_bf16 a[196:199], v[192:195], a[156:159], a[196:199]\n\t"
    "ds_read_b64_tr_b16 v[192:193], v226 offset:512\n\t"
    "s_waitcnt lgkmcnt(3)\n\t"
    "ds_read_b64_tr_b16 v[194:195], v226 offset:4608\n\t"
    "v_mfma_f32_16x16x32_bf16 a[200:203], v[204:207], a[140:143], a[200:203]\n\t"
    "v_mfma_f32_16x16x32_bf16 a[204:207], v[204:207], a[144:147], a[204:207]\n\t"
    "v_mfma_f32_16x16x32_bf16 a[208:211], v[204:207], a[148:151], a[208:211]\n\t"
    "v_mfma_f32_16x16x32_bf16 a[212:215], v[204:207], a[152:155], a[212:215]\n\t"
    "v_mfma_f32_16x16x32_bf16 a[216:219], v[204:207], a[156:159], a[216:219]\n\t"
    "v_mfma_f32_16x16x32_bf16 a[220:223], v[208:211], a[140:143], a[220:223]\n\t"
    "v_mfma_f32_16x16x32_bf16 a[224:227], v[208:211], a[144:147], a[224:227]\n\t"
    "v_mfma_f32_16x16x32_bf16 a[228:231], v[208:211], a[148:151], a[228:231]\n\t"
    "v_mfma_f32_16x16x32_bf16 a[232:235], v[208:211], a[152:155], a[232:235]\n\t"
    "v_mfma_f32_16x16x32_bf16 a[236:239], v[208:211], a[156:159], a[236:239]\n\t"
    "s_waitcnt vmcnt(5)\n\t"
    "v_mfma_f32_16x16x32_bf16 v[12:15], v[172:175], v[92:95], v[12:15]\n\t"
    "s_add_u32 s74, s68, 0\n\t"
    "s_add_u32 s75, s68, 0x1000\n\t"
    "v_mfma_f32_16x16x32_bf16 v[16:19], v[172:175], v[96:99], v[16:19]\n\t"
    "buffer_load_dwordx4 v[132:135], v239, s[52:55], s74 offen offset:2048 sc1\n\t"
    "v_mfma_f32_16x16x32_bf16 v[20:23], v[172:175], v[100:103], v[20:23]\n\t"
    "buffer_load_dwordx4 v[136:139], v239, s[52:55], s75 offen offset:2048 sc1\n\t"
    "v_mfma_f32_16x16x32_bf16 v[24:27], v[172:175], v[104:107], v[24:27]\n\t"
    "s_add_u32 s74, s68, 0x2000\n\t"
    "s_add_u32 s75, s68, 0x3000\n\t"
    "v_mfma_f32_16x16x32_bf16 v[28:31], v[172:175], v[108:111], v[28:31]\n\t"
    "buffer_load_dwordx4 v[140:143], v239, s[52:55], s74 offen offset:2048 sc1\n\t"
    "v_mfma_f32_16x16x32_bf16 v[32:35], v[176:179], v[92:95], v[32:35]\n\t"
    "buffer_load_dwordx4 v[144:147], v239, s[52:55], s75 offen offset:2048 sc1\n\t"
    "v_mfma_f32_16x16x32_bf16 v[36:39], v[176:179], v[96:99], v[36:39]\n\t"
    "s_add_u32 s74, s68, 0x4000\n\t"
    "ds_read_b64_tr_b16 v[172:173], v225 offset:16384\n\t"
    "v_mfma_f32_16x16x32_bf16 v[40:43], v[176:179], v[100:103], v[40:43]\n\t"
    "buffer_load_dwordx4 v[148:151], v239, s[52:55], s74 offen offset:2048 sc1\n\t"
    "v_mfma_f32_16x16x32_bf16 v[44:47], v[176:179], v[104:107], v[44:47]\n\t"
    "ds_read_b64_tr_b16 v[174:175], v225 offset:20480\n\t"
    "ds_read_b64_tr_b16 v[204:205], v226 offset:17408\n\t"
    "v_mfma_f32_16x16x32_bf16 v[48:51], v[176:179], v[108:111], v[48:51]\n\t"
    "ds_read_b64_tr_b16 v[176:177], v225 offset:16896\n\t"
    "ds_read_b64_tr_b16 v[178:179], v225 offset:20992\n\t"
    "v_mfma_f32_16x16x32_bf16 v[52:55], v[180:183], v[92:95], v[52:55]\n\t"
    "ds_read_b64_tr_b16 v[206:207], v226 offset:21504\n\t"
    "ds_read_b64_tr_b16 v[208:209], v226 offset:17920\n\t"
    "v_mfma_f32_16x16x32_bf16 v[56:59], v[180:183], v[96:99], v[56:59]\n\t"
    "ds_read_b64_tr_b16 v[210:211], v226 offset:22016\n\t"
    "v_mfma_f32_16x16x32_bf16 v[60:63], v[180:183], v[100:103], v[60:63]\n\t"
    "v_mfma_f32_16x16x32_bf16 v[64:67], v[180:183], v[104:107], v[64:67]\n\t"
    "v_mfma_f32_16x16x32_bf16 v[68:71], v[180:183], v[108:111], v[68:71]\n\t"
    "ds_read_b64_tr_b16 v[180:181], v225 offset:17408\n\t"
    "ds_read_b64_tr_b16 v[182:183], v225 offset:21504\n\t"
    "v_mfma_f32_16x16x32_bf16 v[72:75], v[184:187], v[92:95], v[72:75]\n\t"
    "v_mfma_f32_16x16x32_bf16 v[76:79], v[184:187], v[96:99], v[76:79]\n\t"
    "v_mfma_f32_16x16x32_bf16 v[80:83], v[184:187], v[100:103], v[80:83]\n\t"
    "v_mfma_f32_16x16x32_bf16 v[84:87], v[184:187], v[104:107], v[84:87]\n\t"
    "v_mfma_f32_16x16x32_bf16 v[88:91], v[184:187], v[108:111], v[88:91]\n\t"
    "ds_read_b64_tr_b16 v[184:185], v225 offset:17920\n\t"
    "s_waitcnt lgkmcnt(6)\n\t"
    "ds_read_b64_tr_b16 v[186:187], v225 offset:22016\n\t"
    "v_mfma_f32_16x16x32_bf16 a[160:163], v[188:191], v[92:95], a[160:163]\n\t"
    "v_mfma_f32_16x16x32_bf16 a[164:167], v[188:191], v[96:99], a[164:167]\n\t"
    "v_mfma_f32_16x16x32_bf16 a[168:171], v[188:191], v[100:103], a[168:171]\n\t"
    "v_mfma_f32_16x16x32_bf16 a[172:175], v[188:191], v[104:107], a[172:175]\n\t"
    "v_mfma_f32_16x16x32_bf16 a[176:179], v[188:191], v[108:111], a[176:179]\n\t"
    "ds_read_b64_tr_b16 v[188:189], v226 offset:16384\n\t"
    "ds_read_b64_tr_b16 v[190:191], v226 offset:20480\n\t"
    "v_mfma_f32_16x16x32_bf16 a[180:183], v[192:195], v[92:95], a[180:183]\n\t"
    "v_mfma_f32_16x16x32_bf16 a[184:187], v[192:195], v[96:99], a[184:187]\n\t"
    "v_mfma_f32_16x16x32_bf16 a[188:191], v[192:195], v[100:103], a[188:191]\n\t"
    "v_mfma_f32_16x16x32_bf16 a[192:195], v[192:195], v[104:107], a[192:195]\n\t"
    "v_mfma_f32_16x16x32_bf16 a[196:199], v[192:195], v[108:111], a[196:199]\n\t"
    "ds_read_b64_tr_b16 v[192:193], v226 offset:16896\n\t"
    "ds_read_b64_tr_b16 v[194:195], v226 offset:20992\n\t"
    "v_mfma_f32_16x16x32_bf16 a[200:203], v[196:199], v[92:95], a[200:203]\n\t"
    "v_mfma_f32_16x16x32_bf16 a[204:207], v[196:199], v[96:99], a[204:207]\n\t"
    "v_mfma_f32_16x16x32_bf16 a[208:211], v[196:199], v[100:103], a[208:211]\n\t"
    "v_mfma_f32_16x16x32_bf16 a[212:215], v[196:199], v[104:107], a[212:215]\n\t"
    "v_mfma_f32_16x16x32_bf16 a[216:219], v[196:199], v[108:111], a[216:219]\n\t"
    "v_mfma_f32_16x16x32_bf16 a[220:223], v[200:203], v[92:95], a[220:223]\n\t"
    "v_mfma_f32_16x16x32_bf16 a[224:227], v[200:203], v[96:99], a[224:227]\n\t"
    "v_mfma_f32_16x16x32_bf16 a[228:231], v[200:203], v[100:103], a[228:231]\n\t"
    "v_mfma_f32_16x16x32_bf16 a[232:235], v[200:203], v[104:107], a[232:235]\n\t"
    "v_mfma_f32_16x16x32_bf16 a[236:239], v[200:203], v[108:111], a[236:239]\n\t"
    "s_waitcnt vmcnt(5) lgkmcnt(0)\n\t"
    "s_barrier\n\t"
    "v_mfma_f32_16x16x32_bf16 v[12:15], v[172:175], v[112:115], v[12:15]\n\t"
    "s_add_u32 s74, s68, 0\n\t"
    "s_add_u32 s75, s68, 0x1000\n\t"
    "v_mfma_f32_16x16x32_bf16 v[16:19], v[172:175], v[116:119], v[16:19]\n\t"
    "buffer_load_dwordx4 v[152:155], v239, s[52:55], s74 offen offset:3072 sc1\n\t"
    "v_mfma_f32_16x16x32_bf16 v[20:23], v[172:175], v[120:123], v[20:23]\n\t"
    "buffer_load_dwordx4 v[156:159], v239, s[52:55], s75 offen offset:3072 sc1\n\t"
    "v_mfma_f32_16x16x32_bf16 v[24:27], v[172:175], v[124:127], v[24:27]\n\t"
    "s_add_u32 s74, s68, 0x2000\n\t"
    "s_add_u32 s75, s68, 0x3000\n\t"
    "v_mfma_f32_16x16x32_bf16 v[28:31], v[172:175], v[128:131], v[28:31]\n\t"
    "buffer_load_dwordx4 v[160:163], v239, s[52:55], s74 offen offset:3072 sc1\n\t"
    "v_mfma_f32_16x16x32_bf16 v[32:35], v[176:179], v[112:115], v[32:35]\n\t"
    "buffer_load_dwordx4 v[164:167], v239, s[52:55], s75 offen offset:3072 sc1\n\t"
    "v_mfma_f32_16x16x32_bf16 v[36:39], v[176:179], v[116:119], v[36:39]\n\t"
    "s_add_u32 s74, s68, 0x4000\n\t"
    "ds_read_b64_tr_b16 v[172:173], v221 offset:0\n\t"
    "v_mfma_f32_16x16x32_bf16 v[40:43], v[176:179], v[120:123], v[40:43]\n\t"
    "buffer_load_dwordx4 v[168:171], v239, s[52:55], s74 offen offset:3072 sc1\n\t"
    "v_mfma_f32_16x16x32_bf16 v[44:47], v[176:179], v[124:127], v[44:47]\n\t"
    "ds_read_b64_tr_b16 v[174:175], v221 offset:4096\n\t"
    "ds_read_b64_tr_b16 v[196:197], v222 offset:1024\n\t"
    "v_mfma_f32_16x16x32_bf16 v[48:51], v[176:179], v[128:131], v[48:51]\n\t"
    "ds_read_b64_tr_b16 v[176:177], v221 offset:512\n\t"
    "ds_read_b64_tr_b16 v[178:179], v221 offset:4608\n\t"
    "v_mfma_f32_16x16x32_bf16 v[52:55], v[180:183], v[112:115], v[52:55]\n\t"
    "ds_read_b64_tr_b16 v[198:199], v222 offset:5120\n\t"
    "ds_read_b64_tr_b16 v[200:201], v222 offset:1536\n\t"
    "v_mfma_f32_16x16x32_bf16 v[56:59], v[180:183], v[116:119], v[56:59]\n\t"
    "ds_read_b64_tr_b16 v[202:203], v222 offset:5632\n\t"
    "v_mfma_f32_16x16x32_bf16 v[60:63], v[180:183], v[120:123], v[60:63]\n\t"
    "v_mfma_f32_16x16x32_bf16 v[64:67], v[180:183], v[124:127], v[64:67]\n\t"
    "v_mfma_f32_16x16x32_bf16 v[68:71], v[180:183], v[128:131], v[68:71]\n\t"
    "ds_read_b64_tr_b16 v[180:181], v221 offset:1024\n\t"
    "ds_read_b64_tr_b16 v[182:183], v221 offset:5120\n\t"
    "v_mfma_f32_16x16x32_bf16 v[72:75], v[184:187], v[112:115], v[72:75]\n\t"
    "v_mfma_f32_16x16x32_bf16 v[76:79], v[184:187], v[116:119], v[76:79]\n\t"
    "v_mfma_f32_16x16x32_bf16 v[80:83], v[184:187], v[120:123], v[80:83]\n\t"
    "v_mfma_f32_16x16x32_bf16 v[84:87], v[184:187], v[124:127], v[84:87]\n\t"
    "v_mfma_f32_16x16x32_bf16 v[88:91], v[184:187], v[128:131], v[88:91]\n\t"
    "ds_read_b64_tr_b16 v[184:185], v221 offset:1536\n\t"
    "ds_read_b64_tr_b16 v[186:187], v221 offset:5632\n\t"
    "v_mfma_f32_16x16x32_bf16 a[160:163], v[188:191], v[112:115], a[160:163]\n\t"
    "v_mfma_f32_16x16x32_bf16 a[164:167], v[188:191], v[116:119], a[164:167]\n\t"
    "v_mfma_f32_16x16x32_bf16 a[168:171], v[188:191], v[120:123], a[168:171]\n\t"
    "v_mfma_f32_16x16x32_bf16 a[172:175], v[188:191], v[124:127], a[172:175]\n\t"
    "v_mfma_f32_16x16x32_bf16 a[176:179], v[188:191], v[128:131], a[176:179]\n\t"
    "ds_read_b64_tr_b16 v[188:189], v222 offset:0\n\t"
    "ds_read_b64_tr_b16 v[190:191], v222 offset:4096\n\t"
    "v_mfma_f32_16x16x32_bf16 a[180:183], v[192:195], v[112:115], a[180:183]\n\t"
    "v_mfma_f32_16x16x32_bf16 a[184:187], v[192:195], v[116:119], a[184:187]\n\t"
    "v_mfma_f32_16x16x32_bf16 a[188:191], v[192:195], v[120:123], a[188:191]\n\t"
    "v_mfma_f32_16x16x32_bf16 a[192:195], v[192:195], v[124:127], a[192:195]\n\t"
    "v_mfma_f32_16x16x32_bf16 a[196:199], v[192:195], v[128:131], a[196:199]\n\t"
    "ds_read_b64_tr_b16 v[192:193], v222 offset:512\n\t"
    "s_waitcnt lgkmcnt(3)\n\t"
    "ds_read_b64_tr_b16 v[194:195], v222 offset:4608\n\t"
    "v_mfma_f32_16x16x32_bf16 a[200:203], v[204:207], v[112:115], a[200:203]\n\t"
    "v_mfma_f32_16x16x32_bf16 a[204:207], v[204:207], v[116:119], a[204:207]\n\t"
    "v_mfma_f32_16x16x32_bf16 a[208:211], v[204:207], v[120:123], a[208:211]\n\t"
    "v_mfma_f32_16x16x32_bf16 a[212:215], v[204:207], v[124:127], a[212:215]\n\t"
    "v_mfma_f32_16x16x32_bf16 a[216:219], v[204:207], v[128:131], a[216:219]\n\t"
    "v_mfma_f32_16x16x32_bf16 a[220:223], v[208:211], v[112:115], a[220:223]\n\t"
    "v_mfma_f32_16x16x32_bf16 a[224:227], v[208:211], v[116:119], a[224:227]\n\t"
    "v_mfma_f32_16x16x32_bf16 a[228:231], v[208:211], v[120:123], a[228:231]\n\t"
    "v_mfma_f32_16x16x32_bf16 a[232:235], v[208:211], v[124:127], a[232:235]\n\t"
    "v_mfma_f32_16x16x32_bf16 a[236:239], v[208:211], v[128:131], a[236:239]\n\t"
    "s_waitcnt vmcnt(5) lgkmcnt(0)\n\t"
    "s_barrier\n\t"
    "v_mfma_f32_16x16x32_bf16 v[12:15], v[172:175], v[132:135], v[12:15]\n\t"
    "ds_read_b64_tr_b16 v[204:205], v222 offset:17408\n\t"
    "ds_read_b64_tr_b16 v[206:207], v222 offset:21504\n\t"
    "v_mfma_f32_16x16x32_bf16 v[16:19], v[172:175], v[136:139], v[16:19]\n\t"
    "ds_read_b64_tr_b16 v[208:209], v222 offset:17920\n\t"
    "ds_read_b64_tr_b16 v[210:211], v222 offset:22016\n\t"
    "v_mfma_f32_16x16x32_bf16 v[20:23], v[172:175], v[140:143], v[20:23]\n\t"
    "v_mfma_f32_16x16x32_bf16 v[24:27], v[172:175], v[144:147], v[24:27]\n\t"
    "v_mfma_f32_16x16x32_bf16 v[28:31], v[172:175], v[148:151], v[28:31]\n\t"
    "ds_read_b64_tr_b16 v[172:173], v221 offset:16384\n\t"
    "ds_read_b64_tr_b16 v[174:175], v221 offset:20480\n\t"
    "v_mfma_f32_16x16x32_bf16 v[32:35], v[176:179], v[132:135], v[32:35]\n\t"
    "v_mfma_f32_16x16x32_bf16 v[36:39], v[176:179], v[136:139], v[36:39]\n\t"
    "v_mfma_f32_16x16x32_bf16 v[40:43], v[176:179], v[140:143], v[40:43]\n\t"
    "v_mfma_f32_16x16x32_bf16 v[44:47], v[176:179], v[144:147], v[44:47]\n\t"
    "v_mfma_f32_16x16x32_bf16 v[48:51], v[176:179], v[148:151], v[48:51]\n\t"
    "ds_read_b64_tr_b16 v[176:177], v221 offset:16896\n\t"
    "ds_read_b64_tr_b16 v[178:179], v221 offset:20992\n\t"
    "v_mfma_f32_16x16x32_bf16 v[52:55], v[180:183], v[132:135], v[52:55]\n\t"
    "v_mfma_f32_16x16x32_bf16 v[56:59], v[180:183], v[136:139], v[56:59]\n\t"
    "v_mfma_f32_16x16x32_bf16 v[60:63], v[180:183], v[140:143], v[60:63]\n\t"
    "v_mfma_f32_16x16x32_bf16 v[64:67], v[180:183], v[144:147], v[64:67]\n\t"
    "v_mfma_f32_16x16x32_bf16 v[68:71], v[180:183], v[148:151], v[68:71]\n\t"
    "ds_read_b64_tr_b16 v[180:181], v221 offset:17408\n\t"
    "ds_read_b64_tr_b16 v[182:183], v221 offset:21504\n\t"
    "v_mfma_f32_16x16x32_bf16 v[72:75], v[184:187], v[132:135], v[72:75]\n\t"
    "v_mfma_f32_16x16x32_bf16 v[76:79], v[184:187], v[136:139], v[76:79]\n\t"
    "v_mfma_f32_16x16x32_bf16 v[80:83], v[184:187], v[140:143], v[80:83]\n\t"
    "v_mfma_f32_16x16x32_bf16 v[84:87], v[184:187], v[144:147], v[84:87]\n\t"
    "v_mfma_f32_16x16x32_bf16 v[88:91], v[184:187], v[148:151], v[88:91]\n\t"
    "ds_read_b64_tr_b16 v[184:185], v221 offset:17920\n\t"
    "ds_read_b64_tr_b16 v[186:187], v221 offset:22016\n\t"
    "v_mfma_f32_16x16x32_bf16 a[160:163], v[188:191], v[132:135], a[160:163]\n\t"
    "v_mfma_f32_16x16x32_bf16 a[164:167], v[188:191], v[136:139], a[164:167]\n\t"
    "v_mfma_f32_16x16x32_bf16 a[168:171], v[188:191], v[140:143], a[168:171]\n\t"
    "v_mfma_f32_16x16x32_bf16 a[172:175], v[188:191], v[144:147], a[172:175]\n\t"
    "v_mfma_f32_16x16x32_bf16 a[176:179], v[188:191], v[148:151], a[176:179]\n\t"
    "ds_read_b64_tr_b16 v[188:189], v222 offset:16384\n\t"
    "ds_read_b64_tr_b16 v[190:191], v222 offset:20480\n\t"
    "v_mfma_f32_16x16x32_bf16 a[180:183], v[192:195], v[132:135], a[180:183]\n\t"
    "v_mfma_f32_16x16x32_bf16 a[184:187], v[192:195], v[136:139], a[184:187]\n\t"
    "v_mfma_f32_16x16x32_bf16 a[188:191], v[192:195], v[140:143], a[188:191]\n\t"
    "v_mfma_f32_16x16x32_bf16 a[192:195], v[192:195], v[144:147], a[192:195]\n\t"
    "v_mfma_f32_16x16x32_bf16 a[196:199], v[192:195], v[148:151], a[196:199]\n\t"
    "ds_read_b64_tr_b16 v[192:193], v222 offset:16896\n\t"
    "s_waitcnt lgkmcnt(3)\n\t"
    "ds_read_b64_tr_b16 v[194:195], v222 offset:20992\n\t"
    "v_mfma_f32_16x16x32_bf16 a[200:203], v[196:199], v[132:135], a[200:203]\n\t"
    "v_mfma_f32_16x16x32_bf16 a[204:207], v[196:199], v[136:139], a[204:207]\n\t"
    "v_mfma_f32_16x16x32_bf16 a[208:211], v[196:199], v[140:143], a[208:211]\n\t"
    "v_mfma_f32_16x16x32_bf16 a[212:215], v[196:199], v[144:147], a[212:215]\n\t"
    "v_mfma_f32_16x16x32_bf16 a[216:219], v[196:199], v[148:151], a[216:219]\n\t"
    "v_mfma_f32_16x16x32_bf16 a[220:223], v[200:203], v[132:135], a[220:223]\n\t"
    "v_mfma_f32_16x16x32_bf16 a[224:227], v[200:203], v[136:139], a[224:227]\n\t"
    "v_mfma_f32_16x16x32_bf16 a[228:231], v[200:203], v[140:143], a[228:231]\n\t"
    "v_mfma_f32_16x16x32_bf16 a[232:235], v[200:203], v[144:147], a[232:235]\n\t"
    "v_mfma_f32_16x16x32_bf16 a[236:239], v[200:203], v[148:151], a[236:239]\n\t"
    "s_waitcnt vmcnt(0)\n\t"
    "v_mfma_f32_16x16x32_bf16 v[12:15], v[172:175], v[152:155], v[12:15]\n\t"
    "v_mfma_f32_16x16x32_bf16 v[16:19], v[172:175], v[156:159], v[16:19]\n\t"
    "v_mfma_f32_16x16x32_bf16 v[20:23], v[172:175], v[160:163], v[20:23]\n\t"
    "v_mfma_f32_16x16x32_bf16 v[24:27], v[172:175], v[164:167], v[24:27]\n\t"
    "v_mfma_f32_16x16x32_bf16 v[28:31], v[172:175], v[168:171], v[28:31]\n\t"
    "v_mfma_f32_16x16x32_bf16 v[32:35], v[176:179], v[152:155], v[32:35]\n\t"
    "v_mfma_f32_16x16x32_bf16 v[36:39], v[176:179], v[156:159], v[36:39]\n\t"
    "v_mfma_f32_16x16x32_bf16 v[40:43], v[176:179], v[160:163], v[40:43]\n\t"
    "v_mfma_f32_16x16x32_bf16 v[44:47], v[176:179], v[164:167], v[44:47]\n\t"
    "v_mfma_f32_16x16x32_bf16 v[48:51], v[176:179], v[168:171], v[48:51]\n\t"
    "v_mfma_f32_16x16x32_bf16 v[52:55], v[180:183], v[152:155], v[52:55]\n\t"
    "v_mfma_f32_16x16x32_bf16 v[56:59], v[180:183], v[156:159], v[56:59]\n\t"
    "v_mfma_f32_16x16x32_bf16 v[60:63], v[180:183], v[160:163], v[60:63]\n\t"
    "v_mfma_f32_16x16x32_bf16 v[64:67], v[180:183], v[164:167], v[64:67]\n\t"
    "v_mfma_f32_16x16x32_bf16 v[68:71], v[180:183], v[168:171], v[68:71]\n\t"
    "v_mfma_f32_16x16x32_bf16 v[72:75], v[184:187], v[152:155], v[72:75]\n\t"
    "v_mfma_f32_16x16x32_bf16 v[76:79], v[184:187], v[156:159], v[76:79]\n\t"
    "v_mfma_f32_16x16x32_bf16 v[80:83], v[184:187], v[160:163], v[80:83]\n\t"
    "v_mfma_f32_16x16x32_bf16 v[84:87], v[184:187], v[164:167], v[84:87]\n\t"
    "v_mfma_f32_16x16x32_bf16 v[88:91], v[184:187], v[168:171], v[88:91]\n\t"
    "s_waitcnt lgkmcnt(0)\n\t"
    "v_mfma_f32_16x16x32_bf16 a[160:163], v[188:191], v[152:155], a[160:163]\n\t"
    "v_mfma_f32_16x16x32_bf16 a[164:167], v[188:191], v[156:159], a[164:167]\n\t"
    "v_mfma_f32_16x16x32_bf16 a[168:171], v[188:191], v[160:163], a[168:171]\n\t"
    "v_mfma_f32_16x16x32_bf16 a[172:175], v[188:191], v[164:167], a[172:175]\n\t"
    "v_mfma_f32_16x16x32_bf16 a[176:179], v[188:191], v[168:171], a[176:179]\n\t"
    "v_mfma_f32_16x16x32_bf16 a[180:183], v[192:195], v[152:155], a[180:183]\n\t"
    "v_mfma_f32_16x16x32_bf16 a[184:187], v[192:195], v[156:159], a[184:187]\n\t"
    "v_mfma_f32_16x16x32_bf16 a[188:191], v[192:195], v[160:163], a[188:191]\n\t"
    "v_mfma_f32_16x16x32_bf16 a[192:195], v[192:195], v[164:167], a[192:195]\n\t"
    "v_mfma_f32_16x16x32_bf16 a[196:199], v[192:195], v[168:171], a[196:199]\n\t"
    "v_mfma_f32_16x16x32_bf16 a[200:203], v[204:207], v[152:155], a[200:203]\n\t"
    "v_mfma_f32_16x16x32_bf16 a[204:207], v[204:207], v[156:159], a[204:207]\n\t"
    "v_mfma_f32_16x16x32_bf16 a[208:211], v[204:207], v[160:163], a[208:211]\n\t"
    "v_mfma_f32_16x16x32_bf16 a[212:215], v[204:207], v[164:167], a[212:215]\n\t"
    "v_mfma_f32_16x16x32_bf16 a[216:219], v[204:207], v[168:171], a[216:219]\n\t"
    "v_mfma_f32_16x16x32_bf16 a[220:223], v[208:211], v[152:155], a[220:223]\n\t"
    "v_mfma_f32_16x16x32_bf16 a[224:227], v[208:211], v[156:159], a[224:227]\n\t"
    "v_mfma_f32_16x16x32_bf16 a[228:231], v[208:211], v[160:163], a[228:231]\n\t"
    "v_mfma_f32_16x16x32_bf16 a[232:235], v[208:211], v[164:167], a[232:235]\n\t"
    "v_mfma_f32_16x16x32_bf16 a[236:239], v[208:211], v[168:171], a[236:239]\n\t"
    "ds_write_b128 v227, a[160:163] offset:0\n\t"
    "ds_write_b128 v227, a[164:167] offset:1024\n\t"
    "ds_write_b128 v227, a[168:171] offset:2048\n\t"
    "ds_write_b128 v227, a[172:175] offset:3072\n\t"
    "ds_write_b128 v227, a[176:179] offset:4096\n\t"
    "ds_write_b128 v227, a[180:183] offset:5120\n\t"
    "ds_write_b128 v227, a[184:187] offset:6144\n\t"
    "ds_write_b128 v227, a[188:191] offset:7168\n\t"
    "ds_write_b128 v227, a[192:195] offset:8192\n\t"
    "ds_write_b128 v227, a[196:199] offset:9216\n\t"
    "ds_write_b128 v227, a[200:203] offset:10240\n\t"
    "ds_write_b128 v227, a[204:207] offset:11264\n\t"
    "ds_write_b128 v227, a[208:211] offset:12288\n\t"
    "ds_write_b128 v227, a[212:215] offset:13312\n\t"
    "ds_write_b128 v227, a[216:219] offset:14336\n\t"
    "s_waitcnt lgkmcnt(14)\n\t"
    "ds_write_b128 v227, a[220:223] offset:15360\n\t"
    "s_waitcnt lgkmcnt(14)\n\t"
    "ds_write_b128 v227, a[224:227] offset:16384\n\t"
    "s_waitcnt lgkmcnt(14)\n\t"
    "ds_write_b128 v227, a[228:231] offset:17408\n\t"
    "s_waitcnt lgkmcnt(14)\n\t"
    "ds_write_b128 v227, a[232:235] offset:18432\n\t"
    "s_waitcnt lgkmcnt(14)\n\t"
    "ds_write_b128 v227, a[236:239] offset:19456\n\t"
    "s_waitcnt lgkmcnt(0)\n\t"
    "s_barrier\n\t"
    "ds_read_b128 v[112:115], v228 offset:5120\n\t"
    "ds_read_b128 v[116:119], v228 offset:6144\n\t"
    "ds_read_b128 v[120:123], v228 offset:7168\n\t"
    "ds_read_b128 v[124:127], v228 offset:8192\n\t"
    "ds_read_b128 v[128:131], v228 offset:9216\n\t"
    "ds_read_b128 v[132:135], v228 offset:10240\n\t"
    "ds_read_b128 v[136:139], v228 offset:11264\n\t"
    "ds_read_b128 v[140:143], v228 offset:12288\n\t"
    "ds_read_b128 v[144:147], v228 offset:13312\n\t"
    "ds_read_b128 v[148:151], v228 offset:14336\n\t"
    "ds_read_b128 v[152:155], v228 offset:15360\n\t"
    "ds_read_b128 v[156:159], v228 offset:16384\n\t"
    "ds_read_b128 v[160:163], v228 offset:17408\n\t"
    "ds_read_b128 v[164:167], v228 offset:18432\n\t"
    "ds_read_b128 v[168:171], v228 offset:19456\n\t"
    "s_waitcnt lgkmcnt(14)\n\t"
    "ds_read_b128 v[92:95], v228 offset:0\n\t"
    "s_waitcnt lgkmcnt(14)\n\t"
    "ds_read_b128 v[96:99], v228 offset:1024\n\t"
    "s_waitcnt lgkmcnt(14)\n\t"
    "ds_read_b128 v[100:103], v228 offset:2048\n\t"
    "s_waitcnt lgkmcnt(14)\n\t"
    "ds_read_b128 v[104:107], v228 offset:3072\n\t"
    "s_waitcnt lgkmcnt(14)\n\t"
    "ds_read_b128 v[108:111], v228 offset:4096\n\t"
    "v_add_f32 v32, v32, v112\n\t"
    "v_add_f32 v33, v33, v113\n\t"
    "v_add_f32 v34, v34, v114\n\t"
    "v_add_f32 v35, v35, v115\n\t"
    "v_cvt_pk_bf16_f32 v172, v32, v33\n\t"
    "v_cvt_pk_bf16_f32 v173, v34, v35\n\t"
    "v_add_f32 v36, v36, v116\n\t"
    "v_add_f32 v37, v37, v117\n\t"
    "v_add_f32 v38, v38, v118\n\t"
    "v_add_f32 v39, v39, v119\n\t"
    "v_cvt_pk_bf16_f32 v174, v36, v37\n\t"
    "v_cvt_pk_bf16_f32 v175, v38, v39\n\t"
    "v_add_f32 v40, v40, v120\n\t"
    "buffer_store_dwordx4 v[172:175], v239, s[40:43], s70 offen sc1\n\t"
    "v_add_f32 v41, v41, v121\n\t"
    "v_add_f32 v42, v42, v122\n\t"
    "v_add_f32 v43, v43, v123\n\t"
    "v_cvt_pk_bf16_f32 v176, v40, v41\n\t"
    "v_cvt_pk_bf16_f32 v177, v42, v43\n\t"
    "v_add_f32 v44, v44, v124\n\t"
    "v_add_f32 v45, v45, v125\n\t"
    "v_add_f32 v46, v46, v126\n\t"
    "v_add_f32 v47, v47, v127\n\t"
    "v_cvt_pk_bf16_f32 v178, v44, v45\n\t"
    "v_cvt_pk_bf16_f32 v179, v46, v47\n\t"
    "v_add_f32 v48, v48, v128\n\t"
    "buffer_store_dwordx4 v[176:179], v239, s[40:43], s70 offen offset:1024 sc1\n\t"
    "v_add_f32 v49, v49, v129\n\t"
    "v_add_f32 v50, v50, v130\n\t"
    "v_add_f32 v51, v51, v131\n\t"
    "v_cvt_pk_bf16_f32 v180, v48, v49\n\t"
    "v_cvt_pk_bf16_f32 v181, v50, v51\n\t"
    "s_waitcnt lgkmcnt(0)\n\t"
    "v_add_f32 v52, v52, v132\n\t"
    "buffer_store_dwordx2 v[180:181], v233, s[40:43], s70 offen offset:2048 sc1\n\t"
    "v_add_f32 v53, v53, v133\n\t"
    "v_add_f32 v54, v54, v134\n\t"
    "v_add_f32 v55, v55, v135\n\t"
    "v_cvt_pk_bf16_f32 v182, v52, v53\n\t"
    "v_cvt_pk_bf16_f32 v183, v54, v55\n\t"
    "v_add_f32 v56, v56, v136\n\t"
    "v_add_f32 v57, v57, v137\n\t"
    "v_add_f32 v58, v58, v138\n\t"
    "v_add_f32 v59, v59, v139\n\t"
    "v_cvt_pk_bf16_f32 v184, v56, v57\n\t"
    "v_cvt_pk_bf16_f32 v185, v58, v59\n\t"
    "v_add_f32 v60, v60, v140\n\t"
    "buffer_store_dwordx4 v[182:185], v239, s[40:43], s71 offen sc1\n\t"
    "v_add_f32 v61, v61, v141\n\t"
    "v_add_f32 v62, v62, v142\n\t"
    "v_add_f32 v63, v63, v143\n\t"
    "v_cvt_pk_bf16_f32 v186, v60, v61\n\t"
    "v_cvt_pk_bf16_f32 v187, v62, v63\n\t"
    "v_add_f32 v64, v64, v144\n\t"
    "v_add_f32 v65, v65, v145\n\t"
    "v_add_f32 v66, v66, v146\n\t"
    "v_add_f32 v67, v67, v147\n\t"
    "v_cvt_pk_bf16_f32 v188, v64, v65\n\t"
    "v_cvt_pk_bf16_f32 v189, v66, v67\n\t"
    "v_add_f32 v68, v68, v148\n\t"
    "buffer_store_dwordx4 v[186:189], v239, s[40:43], s71 offen offset:1024 sc1\n\t"
    "v_add_f32 v69, v69, v149\n\t"
    "v_add_f32 v70, v70, v150\n\t"
    "v_add_f32 v71, v71, v151\n\t"
    "v_cvt_pk_bf16_f32 v190, v68, v69\n\t"
    "v_cvt_pk_bf16_f32 v191, v70, v71\n\t"
    "v_add_f32 v72, v72, v152\n\t"
    "buffer_store_dwordx2 v[190:191], v233, s[40:43], s71 offen offset:2048 sc1\n\t"
    "v_add_f32 v73, v73, v153\n\t"
    "v_add_f32 v74, v74, v154\n\t"
    "v_add_f32 v75, v75, v155\n\t"
    "v_cvt_pk_bf16_f32 v192, v72, v73\n\t"
    "v_cvt_pk_bf16_f32 v193, v74, v75\n\t"
    "v_add_f32 v76, v76, v156\n\t"
    "v_add_f32 v77, v77, v157\n\t"
    "v_add_f32 v78, v78, v158\n\t"
    "v_add_f32 v79, v79, v159\n\t"
    "v_cvt_pk_bf16_f32 v194, v76, v77\n\t"
    "v_cvt_pk_bf16_f32 v195, v78, v79\n\t"
    "v_add_f32 v80, v80, v160\n\t"
    "buffer_store_dwordx4 v[192:195], v239, s[40:43], s72 offen sc1\n\t"
    "v_add_f32 v81, v81, v161\n\t"
    "v_add_f32 v82, v82, v162\n\t"
    "v_add_f32 v83, v83, v163\n\t"
    "v_cvt_pk_bf16_f32 v196, v80, v81\n\t"
    "v_cvt_pk_bf16_f32 v197, v82, v83\n\t"
    "v_add_f32 v84, v84, v164\n\t"
    "v_add_f32 v85, v85, v165\n\t"
    "v_add_f32 v86, v86, v166\n\t"
    "v_add_f32 v87, v87, v167\n\t"
    "v_cvt_pk_bf16_f32 v198, v84, v85\n\t"
    "v_cvt_pk_bf16_f32 v199, v86, v87\n\t"
    "v_add_f32 v88, v88, v168\n\t"
    "buffer_store_dwordx4 v[196:199], v239, s[40:43], s72 offen offset:1024 sc1\n\t"
    "v_add_f32 v89, v89, v169\n\t"
    "v_add_f32 v90, v90, v170\n\t"
    "v_add_f32 v91, v91, v171\n\t"
    "v_cvt_pk_bf16_f32 v200, v88, v89\n\t"
    "v_cvt_pk_bf16_f32 v201, v90, v91\n\t"
    "v_add_f32 v12, v12, v92\n\t"
    "buffer_store_dwordx2 v[200:201], v233, s[40:43], s72 offen offset:2048 sc1\n\t"
    "v_add_f32 v13, v13, v93\n\t"
    "v_add_f32 v14, v14, v94\n\t"
    "v_add_f32 v15, v15, v95\n\t"
    "v_add_f32 v16, v16, v96\n\t"
    "v_add_f32 v17, v17, v97\n\t"
    "v_add_f32 v18, v18, v98\n\t"
    "v_add_f32 v19, v19, v99\n\t"
    "v_add_f32 v20, v20, v100\n\t"
    "v_add_f32 v21, v21, v101\n\t"
    "v_add_f32 v22, v22, v102\n\t"
    "v_add_f32 v23, v23, v103\n\t"
    "v_add_f32 v24, v24, v104\n\t"
    "v_add_f32 v25, v25, v105\n\t"
    "v_add_f32 v26, v26, v106\n\t"
    "v_add_f32 v27, v27, v107\n\t"
    "v_add_f32 v28, v28, v108\n\t"
    "v_add_f32 v29, v29, v109\n\t"
    "v_add_f32 v30, v30, v110\n\t"
    "v_add_f32 v31, v31, v111\n\t"
    "s_waitcnt vmcnt(0)\n\t"
    "s_mov_b64 exec, 1\n\t"
    "buffer_store_dwordx2 v[212:213], v217, s[56:59], 0 offen sc1\n\t"
    "s_mov_b64 exec, -1\n\t"
    "s_mov_b32 s64, 0x10000\n\t"
    "K1_POLL2:\n\t"
    "buffer_load_dwordx2 v[214:215], v217, s[56:59], 0 offen sc1\n\t"
    "s_waitcnt vmcnt(0)\n\t"
    "v_cmp_eq_u32 vcc, v214, v212\n\t"
    "v_cmp_eq_u32 s[76:77], v215, v213\n\t"
    "s_nop 3\n\t"
    "s_and_b64 vcc, vcc, s[76:77]\n\t"
    "s_and_b32 vcc_lo, vcc_lo, 112\n\t"
    "s_cmp_eq_u32 vcc_lo, 112\n\t"
    "s_cbranch_scc1 K1_POLL2_OK\n\t"
    "s_sleep 1\n\t"
    "s_sub_u32 s64, s64, 1\n\t"
    "s_cmp_lg_u32 s64, 0\n\t"
    "s_cbranch_scc1 K1_POLL2\n\t"
    "K1_POLL2_OK:\n\t"
    "; vm reset\n\t"
    "s_mov_b32 s64, 0x3f80\n\t"
    "s_mov_b32 s69, 0x3f800000\n\t"
    "s_add_u32 s74, s73, 0\n\t"
    "buffer_load_dwordx4 v[92:95], v239, s[40:43], s74 offen sc1\n\t"
    "s_add_u32 s75, s73, 0xa00\n\t"
    "buffer_load_dwordx4 v[102:105], v239, s[40:43], s75 offen sc1\n\t"
    "s_add_u32 s74, s73, 0x1400\n\t"
    "buffer_load_dwordx4 v[112:115], v239, s[40:43], s74 offen sc1\n\t"
    "s_add_u32 s74, s73, 0\n\t"
    "buffer_load_dwordx4 v[96:99], v239, s[40:43], s74 offen offset:1024 sc1\n\t"
    "s_add_u32 s75, s73, 0xa00\n\t"
    "buffer_load_dwordx4 v[106:109], v239, s[40:43], s75 offen offset:1024 sc1\n\t"
    "s_add_u32 s74, s73, 0x1400\n\t"
    "buffer_load_dwordx4 v[116:119], v239, s[40:43], s74 offen offset:1024 sc1\n\t"
    "s_add_u32 s74, s73, 0\n\t"
    "buffer_load_dwordx2 v[100:101], v233, s[40:43], s74 offen offset:2048 sc1\n\t"
    "s_add_u32 s75, s73, 0xa00\n\t"
    "buffer_load_dwordx2 v[110:111], v233, s[40:43], s75 offen offset:2048 sc1\n\t"
    "s_add_u32 s74, s73, 0x1400\n\t"
    "buffer_load_dwordx2 v[120:121], v233, s[40:43], s74 offen offset:2048 sc1\n\t"
    "s_waitcnt vmcnt(6)\n\t"
    "v_dot2c_f32_bf16 v12, s64, v92\n\t"
    "v_dot2c_f32_bf16 v13, s69, v92\n\t"
    "v_dot2c_f32_bf16 v14, s64, v93\n\t"
    "v_dot2c_f32_bf16 v15, s69, v93\n\t"
    "v_dot2c_f32_bf16 v12, s64, v102\n\t"
    "v_dot2c_f32_bf16 v13, s69, v102\n\t"
    "v_dot2c_f32_bf16 v14, s64, v103\n\t"
    "v_dot2c_f32_bf16 v15, s69, v103\n\t"
    "v_dot2c_f32_bf16 v12, s64, v112\n\t"
    "v_dot2c_f32_bf16 v13, s69, v112\n\t"
    "v_dot2c_f32_bf16 v14, s64, v113\n\t"
    "v_dot2c_f32_bf16 v15, s69, v113\n\t"
    "s_nop 0\n\t"
    "v_cvt_pk_bf16_f32 v12, v12, v13\n\t"
    "s_nop 0\n\t"
    "v_cvt_pk_bf16_f32 v13, v14, v15\n\t"
    "s_mov_b32 s74, 0\n\t"
    "buffer_store_dwordx2 v[12:13], v231, s[44:47], s74 offen\n\t"
    "v_dot2c_f32_bf16 v16, s64, v94\n\t"
    "v_dot2c_f32_bf16 v17, s69, v94\n\t"
    "v_dot2c_f32_bf16 v18, s64, v95\n\t"
    "v_dot2c_f32_bf16 v19, s69, v95\n\t"
    "v_dot2c_f32_bf16 v16, s64, v104\n\t"
    "v_dot2c_f32_bf16 v17, s69, v104\n\t"
    "v_dot2c_f32_bf16 v18, s64, v105\n\t"
    "v_dot2c_f32_bf16 v19, s69, v105\n\t"
    "v_dot2c_f32_bf16 v16, s64, v114\n\t"
    "v_dot2c_f32_bf16 v17, s69, v114\n\t"
    "v_dot2c_f32_bf16 v18, s64, v115\n\t"
    "v_dot2c_f32_bf16 v19, s69, v115\n\t"
    "s_nop 0\n\t"
    "v_cvt_pk_bf16_f32 v16, v16, v17\n\t"
    "s_nop 0\n\t"
    "v_cvt_pk_bf16_f32 v17, v18, v19\n\t"
    "s_mov_b32 s75, 0x8000\n\t"
    "buffer_store_dwordx2 v[16:17], v231, s[44:47], s75 offen\n\t"
    "s_waitcnt vmcnt(5)\n\t"
    "v_dot2c_f32_bf16 v20, s64, v96\n\t"
    "v_dot2c_f32_bf16 v21, s69, v96\n\t"
    "v_dot2c_f32_bf16 v22, s64, v97\n\t"
    "v_dot2c_f32_bf16 v23, s69, v97\n\t"
    "v_dot2c_f32_bf16 v20, s64, v106\n\t"
    "v_dot2c_f32_bf16 v21, s69, v106\n\t"
    "v_dot2c_f32_bf16 v22, s64, v107\n\t"
    "v_dot2c_f32_bf16 v23, s69, v107\n\t"
    "v_dot2c_f32_bf16 v20, s64, v116\n\t"
    "v_dot2c_f32_bf16 v21, s69, v116\n\t"
    "v_dot2c_f32_bf16 v22, s64, v117\n\t"
    "v_dot2c_f32_bf16 v23, s69, v117\n\t"
    "s_nop 0\n\t"
    "v_cvt_pk_bf16_f32 v20, v20, v21\n\t"
    "s_nop 0\n\t"
    "v_cvt_pk_bf16_f32 v21, v22, v23\n\t"
    "s_mov_b32 s74, 0x10000\n\t"
    "buffer_store_dwordx2 v[20:21], v231, s[44:47], s74 offen\n\t"
    "v_dot2c_f32_bf16 v24, s64, v98\n\t"
    "v_dot2c_f32_bf16 v25, s69, v98\n\t"
    "v_dot2c_f32_bf16 v26, s64, v99\n\t"
    "v_dot2c_f32_bf16 v27, s69, v99\n\t"
    "v_dot2c_f32_bf16 v24, s64, v108\n\t"
    "v_dot2c_f32_bf16 v25, s69, v108\n\t"
    "v_dot2c_f32_bf16 v26, s64, v109\n\t"
    "v_dot2c_f32_bf16 v27, s69, v109\n\t"
    "v_dot2c_f32_bf16 v24, s64, v118\n\t"
    "v_dot2c_f32_bf16 v25, s69, v118\n\t"
    "v_dot2c_f32_bf16 v26, s64, v119\n\t"
    "v_dot2c_f32_bf16 v27, s69, v119\n\t"
    "s_nop 0\n\t"
    "v_cvt_pk_bf16_f32 v24, v24, v25\n\t"
    "s_nop 0\n\t"
    "v_cvt_pk_bf16_f32 v25, v26, v27\n\t"
    "s_mov_b32 s75, 0x18000\n\t"
    "buffer_store_dwordx2 v[24:25], v231, s[44:47], s75 offen\n\t"
    "s_waitcnt vmcnt(4)\n\t"
    "v_dot2c_f32_bf16 v28, s64, v100\n\t"
    "v_dot2c_f32_bf16 v29, s69, v100\n\t"
    "v_dot2c_f32_bf16 v30, s64, v101\n\t"
    "v_dot2c_f32_bf16 v31, s69, v101\n\t"
    "v_dot2c_f32_bf16 v28, s64, v110\n\t"
    "v_dot2c_f32_bf16 v29, s69, v110\n\t"
    "v_dot2c_f32_bf16 v30, s64, v111\n\t"
    "v_dot2c_f32_bf16 v31, s69, v111\n\t"
    "v_dot2c_f32_bf16 v28, s64, v120\n\t"
    "v_dot2c_f32_bf16 v29, s69, v120\n\t"
    "v_dot2c_f32_bf16 v30, s64, v121\n\t"
    "v_dot2c_f32_bf16 v31, s69, v121\n\t"
    "s_nop 0\n\t"
    "v_cvt_pk_bf16_f32 v28, v28, v29\n\t"
    "s_nop 0\n\t"
    "v_cvt_pk_bf16_f32 v29, v30, v31\n\t"
    "s_mov_b32 s74, 0x20000\n\t"
    "buffer_store_dwordx2 v[28:29], v231, s[44:47], s74 offen\n\t"
    "s_mov_b64 exec, 1\n\t"
    "buffer_store_dword v212, %8, s[56:59], 0 offen sc1\n\t"
    "s_mov_b64 exec, -1\n\t"
    "s_branch K1_END\n\t"
    "K1_RARE0:\n\t"
    "s_nop 15\n\t"
    "v_max3_f32 v242, v16, v17, v18\n\t"
    "v_max3_f32 v242, v242, v19, v56\n\t"
    "v_max3_f32 v242, v242, v57, v58\n\t"
    "v_max_f32 v242, v242, v59\n\t"
    "v_mov_b32 v243, v242\n\t"
    "s_nop 1\n\t"
    "v_permlane32_swap_b32 v242, v243\n\t"
    "v_max_f32 v242, v242, v243\n\t"
    "v_mov_b32 v243, v242\n\t"
    "s_nop 1\n\t"
    "v_permlane16_swap_b32 v242, v243\n\t"
    "v_max3_f32 v242, v242, v243, 0\n\t"
    "v_sub_f32 v244, 0, v242\n\t"
    "v_exp_f32 v244, v244\n\t"
    "v_sub_f32 v16, v16, v242\n\t"
    "v_sub_f32 v17, v17, v242\n\t"
    "v_sub_f32 v18, v18, v242\n\t"
    "v_sub_f32 v19, v19, v242\n\t"
    "v_sub_f32 v56, v56, v242\n\t"
    "v_sub_f32 v57, v57, v242\n\t"
    "v_sub_f32 v58, v58, v242\n\t"
    "v_sub_f32 v59, v59, v242\n\t"
    "v_sub_f32 v192, v192, v242\n\t"
    "v_sub_f32 v193, v193, v242\n\t"
    "v_sub_f32 v194, v194, v242\n\t"
    "v_sub_f32 v195, v195, v242\n\t"
    "v_accvgpr_read_b32 v245, a0\n\t"
    "v_mul_f32 v245, v245, v244\n\t"
    "v_accvgpr_write_b32 a0, v245\n\t"
    "v_accvgpr_read_b32 v246, a1\n\t"
    "v_mul_f32 v246, v246, v244\n\t"
    "v_accvgpr_write_b32 a1, v246\n\t"
    "v_accvgpr_read_b32 v247, a2\n\t"
    "v_mul_f32 v247, v247, v244\n\t"
    "v_accvgpr_write_b32 a2, v247\n\t"
    "v_accvgpr_read_b32 v248, a3\n\t"
    "v_mul_f32 v248, v248, v244\n\t"
    "v_accvgpr_write_b32 a3, v248\n\t"
    "v_accvgpr_read_b32 v249, a20\n\t"
    "v_mul_f32 v249, v249, v244\n\t"
    "v_accvgpr_write_b32 a20, v249\n\t"
    "v_accvgpr_read_b32 v250, a21\n\t"
    "v_mul_f32 v250, v250, v244\n\t"
    "v_accvgpr_write_b32 a21, v250\n\t"
    "v_accvgpr_read_b32 v245, a22\n\t"
    "v_mul_f32 v245, v245, v244\n\t"
    "v_accvgpr_write_b32 a22, v245\n\t"
    "v_accvgpr_read_b32 v246, a23\n\t"
    "v_mul_f32 v246, v246, v244\n\t"
    "v_accvgpr_write_b32 a23, v246\n\t"
    "v_accvgpr_read_b32 v247, a40\n\t"
    "v_mul_f32 v247, v247, v244\n\t"
    "v_accvgpr_write_b32 a40, v247\n\t"
    "v_accvgpr_read_b32 v248, a41\n\t"
    "v_mul_f32 v248, v248, v244\n\t"
    "v_accvgpr_write_b32 a41, v248\n\t"
    "v_accvgpr_read_b32 v249, a42\n\t"
    "v_mul_f32 v249, v249, v244\n\t"
    "v_accvgpr_write_b32 a42, v249\n\t"
    "v_accvgpr_read_b32 v250, a43\n\t"
    "v_mul_f32 v250, v250, v244\n\t"
    "v_accvgpr_write_b32 a43, v250\n\t"
    "v_accvgpr_read_b32 v245, a60\n\t"
    "v_mul_f32 v245, v245, v244\n\t"
    "v_accvgpr_write_b32 a60, v245\n\t"
    "v_accvgpr_read_b32 v246, a61\n\t"
    "v_mul_f32 v246, v246, v244\n\t"
    "v_accvgpr_write_b32 a61, v246\n\t"
    "v_accvgpr_read_b32 v247, a62\n\t"
    "v_mul_f32 v247, v247, v244\n\t"
    "v_accvgpr_write_b32 a62, v247\n\t"
    "v_accvgpr_read_b32 v248, a63\n\t"
    "v_mul_f32 v248, v248, v244\n\t"
    "v_accvgpr_write_b32 a63, v248\n\t"
    "v_accvgpr_read_b32 v249, a80\n\t"
    "v_mul_f32 v249, v249, v244\n\t"
    "v_accvgpr_write_b32 a80, v249\n\t"
    "v_accvgpr_read_b32 v250, a81\n\t"
    "v_mul_f32 v250, v250, v244\n\t"
    "v_accvgpr_write_b32 a81, v250\n\t"
    "v_accvgpr_read_b32 v245, a82\n\t"
    "v_mul_f32 v245, v245, v244\n\t"
    "v_accvgpr_write_b32 a82, v245\n\t"
    "v_accvgpr_read_b32 v246, a83\n\t"
    "v_mul_f32 v246, v246, v244\n\t"
    "v_accvgpr_write_b32 a83, v246\n\t"
    "v_accvgpr_read_b32 v247, a100\n\t"
    "v_mul_f32 v247, v247, v244\n\t"
    "v_accvgpr_write_b32 a100, v247\n\t"
    "v_accvgpr_read_b32 v248, a101\n\t"
    "v_mul_f32 v248, v248, v244\n\t"
    "v_accvgpr_write_b32 a101, v248\n\t"
    "v_accvgpr_read_b32 v249, a102\n\t"
    "v_mul_f32 v249, v249, v244\n\t"
    "v_accvgpr_write_b32 a102, v249\n\t"
    "v_accvgpr_read_b32 v250, a103\n\t"
    "v_mul_f32 v250, v250, v244\n\t"
    "v_accvgpr_write_b32 a103, v250\n\t"
    "v_accvgpr_read_b32 v245, a120\n\t"
    "v_mul_f32 v245, v245, v244\n\t"
    "v_accvgpr_write_b32 a120, v245\n\t"
    "v_accvgpr_read_b32 v246, a121\n\t"
    "v_mul_f32 v246, v246, v244\n\t"
    "v_accvgpr_write_b32 a121, v246\n\t"
    "v_accvgpr_read_b32 v247, a122\n\t"
    "v_mul_f32 v247, v247, v244\n\t"
    "v_accvgpr_write_b32 a122, v247\n\t"
    "v_accvgpr_read_b32 v248, a123\n\t"
    "v_mul_f32 v248, v248, v244\n\t"
    "v_accvgpr_write_b32 a123, v248\n\t"
    "v_accvgpr_read_b32 v249, a140\n\t"
    "v_mul_f32 v249, v249, v244\n\t"
    "v_accvgpr_write_b32 a140, v249\n\t"
    "v_accvgpr_read_b32 v250, a141\n\t"
    "v_mul_f32 v250, v250, v244\n\t"
    "v_accvgpr_write_b32 a141, v250\n\t"
    "v_accvgpr_read_b32 v245, a142\n\t"
    "v_mul_f32 v245, v245, v244\n\t"
    "v_accvgpr_write_b32 a142, v245\n\t"
    "v_accvgpr_read_b32 v246, a143\n\t"
    "v_mul_f32 v246, v246, v244\n\t"
    "v_accvgpr_write_b32 a143, v246\n\t"
    "v_accvgpr_read_b32 v247, a160\n\t"
    "v_mul_f32 v247, v247, v244\n\t"
    "v_accvgpr_write_b32 a160, v247\n\t"
    "v_accvgpr_read_b32 v248, a161\n\t"
    "v_mul_f32 v248, v248, v244\n\t"
    "v_accvgpr_write_b32 a161, v248\n\t"
    "v_accvgpr_read_b32 v249, a162\n\t"
    "v_mul_f32 v249, v249, v244\n\t"
    "v_accvgpr_write_b32 a162, v249\n\t"
    "v_accvgpr_read_b32 v250, a163\n\t"
    "v_mul_f32 v250, v250, v244\n\t"
    "v_accvgpr_write_b32 a163, v250\n\t"
    "v_max3_f32 v242, v24, v25, v26\n\t"
    "v_max3_f32 v242, v242, v27, v64\n\t"
    "v_max3_f32 v242, v242, v65, v66\n\t"
    "v_max_f32 v242, v242, v67\n\t"
    "v_mov_b32 v243, v242\n\t"
    "s_nop 1\n\t"
    "v_permlane32_swap_b32 v242, v243\n\t"
    "v_max_f32 v242, v242, v243\n\t"
    "v_mov_b32 v243, v242\n\t"
    "s_nop 1\n\t"
    "v_permlane16_swap_b32 v242, v243\n\t"
    "v_max3_f32 v242, v242, v243, 0\n\t"
    "v_sub_f32 v244, 0, v242\n\t"
    "v_exp_f32 v244, v244\n\t"
    "v_sub_f32 v24, v24, v242\n\t"
    "v_sub_f32 v25, v25, v242\n\t"
    "v_sub_f32 v26, v26, v242\n\t"
    "v_sub_f32 v27, v27, v242\n\t"
    "v_sub_f32 v64, v64, v242\n\t"
    "v_sub_f32 v65, v65, v242\n\t"
    "v_sub_f32 v66, v66, v242\n\t"
    "v_sub_f32 v67, v67, v242\n\t"
    "v_sub_f32 v196, v196, v242\n\t"
    "v_sub_f32 v197, v197, v242\n\t"
    "v_sub_f32 v198, v198, v242\n\t"
    "v_sub_f32 v199, v199, v242\n\t"
    "v_accvgpr_read_b32 v245, a4\n\t"
    "v_mul_f32 v245, v245, v244\n\t"
    "v_accvgpr_write_b32 a4, v245\n\t"
    "v_accvgpr_read_b32 v246, a5\n\t"
    "v_mul_f32 v246, v246, v244\n\t"
    "v_accvgpr_write_b32 a5, v246\n\t"
    "v_accvgpr_read_b32 v247, a6\n\t"
    "v_mul_f32 v247, v247, v244\n\t"
    "v_accvgpr_write_b32 a6, v247\n\t"
    "v_accvgpr_read_b32 v248, a7\n\t"
    "v_mul_f32 v248, v248, v244\n\t"
    "v_accvgpr_write_b32 a7, v248\n\t"
    "v_accvgpr_read_b32 v249, a24\n\t"
    "v_mul_f32 v249, v249, v244\n\t"
    "v_accvgpr_write_b32 a24, v249\n\t"
    "v_accvgpr_read_b32 v250, a25\n\t"
    "v_mul_f32 v250, v250, v244\n\t"
    "v_accvgpr_write_b32 a25, v250\n\t"
    "v_accvgpr_read_b32 v245, a26\n\t"
    "v_mul_f32 v245, v245, v244\n\t"
    "v_accvgpr_write_b32 a26, v245\n\t"
    "v_accvgpr_read_b32 v246, a27\n\t"
    "v_mul_f32 v246, v246, v244\n\t"
    "v_accvgpr_write_b32 a27, v246\n\t"
    "v_accvgpr_read_b32 v247, a44\n\t"
    "v_mul_f32 v247, v247, v244\n\t"
    "v_accvgpr_write_b32 a44, v247\n\t"
    "v_accvgpr_read_b32 v248, a45\n\t"
    "v_mul_f32 v248, v248, v244\n\t"
    "v_accvgpr_write_b32 a45, v248\n\t"
    "v_accvgpr_read_b32 v249, a46\n\t"
    "v_mul_f32 v249, v249, v244\n\t"
    "v_accvgpr_write_b32 a46, v249\n\t"
    "v_accvgpr_read_b32 v250, a47\n\t"
    "v_mul_f32 v250, v250, v244\n\t"
    "v_accvgpr_write_b32 a47, v250\n\t"
    "v_accvgpr_read_b32 v245, a64\n\t"
    "v_mul_f32 v245, v245, v244\n\t"
    "v_accvgpr_write_b32 a64, v245\n\t"
    "v_accvgpr_read_b32 v246, a65\n\t"
    "v_mul_f32 v246, v246, v244\n\t"
    "v_accvgpr_write_b32 a65, v246\n\t"
    "v_accvgpr_read_b32 v247, a66\n\t"
    "v_mul_f32 v247, v247, v244\n\t"
    "v_accvgpr_write_b32 a66, v247\n\t"
    "v_accvgpr_read_b32 v248, a67\n\t"
    "v_mul_f32 v248, v248, v244\n\t"
    "v_accvgpr_write_b32 a67, v248\n\t"
    "v_accvgpr_read_b32 v249, a84\n\t"
    "v_mul_f32 v249, v249, v244\n\t"
    "v_accvgpr_write_b32 a84, v249\n\t"
    "v_accvgpr_read_b32 v250, a85\n\t"
    "v_mul_f32 v250, v250, v244\n\t"
    "v_accvgpr_write_b32 a85, v250\n\t"
    "v_accvgpr_read_b32 v245, a86\n\t"
    "v_mul_f32 v245, v245, v244\n\t"
    "v_accvgpr_write_b32 a86, v245\n\t"
    "v_accvgpr_read_b32 v246, a87\n\t"
    "v_mul_f32 v246, v246, v244\n\t"
    "v_accvgpr_write_b32 a87, v246\n\t"
    "v_accvgpr_read_b32 v247, a104\n\t"
    "v_mul_f32 v247, v247, v244\n\t"
    "v_accvgpr_write_b32 a104, v247\n\t"
    "v_accvgpr_read_b32 v248, a105\n\t"
    "v_mul_f32 v248, v248, v244\n\t"
    "v_accvgpr_write_b32 a105, v248\n\t"
    "v_accvgpr_read_b32 v249, a106\n\t"
    "v_mul_f32 v249, v249, v244\n\t"
    "v_accvgpr_write_b32 a106, v249\n\t"
    "v_accvgpr_read_b32 v250, a107\n\t"
    "v_mul_f32 v250, v250, v244\n\t"
    "v_accvgpr_write_b32 a107, v250\n\t"
    "v_accvgpr_read_b32 v245, a124\n\t"
    "v_mul_f32 v245, v245, v244\n\t"
    "v_accvgpr_write_b32 a124, v245\n\t"
    "v_accvgpr_read_b32 v246, a125\n\t"
    "v_mul_f32 v246, v246, v244\n\t"
    "v_accvgpr_write_b32 a125, v246\n\t"
    "v_accvgpr_read_b32 v247, a126\n\t"
    "v_mul_f32 v247, v247, v244\n\t"
    "v_accvgpr_write_b32 a126, v247\n\t"
    "v_accvgpr_read_b32 v248, a127\n\t"
    "v_mul_f32 v248, v248, v244\n\t"
    "v_accvgpr_write_b32 a127, v248\n\t"
    "v_accvgpr_read_b32 v249, a144\n\t"
    "v_mul_f32 v249, v249, v244\n\t"
    "v_accvgpr_write_b32 a144, v249\n\t"
    "v_accvgpr_read_b32 v250, a145\n\t"
    "v_mul_f32 v250, v250, v244\n\t"
    "v_accvgpr_write_b32 a145, v250\n\t"
    "v_accvgpr_read_b32 v245, a146\n\t"
    "v_mul_f32 v245, v245, v244\n\t"
    "v_accvgpr_write_b32 a146, v245\n\t"
    "v_accvgpr_read_b32 v246, a147\n\t"
    "v_mul_f32 v246, v246, v244\n\t"
    "v_accvgpr_write_b32 a147, v246\n\t"
    "v_accvgpr_read_b32 v247, a164\n\t"
    "v_mul_f32 v247, v247, v244\n\t"
    "v_accvgpr_write_b32 a164, v247\n\t"
    "v_accvgpr_read_b32 v248, a165\n\t"
    "v_mul_f32 v248, v248, v244\n\t"
    "v_accvgpr_write_b32 a165, v248\n\t"
    "v_accvgpr_read_b32 v249, a166\n\t"
    "v_mul_f32 v249, v249, v244\n\t"
    "v_accvgpr_write_b32 a166, v249\n\t"
    "v_accvgpr_read_b32 v250, a167\n\t"
    "v_mul_f32 v250, v250, v244\n\t"
    "v_accvgpr_write_b32 a167, v250\n\t"
    "v_max3_f32 v242, v32, v33, v34\n\t"
    "v_max3_f32 v242, v242, v35, v72\n\t"
    "v_max3_f32 v242, v242, v73, v74\n\t"
    "v_max_f32 v242, v242, v75\n\t"
    "v_mov_b32 v243, v242\n\t"
    "s_nop 1\n\t"
    "v_permlane32_swap_b32 v242, v243\n\t"
    "v_max_f32 v242, v242, v243\n\t"
    "v_mov_b32 v243, v242\n\t"
    "s_nop 1\n\t"
    "v_permlane16_swap_b32 v242, v243\n\t"
    "v_max3_f32 v242, v242, v243, 0\n\t"
    "v_sub_f32 v244, 0, v242\n\t"
    "v_exp_f32 v244, v244\n\t"
    "v_sub_f32 v32, v32, v242\n\t"
    "v_sub_f32 v33, v33, v242\n\t"
    "v_sub_f32 v34, v34, v242\n\t"
    "v_sub_f32 v35, v35, v242\n\t"
    "v_sub_f32 v72, v72, v242\n\t"
    "v_sub_f32 v73, v73, v242\n\t"
    "v_sub_f32 v74, v74, v242\n\t"
    "v_sub_f32 v75, v75, v242\n\t"
    "v_sub_f32 v200, v200, v242\n\t"
    "v_sub_f32 v201, v201, v242\n\t"
    "v_sub_f32 v202, v202, v242\n\t"
    "v_sub_f32 v203, v203, v242\n\t"
    "v_accvgpr_read_b32 v245, a8\n\t"
    "v_mul_f32 v245, v245, v244\n\t"
    "v_accvgpr_write_b32 a8, v245\n\t"
    "v_accvgpr_read_b32 v246, a9\n\t"
    "v_mul_f32 v246, v246, v244\n\t"
    "v_accvgpr_write_b32 a9, v246\n\t"
    "v_accvgpr_read_b32 v247, a10\n\t"
    "v_mul_f32 v247, v247, v244\n\t"
    "v_accvgpr_write_b32 a10, v247\n\t"
    "v_accvgpr_read_b32 v248, a11\n\t"
    "v_mul_f32 v248, v248, v244\n\t"
    "v_accvgpr_write_b32 a11, v248\n\t"
    "v_accvgpr_read_b32 v249, a28\n\t"
    "v_mul_f32 v249, v249, v244\n\t"
    "v_accvgpr_write_b32 a28, v249\n\t"
    "v_accvgpr_read_b32 v250, a29\n\t"
    "v_mul_f32 v250, v250, v244\n\t"
    "v_accvgpr_write_b32 a29, v250\n\t"
    "v_accvgpr_read_b32 v245, a30\n\t"
    "v_mul_f32 v245, v245, v244\n\t"
    "v_accvgpr_write_b32 a30, v245\n\t"
    "v_accvgpr_read_b32 v246, a31\n\t"
    "v_mul_f32 v246, v246, v244\n\t"
    "v_accvgpr_write_b32 a31, v246\n\t"
    "v_accvgpr_read_b32 v247, a48\n\t"
    "v_mul_f32 v247, v247, v244\n\t"
    "v_accvgpr_write_b32 a48, v247\n\t"
    "v_accvgpr_read_b32 v248, a49\n\t"
    "v_mul_f32 v248, v248, v244\n\t"
    "v_accvgpr_write_b32 a49, v248\n\t"
    "v_accvgpr_read_b32 v249, a50\n\t"
    "v_mul_f32 v249, v249, v244\n\t"
    "v_accvgpr_write_b32 a50, v249\n\t"
    "v_accvgpr_read_b32 v250, a51\n\t"
    "v_mul_f32 v250, v250, v244\n\t"
    "v_accvgpr_write_b32 a51, v250\n\t"
    "v_accvgpr_read_b32 v245, a68\n\t"
    "v_mul_f32 v245, v245, v244\n\t"
    "v_accvgpr_write_b32 a68, v245\n\t"
    "v_accvgpr_read_b32 v246, a69\n\t"
    "v_mul_f32 v246, v246, v244\n\t"
    "v_accvgpr_write_b32 a69, v246\n\t"
    "v_accvgpr_read_b32 v247, a70\n\t"
    "v_mul_f32 v247, v247, v244\n\t"
    "v_accvgpr_write_b32 a70, v247\n\t"
    "v_accvgpr_read_b32 v248, a71\n\t"
    "v_mul_f32 v248, v248, v244\n\t"
    "v_accvgpr_write_b32 a71, v248\n\t"
    "v_accvgpr_read_b32 v249, a88\n\t"
    "v_mul_f32 v249, v249, v244\n\t"
    "v_accvgpr_write_b32 a88, v249\n\t"
    "v_accvgpr_read_b32 v250, a89\n\t"
    "v_mul_f32 v250, v250, v244\n\t"
    "v_accvgpr_write_b32 a89, v250\n\t"
    "v_accvgpr_read_b32 v245, a90\n\t"
    "v_mul_f32 v245, v245, v244\n\t"
    "v_accvgpr_write_b32 a90, v245\n\t"
    "v_accvgpr_read_b32 v246, a91\n\t"
    "v_mul_f32 v246, v246, v244\n\t"
    "v_accvgpr_write_b32 a91, v246\n\t"
    "v_accvgpr_read_b32 v247, a108\n\t"
    "v_mul_f32 v247, v247, v244\n\t"
    "v_accvgpr_write_b32 a108, v247\n\t"
    "v_accvgpr_read_b32 v248, a109\n\t"
    "v_mul_f32 v248, v248, v244\n\t"
    "v_accvgpr_write_b32 a109, v248\n\t"
    "v_accvgpr_read_b32 v249, a110\n\t"
    "v_mul_f32 v249, v249, v244\n\t"
    "v_accvgpr_write_b32 a110, v249\n\t"
    "v_accvgpr_read_b32 v250, a111\n\t"
    "v_mul_f32 v250, v250, v244\n\t"
    "v_accvgpr_write_b32 a111, v250\n\t"
    "v_accvgpr_read_b32 v245, a128\n\t"
    "v_mul_f32 v245, v245, v244\n\t"
    "v_accvgpr_write_b32 a128, v245\n\t"
    "v_accvgpr_read_b32 v246, a129\n\t"
    "v_mul_f32 v246, v246, v244\n\t"
    "v_accvgpr_write_b32 a129, v246\n\t"
    "v_accvgpr_read_b32 v247, a130\n\t"
    "v_mul_f32 v247, v247, v244\n\t"
    "v_accvgpr_write_b32 a130, v247\n\t"
    "v_accvgpr_read_b32 v248, a131\n\t"
    "v_mul_f32 v248, v248, v244\n\t"
    "v_accvgpr_write_b32 a131, v248\n\t"
    "v_accvgpr_read_b32 v249, a148\n\t"
    "v_mul_f32 v249, v249, v244\n\t"
    "v_accvgpr_write_b32 a148, v249\n\t"
    "v_accvgpr_read_b32 v250, a149\n\t"
    "v_mul_f32 v250, v250, v244\n\t"
    "v_accvgpr_write_b32 a149, v250\n\t"
    "v_accvgpr_read_b32 v245, a150\n\t"
    "v_mul_f32 v245, v245, v244\n\t"
    "v_accvgpr_write_b32 a150, v245\n\t"
    "v_accvgpr_read_b32 v246, a151\n\t"
    "v_mul_f32 v246, v246, v244\n\t"
    "v_accvgpr_write_b32 a151, v246\n\t"
    "v_accvgpr_read_b32 v247, a168\n\t"
    "v_mul_f32 v247, v247, v244\n\t"
    "v_accvgpr_write_b32 a168, v247\n\t"
    "v_accvgpr_read_b32 v248, a169\n\t"
    "v_mul_f32 v248, v248, v244\n\t"
    "v_accvgpr_write_b32 a169, v248\n\t"
    "v_accvgpr_read_b32 v249, a170\n\t"
    "v_mul_f32 v249, v249, v244\n\t"
    "v_accvgpr_write_b32 a170, v249\n\t"
    "v_accvgpr_read_b32 v250, a171\n\t"
    "v_mul_f32 v250, v250, v244\n\t"
    "v_accvgpr_write_b32 a171, v250\n\t"
    "v_max3_f32 v242, v40, v41, v42\n\t"
    "v_max3_f32 v242, v242, v43, v80\n\t"
    "v_max3_f32 v242, v242, v81, v82\n\t"
    "v_max_f32 v242, v242, v83\n\t"
    "v_mov_b32 v243, v242\n\t"
    "s_nop 1\n\t"
    "v_permlane32_swap_b32 v242, v243\n\t"
    "v_max_f32 v242, v242, v243\n\t"
    "v_mov_b32 v243, v242\n\t"
    "s_nop 1\n\t"
    "v_permlane16_swap_b32 v242, v243\n\t"
    "v_max3_f32 v242, v242, v243, 0\n\t"
    "v_sub_f32 v244, 0, v242\n\t"
    "v_exp_f32 v244, v244\n\t"
    "v_sub_f32 v40, v40, v242\n\t"
    "v_sub_f32 v41, v41, v242\n\t"
    "v_sub_f32 v42, v42, v242\n\t"
    "v_sub_f32 v43, v43, v242\n\t"
    "v_sub_f32 v80, v80, v242\n\t"
    "v_sub_f32 v81, v81, v242\n\t"
    "v_sub_f32 v82, v82, v242\n\t"
    "v_sub_f32 v83, v83, v242\n\t"
    "v_sub_f32 v204, v204, v242\n\t"
    "v_sub_f32 v205, v205, v242\n\t"
    "v_sub_f32 v206, v206, v242\n\t"
    "v_sub_f32 v207, v207, v242\n\t"
    "v_accvgpr_read_b32 v245, a12\n\t"
    "v_mul_f32 v245, v245, v244\n\t"
    "v_accvgpr_write_b32 a12, v245\n\t"
    "v_accvgpr_read_b32 v246, a13\n\t"
    "v_mul_f32 v246, v246, v244\n\t"
    "v_accvgpr_write_b32 a13, v246\n\t"
    "v_accvgpr_read_b32 v247, a14\n\t"
    "v_mul_f32 v247, v247, v244\n\t"
    "v_accvgpr_write_b32 a14, v247\n\t"
    "v_accvgpr_read_b32 v248, a15\n\t"
    "v_mul_f32 v248, v248, v244\n\t"
    "v_accvgpr_write_b32 a15, v248\n\t"
    "v_accvgpr_read_b32 v249, a32\n\t"
    "v_mul_f32 v249, v249, v244\n\t"
    "v_accvgpr_write_b32 a32, v249\n\t"
    "v_accvgpr_read_b32 v250, a33\n\t"
    "v_mul_f32 v250, v250, v244\n\t"
    "v_accvgpr_write_b32 a33, v250\n\t"
    "v_accvgpr_read_b32 v245, a34\n\t"
    "v_mul_f32 v245, v245, v244\n\t"
    "v_accvgpr_write_b32 a34, v245\n\t"
    "v_accvgpr_read_b32 v246, a35\n\t"
    "v_mul_f32 v246, v246, v244\n\t"
    "v_accvgpr_write_b32 a35, v246\n\t"
    "v_accvgpr_read_b32 v247, a52\n\t"
    "v_mul_f32 v247, v247, v244\n\t"
    "v_accvgpr_write_b32 a52, v247\n\t"
    "v_accvgpr_read_b32 v248, a53\n\t"
    "v_mul_f32 v248, v248, v244\n\t"
    "v_accvgpr_write_b32 a53, v248\n\t"
    "v_accvgpr_read_b32 v249, a54\n\t"
    "v_mul_f32 v249, v249, v244\n\t"
    "v_accvgpr_write_b32 a54, v249\n\t"
    "v_accvgpr_read_b32 v250, a55\n\t"
    "v_mul_f32 v250, v250, v244\n\t"
    "v_accvgpr_write_b32 a55, v250\n\t"
    "v_accvgpr_read_b32 v245, a72\n\t"
    "v_mul_f32 v245, v245, v244\n\t"
    "v_accvgpr_write_b32 a72, v245\n\t"
    "v_accvgpr_read_b32 v246, a73\n\t"
    "v_mul_f32 v246, v246, v244\n\t"
    "v_accvgpr_write_b32 a73, v246\n\t"
    "v_accvgpr_read_b32 v247, a74\n\t"
    "v_mul_f32 v247, v247, v244\n\t"
    "v_accvgpr_write_b32 a74, v247\n\t"
    "v_accvgpr_read_b32 v248, a75\n\t"
    "v_mul_f32 v248, v248, v244\n\t"
    "v_accvgpr_write_b32 a75, v248\n\t"
    "v_accvgpr_read_b32 v249, a92\n\t"
    "v_mul_f32 v249, v249, v244\n\t"
    "v_accvgpr_write_b32 a92, v249\n\t"
    "v_accvgpr_read_b32 v250, a93\n\t"
    "v_mul_f32 v250, v250, v244\n\t"
    "v_accvgpr_write_b32 a93, v250\n\t"
    "v_accvgpr_read_b32 v245, a94\n\t"
    "v_mul_f32 v245, v245, v244\n\t"
    "v_accvgpr_write_b32 a94, v245\n\t"
    "v_accvgpr_read_b32 v246, a95\n\t"
    "v_mul_f32 v246, v246, v244\n\t"
    "v_accvgpr_write_b32 a95, v246\n\t"
    "v_accvgpr_read_b32 v247, a112\n\t"
    "v_mul_f32 v247, v247, v244\n\t"
    "v_accvgpr_write_b32 a112, v247\n\t"
    "v_accvgpr_read_b32 v248, a113\n\t"
    "v_mul_f32 v248, v248, v244\n\t"
    "v_accvgpr_write_b32 a113, v248\n\t"
    "v_accvgpr_read_b32 v249, a114\n\t"
    "v_mul_f32 v249, v249, v244\n\t"
    "v_accvgpr_write_b32 a114, v249\n\t"
    "v_accvgpr_read_b32 v250, a115\n\t"
    "v_mul_f32 v250, v250, v244\n\t"
    "v_accvgpr_write_b32 a115, v250\n\t"
    "v_accvgpr_read_b32 v245, a132\n\t"
    "v_mul_f32 v245, v245, v244\n\t"
    "v_accvgpr_write_b32 a132, v245\n\t"
    "v_accvgpr_read_b32 v246, a133\n\t"
    "v_mul_f32 v246, v246, v244\n\t"
    "v_accvgpr_write_b32 a133, v246\n\t"
    "v_accvgpr_read_b32 v247, a134\n\t"
    "v_mul_f32 v247, v247, v244\n\t"
    "v_accvgpr_write_b32 a134, v247\n\t"
    "v_accvgpr_read_b32 v248, a135\n\t"
    "v_mul_f32 v248, v248, v244\n\t"
    "v_accvgpr_write_b32 a135, v248\n\t"
    "v_accvgpr_read_b32 v249, a152\n\t"
    "v_mul_f32 v249, v249, v244\n\t"
    "v_accvgpr_write_b32 a152, v249\n\t"
    "v_accvgpr_read_b32 v250, a153\n\t"
    "v_mul_f32 v250, v250, v244\n\t"
    "v_accvgpr_write_b32 a153, v250\n\t"
    "v_accvgpr_read_b32 v245, a154\n\t"
    "v_mul_f32 v245, v245, v244\n\t"
    "v_accvgpr_write_b32 a154, v245\n\t"
    "v_accvgpr_read_b32 v246, a155\n\t"
    "v_mul_f32 v246, v246, v244\n\t"
    "v_accvgpr_write_b32 a155, v246\n\t"
    "v_accvgpr_read_b32 v247, a172\n\t"
    "v_mul_f32 v247, v247, v244\n\t"
    "v_accvgpr_write_b32 a172, v247\n\t"
    "v_accvgpr_read_b32 v248, a173\n\t"
    "v_mul_f32 v248, v248, v244\n\t"
    "v_accvgpr_write_b32 a173, v248\n\t"
    "v_accvgpr_read_b32 v249, a174\n\t"
    "v_mul_f32 v249, v249, v244\n\t"
    "v_accvgpr_write_b32 a174, v249\n\t"
    "v_accvgpr_read_b32 v250, a175\n\t"
    "v_mul_f32 v250, v250, v244\n\t"
    "v_accvgpr_write_b32 a175, v250\n\t"
    "v_max3_f32 v242, v48, v49, v50\n\t"
    "v_max3_f32 v242, v242, v51, v88\n\t"
    "v_max3_f32 v242, v242, v89, v90\n\t"
    "v_max_f32 v242, v242, v91\n\t"
    "v_mov_b32 v243, v242\n\t"
    "s_nop 1\n\t"
    "v_permlane32_swap_b32 v242, v243\n\t"
    "v_max_f32 v242, v242, v243\n\t"
    "v_mov_b32 v243, v242\n\t"
    "s_nop 1\n\t"
    "v_permlane16_swap_b32 v242, v243\n\t"
    "v_max3_f32 v242, v242, v243, 0\n\t"
    "v_sub_f32 v244, 0, v242\n\t"
    "v_exp_f32 v244, v244\n\t"
    "v_sub_f32 v48, v48, v242\n\t"
    "v_sub_f32 v49, v49, v242\n\t"
    "v_sub_f32 v50, v50, v242\n\t"
    "v_sub_f32 v51, v51, v242\n\t"
    "v_sub_f32 v88, v88, v242\n\t"
    "v_sub_f32 v89, v89, v242\n\t"
    "v_sub_f32 v90, v90, v242\n\t"
    "v_sub_f32 v91, v91, v242\n\t"
    "v_sub_f32 v208, v208, v242\n\t"
    "v_sub_f32 v209, v209, v242\n\t"
    "v_sub_f32 v210, v210, v242\n\t"
    "v_sub_f32 v211, v211, v242\n\t"
    "v_accvgpr_read_b32 v245, a16\n\t"
    "v_mul_f32 v245, v245, v244\n\t"
    "v_accvgpr_write_b32 a16, v245\n\t"
    "v_accvgpr_read_b32 v246, a17\n\t"
    "v_mul_f32 v246, v246, v244\n\t"
    "v_accvgpr_write_b32 a17, v246\n\t"
    "v_accvgpr_read_b32 v247, a18\n\t"
    "v_mul_f32 v247, v247, v244\n\t"
    "v_accvgpr_write_b32 a18, v247\n\t"
    "v_accvgpr_read_b32 v248, a19\n\t"
    "v_mul_f32 v248, v248, v244\n\t"
    "v_accvgpr_write_b32 a19, v248\n\t"
    "v_accvgpr_read_b32 v249, a36\n\t"
    "v_mul_f32 v249, v249, v244\n\t"
    "v_accvgpr_write_b32 a36, v249\n\t"
    "v_accvgpr_read_b32 v250, a37\n\t"
    "v_mul_f32 v250, v250, v244\n\t"
    "v_accvgpr_write_b32 a37, v250\n\t"
    "v_accvgpr_read_b32 v245, a38\n\t"
    "v_mul_f32 v245, v245, v244\n\t"
    "v_accvgpr_write_b32 a38, v245\n\t"
    "v_accvgpr_read_b32 v246, a39\n\t"
    "v_mul_f32 v246, v246, v244\n\t"
    "v_accvgpr_write_b32 a39, v246\n\t"
    "v_accvgpr_read_b32 v247, a56\n\t"
    "v_mul_f32 v247, v247, v244\n\t"
    "v_accvgpr_write_b32 a56, v247\n\t"
    "v_accvgpr_read_b32 v248, a57\n\t"
    "v_mul_f32 v248, v248, v244\n\t"
    "v_accvgpr_write_b32 a57, v248\n\t"
    "v_accvgpr_read_b32 v249, a58\n\t"
    "v_mul_f32 v249, v249, v244\n\t"
    "v_accvgpr_write_b32 a58, v249\n\t"
    "v_accvgpr_read_b32 v250, a59\n\t"
    "v_mul_f32 v250, v250, v244\n\t"
    "v_accvgpr_write_b32 a59, v250\n\t"
    "v_accvgpr_read_b32 v245, a76\n\t"
    "v_mul_f32 v245, v245, v244\n\t"
    "v_accvgpr_write_b32 a76, v245\n\t"
    "v_accvgpr_read_b32 v246, a77\n\t"
    "v_mul_f32 v246, v246, v244\n\t"
    "v_accvgpr_write_b32 a77, v246\n\t"
    "v_accvgpr_read_b32 v247, a78\n\t"
    "v_mul_f32 v247, v247, v244\n\t"
    "v_accvgpr_write_b32 a78, v247\n\t"
    "v_accvgpr_read_b32 v248, a79\n\t"
    "v_mul_f32 v248, v248, v244\n\t"
    "v_accvgpr_write_b32 a79, v248\n\t"
    "v_accvgpr_read_b32 v249, a96\n\t"
    "v_mul_f32 v249, v249, v244\n\t"
    "v_accvgpr_write_b32 a96, v249\n\t"
    "v_accvgpr_read_b32 v250, a97\n\t"
    "v_mul_f32 v250, v250, v244\n\t"
    "v_accvgpr_write_b32 a97, v250\n\t"
    "v_accvgpr_read_b32 v245, a98\n\t"
    "v_mul_f32 v245, v245, v244\n\t"
    "v_accvgpr_write_b32 a98, v245\n\t"
    "v_accvgpr_read_b32 v246, a99\n\t"
    "v_mul_f32 v246, v246, v244\n\t"
    "v_accvgpr_write_b32 a99, v246\n\t"
    "v_accvgpr_read_b32 v247, a116\n\t"
    "v_mul_f32 v247, v247, v244\n\t"
    "v_accvgpr_write_b32 a116, v247\n\t"
    "v_accvgpr_read_b32 v248, a117\n\t"
    "v_mul_f32 v248, v248, v244\n\t"
    "v_accvgpr_write_b32 a117, v248\n\t"
    "v_accvgpr_read_b32 v249, a118\n\t"
    "v_mul_f32 v249, v249, v244\n\t"
    "v_accvgpr_write_b32 a118, v249\n\t"
    "v_accvgpr_read_b32 v250, a119\n\t"
    "v_mul_f32 v250, v250, v244\n\t"
    "v_accvgpr_write_b32 a119, v250\n\t"
    "v_accvgpr_read_b32 v245, a136\n\t"
    "v_mul_f32 v245, v245, v244\n\t"
    "v_accvgpr_write_b32 a136, v245\n\t"
    "v_accvgpr_read_b32 v246, a137\n\t"
    "v_mul_f32 v246, v246, v244\n\t"
    "v_accvgpr_write_b32 a137, v246\n\t"
    "v_accvgpr_read_b32 v247, a138\n\t"
    "v_mul_f32 v247, v247, v244\n\t"
    "v_accvgpr_write_b32 a138, v247\n\t"
    "v_accvgpr_read_b32 v248, a139\n\t"
    "v_mul_f32 v248, v248, v244\n\t"
    "v_accvgpr_write_b32 a139, v248\n\t"
    "v_accvgpr_read_b32 v249, a156\n\t"
    "v_mul_f32 v249, v249, v244\n\t"
    "v_accvgpr_write_b32 a156, v249\n\t"
    "v_accvgpr_read_b32 v250, a157\n\t"
    "v_mul_f32 v250, v250, v244\n\t"
    "v_accvgpr_write_b32 a157, v250\n\t"
    "v_accvgpr_read_b32 v245, a158\n\t"
    "v_mul_f32 v245, v245, v244\n\t"
    "v_accvgpr_write_b32 a158, v245\n\t"
    "v_accvgpr_read_b32 v246, a159\n\t"
    "v_mul_f32 v246, v246, v244\n\t"
    "v_accvgpr_write_b32 a159, v246\n\t"
    "v_accvgpr_read_b32 v247, a176\n\t"
    "v_mul_f32 v247, v247, v244\n\t"
    "v_accvgpr_write_b32 a176, v247\n\t"
    "v_accvgpr_read_b32 v248, a177\n\t"
    "v_mul_f32 v248, v248, v244\n\t"
    "v_accvgpr_write_b32 a177, v248\n\t"
    "v_accvgpr_read_b32 v249, a178\n\t"
    "v_mul_f32 v249, v249, v244\n\t"
    "v_accvgpr_write_b32 a178, v249\n\t"
    "v_accvgpr_read_b32 v250, a179\n\t"
    "v_mul_f32 v250, v250, v244\n\t"
    "v_accvgpr_write_b32 a179, v250\n\t"
    "s_nop 3\n\t"
    "s_setpc_b64 s[74:75]\n\t"
    "K1_RARE1:\n\t"
    "s_nop 15\n\t"
    "v_max3_f32 v242, v96, v97, v98\n\t"
    "v_max3_f32 v242, v242, v99, v136\n\t"
    "v_max3_f32 v242, v242, v137, v138\n\t"
    "v_max_f32 v242, v242, v139\n\t"
    "v_mov_b32 v243, v242\n\t"
    "s_nop 1\n\t"
    "v_permlane32_swap_b32 v242, v243\n\t"
    "v_max_f32 v242, v242, v243\n\t"
    "v_mov_b32 v243, v242\n\t"
    "s_nop 1\n\t"
    "v_permlane16_swap_b32 v242, v243\n\t"
    "v_max3_f32 v242, v242, v243, 0\n\t"
    "v_sub_f32 v244, 0, v242\n\t"
    "v_exp_f32 v244, v244\n\t"
    "v_sub_f32 v96, v96, v242\n\t"
    "v_sub_f32 v97, v97, v242\n\t"
    "v_sub_f32 v98, v98, v242\n\t"
    "v_sub_f32 v99, v99, v242\n\t"
    "v_sub_f32 v136, v136, v242\n\t"
    "v_sub_f32 v137, v137, v242\n\t"
    "v_sub_f32 v138, v138, v242\n\t"
    "v_sub_f32 v139, v139, v242\n\t"
    "v_sub_f32 v192, v192, v242\n\t"
    "v_sub_f32 v193, v193, v242\n\t"
    "v_sub_f32 v194, v194, v242\n\t"
    "v_sub_f32 v195, v195, v242\n\t"
    "v_accvgpr_read_b32 v245, a0\n\t"
    "v_mul_f32 v245, v245, v244\n\t"
    "v_accvgpr_write_b32 a0, v245\n\t"
    "v_accvgpr_read_b32 v246, a1\n\t"
    "v_mul_f32 v246, v246, v244\n\t"
    "v_accvgpr_write_b32 a1, v246\n\t"
    "v_accvgpr_read_b32 v247, a2\n\t"
    "v_mul_f32 v247, v247, v244\n\t"
    "v_accvgpr_write_b32 a2, v247\n\t"
    "v_accvgpr_read_b32 v248, a3\n\t"
    "v_mul_f32 v248, v248, v244\n\t"
    "v_accvgpr_write_b32 a3, v248\n\t"
    "v_accvgpr_read_b32 v249, a20\n\t"
    "v_mul_f32 v249, v249, v244\n\t"
    "v_accvgpr_write_b32 a20, v249\n\t"
    "v_accvgpr_read_b32 v250, a21\n\t"
    "v_mul_f32 v250, v250, v244\n\t"
    "v_accvgpr_write_b32 a21, v250\n\t"
    "v_accvgpr_read_b32 v245, a22\n\t"
    "v_mul_f32 v245, v245, v244\n\t"
    "v_accvgpr_write_b32 a22, v245\n\t"
    "v_accvgpr_read_b32 v246, a23\n\t"
    "v_mul_f32 v246, v246, v244\n\t"
    "v_accvgpr_write_b32 a23, v246\n\t"
    "v_accvgpr_read_b32 v247, a40\n\t"
    "v_mul_f32 v247, v247, v244\n\t"
    "v_accvgpr_write_b32 a40, v247\n\t"
    "v_accvgpr_read_b32 v248, a41\n\t"
    "v_mul_f32 v248, v248, v244\n\t"
    "v_accvgpr_write_b32 a41, v248\n\t"
    "v_accvgpr_read_b32 v249, a42\n\t"
    "v_mul_f32 v249, v249, v244\n\t"
    "v_accvgpr_write_b32 a42, v249\n\t"
    "v_accvgpr_read_b32 v250, a43\n\t"
    "v_mul_f32 v250, v250, v244\n\t"
    "v_accvgpr_write_b32 a43, v250\n\t"
    "v_accvgpr_read_b32 v245, a60\n\t"
    "v_mul_f32 v245, v245, v244\n\t"
    "v_accvgpr_write_b32 a60, v245\n\t"
    "v_accvgpr_read_b32 v246, a61\n\t"
    "v_mul_f32 v246, v246, v244\n\t"
    "v_accvgpr_write_b32 a61, v246\n\t"
    "v_accvgpr_read_b32 v247, a62\n\t"
    "v_mul_f32 v247, v247, v244\n\t"
    "v_accvgpr_write_b32 a62, v247\n\t"
    "v_accvgpr_read_b32 v248, a63\n\t"
    "v_mul_f32 v248, v248, v244\n\t"
    "v_accvgpr_write_b32 a63, v248\n\t"
    "v_accvgpr_read_b32 v249, a80\n\t"
    "v_mul_f32 v249, v249, v244\n\t"
    "v_accvgpr_write_b32 a80, v249\n\t"
    "v_accvgpr_read_b32 v250, a81\n\t"
    "v_mul_f32 v250, v250, v244\n\t"
    "v_accvgpr_write_b32 a81, v250\n\t"
    "v_accvgpr_read_b32 v245, a82\n\t"
    "v_mul_f32 v245, v245, v244\n\t"
    "v_accvgpr_write_b32 a82, v245\n\t"
    "v_accvgpr_read_b32 v246, a83\n\t"
    "v_mul_f32 v246, v246, v244\n\t"
    "v_accvgpr_write_b32 a83, v246\n\t"
    "v_accvgpr_read_b32 v247, a100\n\t"
    "v_mul_f32 v247, v247, v244\n\t"
    "v_accvgpr_write_b32 a100, v247\n\t"
    "v_accvgpr_read_b32 v248, a101\n\t"
    "v_mul_f32 v248, v248, v244\n\t"
    "v_accvgpr_write_b32 a101, v248\n\t"
    "v_accvgpr_read_b32 v249, a102\n\t"
    "v_mul_f32 v249, v249, v244\n\t"
    "v_accvgpr_write_b32 a102, v249\n\t"
    "v_accvgpr_read_b32 v250, a103\n\t"
    "v_mul_f32 v250, v250, v244\n\t"
    "v_accvgpr_write_b32 a103, v250\n\t"
    "v_accvgpr_read_b32 v245, a120\n\t"
    "v_mul_f32 v245, v245, v244\n\t"
    "v_accvgpr_write_b32 a120, v245\n\t"
    "v_accvgpr_read_b32 v246, a121\n\t"
    "v_mul_f32 v246, v246, v244\n\t"
    "v_accvgpr_write_b32 a121, v246\n\t"
    "v_accvgpr_read_b32 v247, a122\n\t"
    "v_mul_f32 v247, v247, v244\n\t"
    "v_accvgpr_write_b32 a122, v247\n\t"
    "v_accvgpr_read_b32 v248, a123\n\t"
    "v_mul_f32 v248, v248, v244\n\t"
    "v_accvgpr_write_b32 a123, v248\n\t"
    "v_accvgpr_read_b32 v249, a140\n\t"
    "v_mul_f32 v249, v249, v244\n\t"
    "v_accvgpr_write_b32 a140, v249\n\t"
    "v_accvgpr_read_b32 v250, a141\n\t"
    "v_mul_f32 v250, v250, v244\n\t"
    "v_accvgpr_write_b32 a141, v250\n\t"
    "v_accvgpr_read_b32 v245, a142\n\t"
    "v_mul_f32 v245, v245, v244\n\t"
    "v_accvgpr_write_b32 a142, v245\n\t"
    "v_accvgpr_read_b32 v246, a143\n\t"
    "v_mul_f32 v246, v246, v244\n\t"
    "v_accvgpr_write_b32 a143, v246\n\t"
    "v_accvgpr_read_b32 v247, a160\n\t"
    "v_mul_f32 v247, v247, v244\n\t"
    "v_accvgpr_write_b32 a160, v247\n\t"
    "v_accvgpr_read_b32 v248, a161\n\t"
    "v_mul_f32 v248, v248, v244\n\t"
    "v_accvgpr_write_b32 a161, v248\n\t"
    "v_accvgpr_read_b32 v249, a162\n\t"
    "v_mul_f32 v249, v249, v244\n\t"
    "v_accvgpr_write_b32 a162, v249\n\t"
    "v_accvgpr_read_b32 v250, a163\n\t"
    "v_mul_f32 v250, v250, v244\n\t"
    "v_accvgpr_write_b32 a163, v250\n\t"
    "v_max3_f32 v242, v104, v105, v106\n\t"
    "v_max3_f32 v242, v242, v107, v144\n\t"
    "v_max3_f32 v242, v242, v145, v146\n\t"
    "v_max_f32 v242, v242, v147\n\t"
    "v_mov_b32 v243, v242\n\t"
    "s_nop 1\n\t"
    "v_permlane32_swap_b32 v242, v243\n\t"
    "v_max_f32 v242, v242, v243\n\t"
    "v_mov_b32 v243, v242\n\t"
    "s_nop 1\n\t"
    "v_permlane16_swap_b32 v242, v243\n\t"
    "v_max3_f32 v242, v242, v243, 0\n\t"
    "v_sub_f32 v244, 0, v242\n\t"
    "v_exp_f32 v244, v244\n\t"
    "v_sub_f32 v104, v104, v242\n\t"
    "v_sub_f32 v105, v105, v242\n\t"
    "v_sub_f32 v106, v106, v242\n\t"
    "v_sub_f32 v107, v107, v242\n\t"
    "v_sub_f32 v144, v144, v242\n\t"
    "v_sub_f32 v145, v145, v242\n\t"
    "v_sub_f32 v146, v146, v242\n\t"
    "v_sub_f32 v147, v147, v242\n\t"
    "v_sub_f32 v196, v196, v242\n\t"
    "v_sub_f32 v197, v197, v242\n\t"
    "v_sub_f32 v198, v198, v242\n\t"
    "v_sub_f32 v199, v199, v242\n\t"
    "v_accvgpr_read_b32 v245, a4\n\t"
    "v_mul_f32 v245, v245, v244\n\t"
    "v_accvgpr_write_b32 a4, v245\n\t"
    "v_accvgpr_read_b32 v246, a5\n\t"
    "v_mul_f32 v246, v246, v244\n\t"
    "v_accvgpr_write_b32 a5, v246\n\t"
    "v_accvgpr_read_b32 v247, a6\n\t"
    "v_mul_f32 v247, v247, v244\n\t"
    "v_accvgpr_write_b32 a6, v247\n\t"
    "v_accvgpr_read_b32 v248, a7\n\t"
    "v_mul_f32 v248, v248, v244\n\t"
    "v_accvgpr_write_b32 a7, v248\n\t"
    "v_accvgpr_read_b32 v249, a24\n\t"
    "v_mul_f32 v249, v249, v244\n\t"
    "v_accvgpr_write_b32 a24, v249\n\t"
    "v_accvgpr_read_b32 v250, a25\n\t"
    "v_mul_f32 v250, v250, v244\n\t"
    "v_accvgpr_write_b32 a25, v250\n\t"
    "v_accvgpr_read_b32 v245, a26\n\t"
    "v_mul_f32 v245, v245, v244\n\t"
    "v_accvgpr_write_b32 a26, v245\n\t"
    "v_accvgpr_read_b32 v246, a27\n\t"
    "v_mul_f32 v246, v246, v244\n\t"
    "v_accvgpr_write_b32 a27, v246\n\t"
    "v_accvgpr_read_b32 v247, a44\n\t"
    "v_mul_f32 v247, v247, v244\n\t"
    "v_accvgpr_write_b32 a44, v247\n\t"
    "v_accvgpr_read_b32 v248, a45\n\t"
    "v_mul_f32 v248, v248, v244\n\t"
    "v_accvgpr_write_b32 a45, v248\n\t"
    "v_accvgpr_read_b32 v249, a46\n\t"
    "v_mul_f32 v249, v249, v244\n\t"
    "v_accvgpr_write_b32 a46, v249\n\t"
    "v_accvgpr_read_b32 v250, a47\n\t"
    "v_mul_f32 v250, v250, v244\n\t"
    "v_accvgpr_write_b32 a47, v250\n\t"
    "v_accvgpr_read_b32 v245, a64\n\t"
    "v_mul_f32 v245, v245, v244\n\t"
    "v_accvgpr_write_b32 a64, v245\n\t"
    "v_accvgpr_read_b32 v246, a65\n\t"
    "v_mul_f32 v246, v246, v244\n\t"
    "v_accvgpr_write_b32 a65, v246\n\t"
    "v_accvgpr_read_b32 v247, a66\n\t"
    "v_mul_f32 v247, v247, v244\n\t"
    "v_accvgpr_write_b32 a66, v247\n\t"
    "v_accvgpr_read_b32 v248, a67\n\t"
    "v_mul_f32 v248, v248, v244\n\t"
    "v_accvgpr_write_b32 a67, v248\n\t"
    "v_accvgpr_read_b32 v249, a84\n\t"
    "v_mul_f32 v249, v249, v244\n\t"
    "v_accvgpr_write_b32 a84, v249\n\t"
    "v_accvgpr_read_b32 v250, a85\n\t"
    "v_mul_f32 v250, v250, v244\n\t"
    "v_accvgpr_write_b32 a85, v250\n\t"
    "v_accvgpr_read_b32 v245, a86\n\t"
    "v_mul_f32 v245, v245, v244\n\t"
    "v_accvgpr_write_b32 a86, v245\n\t"
    "v_accvgpr_read_b32 v246, a87\n\t"
    "v_mul_f32 v246, v246, v244\n\t"
    "v_accvgpr_write_b32 a87, v246\n\t"
    "v_accvgpr_read_b32 v247, a104\n\t"
    "v_mul_f32 v247, v247, v244\n\t"
    "v_accvgpr_write_b32 a104, v247\n\t"
    "v_accvgpr_read_b32 v248, a105\n\t"
    "v_mul_f32 v248, v248, v244\n\t"
    "v_accvgpr_write_b32 a105, v248\n\t"
    "v_accvgpr_read_b32 v249, a106\n\t"
    "v_mul_f32 v249, v249, v244\n\t"
    "v_accvgpr_write_b32 a106, v249\n\t"
    "v_accvgpr_read_b32 v250, a107\n\t"
    "v_mul_f32 v250, v250, v244\n\t"
    "v_accvgpr_write_b32 a107, v250\n\t"
    "v_accvgpr_read_b32 v245, a124\n\t"
    "v_mul_f32 v245, v245, v244\n\t"
    "v_accvgpr_write_b32 a124, v245\n\t"
    "v_accvgpr_read_b32 v246, a125\n\t"
    "v_mul_f32 v246, v246, v244\n\t"
    "v_accvgpr_write_b32 a125, v246\n\t"
    "v_accvgpr_read_b32 v247, a126\n\t"
    "v_mul_f32 v247, v247, v244\n\t"
    "v_accvgpr_write_b32 a126, v247\n\t"
    "v_accvgpr_read_b32 v248, a127\n\t"
    "v_mul_f32 v248, v248, v244\n\t"
    "v_accvgpr_write_b32 a127, v248\n\t"
    "v_accvgpr_read_b32 v249, a144\n\t"
    "v_mul_f32 v249, v249, v244\n\t"
    "v_accvgpr_write_b32 a144, v249\n\t"
    "v_accvgpr_read_b32 v250, a145\n\t"
    "v_mul_f32 v250, v250, v244\n\t"
    "v_accvgpr_write_b32 a145, v250\n\t"
    "v_accvgpr_read_b32 v245, a146\n\t"
    "v_mul_f32 v245, v245, v244\n\t"
    "v_accvgpr_write_b32 a146, v245\n\t"
    "v_accvgpr_read_b32 v246, a147\n\t"
    "v_mul_f32 v246, v246, v244\n\t"
    "v_accvgpr_write_b32 a147, v246\n\t"
    "v_accvgpr_read_b32 v247, a164\n\t"
    "v_mul_f32 v247, v247, v244\n\t"
    "v_accvgpr_write_b32 a164, v247\n\t"
    "v_accvgpr_read_b32 v248, a165\n\t"
    "v_mul_f32 v248, v248, v244\n\t"
    "v_accvgpr_write_b32 a165, v248\n\t"
    "v_accvgpr_read_b32 v249, a166\n\t"
    "v_mul_f32 v249, v249, v244\n\t"
    "v_accvgpr_write_b32 a166, v249\n\t"
    "v_accvgpr_read_b32 v250, a167\n\t"
    "v_mul_f32 v250, v250, v244\n\t"
    "v_accvgpr_write_b32 a167, v250\n\t"
    "v_max3_f32 v242, v112, v113, v114\n\t"
    "v_max3_f32 v242, v242, v115, v152\n\t"
    "v_max3_f32 v242, v242, v153, v154\n\t"
    "v_max_f32 v242, v242, v155\n\t"
    "v_mov_b32 v243, v242\n\t"
    "s_nop 1\n\t"
    "v_permlane32_swap_b32 v242, v243\n\t"
    "v_max_f32 v242, v242, v243\n\t"
    "v_mov_b32 v243, v242\n\t"
    "s_nop 1\n\t"
    "v_permlane16_swap_b32 v242, v243\n\t"
    "v_max3_f32 v242, v242, v243, 0\n\t"
    "v_sub_f32 v244, 0, v242\n\t"
    "v_exp_f32 v244, v244\n\t"
    "v_sub_f32 v112, v112, v242\n\t"
    "v_sub_f32 v113, v113, v242\n\t"
    "v_sub_f32 v114, v114, v242\n\t"
    "v_sub_f32 v115, v115, v242\n\t"
    "v_sub_f32 v152, v152, v242\n\t"
    "v_sub_f32 v153, v153, v242\n\t"
    "v_sub_f32 v154, v154, v242\n\t"
    "v_sub_f32 v155, v155, v242\n\t"
    "v_sub_f32 v200, v200, v242\n\t"
    "v_sub_f32 v201, v201, v242\n\t"
    "v_sub_f32 v202, v202, v242\n\t"
    "v_sub_f32 v203, v203, v242\n\t"
    "v_accvgpr_read_b32 v245, a8\n\t"
    "v_mul_f32 v245, v245, v244\n\t"
    "v_accvgpr_write_b32 a8, v245\n\t"
    "v_accvgpr_read_b32 v246, a9\n\t"
    "v_mul_f32 v246, v246, v244\n\t"
    "v_accvgpr_write_b32 a9, v246\n\t"
    "v_accvgpr_read_b32 v247, a10\n\t"
    "v_mul_f32 v247, v247, v244\n\t"
    "v_accvgpr_write_b32 a10, v247\n\t"
    "v_accvgpr_read_b32 v248, a11\n\t"
    "v_mul_f32 v248, v248, v244\n\t"
    "v_accvgpr_write_b32 a11, v248\n\t"
    "v_accvgpr_read_b32 v249, a28\n\t"
    "v_mul_f32 v249, v249, v244\n\t"
    "v_accvgpr_write_b32 a28, v249\n\t"
    "v_accvgpr_read_b32 v250, a29\n\t"
    "v_mul_f32 v250, v250, v244\n\t"
    "v_accvgpr_write_b32 a29, v250\n\t"
    "v_accvgpr_read_b32 v245, a30\n\t"
    "v_mul_f32 v245, v245, v244\n\t"
    "v_accvgpr_write_b32 a30, v245\n\t"
    "v_accvgpr_read_b32 v246, a31\n\t"
    "v_mul_f32 v246, v246, v244\n\t"
    "v_accvgpr_write_b32 a31, v246\n\t"
    "v_accvgpr_read_b32 v247, a48\n\t"
    "v_mul_f32 v247, v247, v244\n\t"
    "v_accvgpr_write_b32 a48, v247\n\t"
    "v_accvgpr_read_b32 v248, a49\n\t"
    "v_mul_f32 v248, v248, v244\n\t"
    "v_accvgpr_write_b32 a49, v248\n\t"
    "v_accvgpr_read_b32 v249, a50\n\t"
    "v_mul_f32 v249, v249, v244\n\t"
    "v_accvgpr_write_b32 a50, v249\n\t"
    "v_accvgpr_read_b32 v250, a51\n\t"
    "v_mul_f32 v250, v250, v244\n\t"
    "v_accvgpr_write_b32 a51, v250\n\t"
    "v_accvgpr_read_b32 v245, a68\n\t"
    "v_mul_f32 v245, v245, v244\n\t"
    "v_accvgpr_write_b32 a68, v245\n\t"
    "v_accvgpr_read_b32 v246, a69\n\t"
    "v_mul_f32 v246, v246, v244\n\t"
    "v_accvgpr_write_b32 a69, v246\n\t"
    "v_accvgpr_read_b32 v247, a70\n\t"
    "v_mul_f32 v247, v247, v244\n\t"
    "v_accvgpr_write_b32 a70, v247\n\t"
    "v_accvgpr_read_b32 v248, a71\n\t"
    "v_mul_f32 v248, v248, v244\n\t"
    "v_accvgpr_write_b32 a71, v248\n\t"
    "v_accvgpr_read_b32 v249, a88\n\t"
    "v_mul_f32 v249, v249, v244\n\t"
    "v_accvgpr_write_b32 a88, v249\n\t"
    "v_accvgpr_read_b32 v250, a89\n\t"
    "v_mul_f32 v250, v250, v244\n\t"
    "v_accvgpr_write_b32 a89, v250\n\t"
    "v_accvgpr_read_b32 v245, a90\n\t"
    "v_mul_f32 v245, v245, v244\n\t"
    "v_accvgpr_write_b32 a90, v245\n\t"
    "v_accvgpr_read_b32 v246, a91\n\t"
    "v_mul_f32 v246, v246, v244\n\t"
    "v_accvgpr_write_b32 a91, v246\n\t"
    "v_accvgpr_read_b32 v247, a108\n\t"
    "v_mul_f32 v247, v247, v244\n\t"
    "v_accvgpr_write_b32 a108, v247\n\t"
    "v_accvgpr_read_b32 v248, a109\n\t"
    "v_mul_f32 v248, v248, v244\n\t"
    "v_accvgpr_write_b32 a109, v248\n\t"
    "v_accvgpr_read_b32 v249, a110\n\t"
    "v_mul_f32 v249, v249, v244\n\t"
    "v_accvgpr_write_b32 a110, v249\n\t"
    "v_accvgpr_read_b32 v250, a111\n\t"
    "v_mul_f32 v250, v250, v244\n\t"
    "v_accvgpr_write_b32 a111, v250\n\t"
    "v_accvgpr_read_b32 v245, a128\n\t"
    "v_mul_f32 v245, v245, v244\n\t"
    "v_accvgpr_write_b32 a128, v245\n\t"
    "v_accvgpr_read_b32 v246, a129\n\t"
    "v_mul_f32 v246, v246, v244\n\t"
    "v_accvgpr_write_b32 a129, v246\n\t"
    "v_accvgpr_read_b32 v247, a130\n\t"
    "v_mul_f32 v247, v247, v244\n\t"
    "v_accvgpr_write_b32 a130, v247\n\t"
    "v_accvgpr_read_b32 v248, a131\n\t"
    "v_mul_f32 v248, v248, v244\n\t"
    "v_accvgpr_write_b32 a131, v248\n\t"
    "v_accvgpr_read_b32 v249, a148\n\t"
    "v_mul_f32 v249, v249, v244\n\t"
    "v_accvgpr_write_b32 a148, v249\n\t"
    "v_accvgpr_read_b32 v250, a149\n\t"
    "v_mul_f32 v250, v250, v244\n\t"
    "v_accvgpr_write_b32 a149, v250\n\t"
    "v_accvgpr_read_b32 v245, a150\n\t"
    "v_mul_f32 v245, v245, v244\n\t"
    "v_accvgpr_write_b32 a150, v245\n\t"
    "v_accvgpr_read_b32 v246, a151\n\t"
    "v_mul_f32 v246, v246, v244\n\t"
    "v_accvgpr_write_b32 a151, v246\n\t"
    "v_accvgpr_read_b32 v247, a168\n\t"
    "v_mul_f32 v247, v247, v244\n\t"
    "v_accvgpr_write_b32 a168, v247\n\t"
    "v_accvgpr_read_b32 v248, a169\n\t"
    "v_mul_f32 v248, v248, v244\n\t"
    "v_accvgpr_write_b32 a169, v248\n\t"
    "v_accvgpr_read_b32 v249, a170\n\t"
    "v_mul_f32 v249, v249, v244\n\t"
    "v_accvgpr_write_b32 a170, v249\n\t"
    "v_accvgpr_read_b32 v250, a171\n\t"
    "v_mul_f32 v250, v250, v244\n\t"
    "v_accvgpr_write_b32 a171, v250\n\t"
    "v_max3_f32 v242, v120, v121, v122\n\t"
    "v_max3_f32 v242, v242, v123, v160\n\t"
    "v_max3_f32 v242, v242, v161, v162\n\t"
    "v_max_f32 v242, v242, v163\n\t"
    "v_mov_b32 v243, v242\n\t"
    "s_nop 1\n\t"
    "v_permlane32_swap_b32 v242, v243\n\t"
    "v_max_f32 v242, v242, v243\n\t"
    "v_mov_b32 v243, v242\n\t"
    "s_nop 1\n\t"
    "v_permlane16_swap_b32 v242, v243\n\t"
    "v_max3_f32 v242, v242, v243, 0\n\t"
    "v_sub_f32 v244, 0, v242\n\t"
    "v_exp_f32 v244, v244\n\t"
    "v_sub_f32 v120, v120, v242\n\t"
    "v_sub_f32 v121, v121, v242\n\t"
    "v_sub_f32 v122, v122, v242\n\t"
    "v_sub_f32 v123, v123, v242\n\t"
    "v_sub_f32 v160, v160, v242\n\t"
    "v_sub_f32 v161, v161, v242\n\t"
    "v_sub_f32 v162, v162, v242\n\t"
    "v_sub_f32 v163, v163, v242\n\t"
    "v_sub_f32 v204, v204, v242\n\t"
    "v_sub_f32 v205, v205, v242\n\t"
    "v_sub_f32 v206, v206, v242\n\t"
    "v_sub_f32 v207, v207, v242\n\t"
    "v_accvgpr_read_b32 v245, a12\n\t"
    "v_mul_f32 v245, v245, v244\n\t"
    "v_accvgpr_write_b32 a12, v245\n\t"
    "v_accvgpr_read_b32 v246, a13\n\t"
    "v_mul_f32 v246, v246, v244\n\t"
    "v_accvgpr_write_b32 a13, v246\n\t"
    "v_accvgpr_read_b32 v247, a14\n\t"
    "v_mul_f32 v247, v247, v244\n\t"
    "v_accvgpr_write_b32 a14, v247\n\t"
    "v_accvgpr_read_b32 v248, a15\n\t"
    "v_mul_f32 v248, v248, v244\n\t"
    "v_accvgpr_write_b32 a15, v248\n\t"
    "v_accvgpr_read_b32 v249, a32\n\t"
    "v_mul_f32 v249, v249, v244\n\t"
    "v_accvgpr_write_b32 a32, v249\n\t"
    "v_accvgpr_read_b32 v250, a33\n\t"
    "v_mul_f32 v250, v250, v244\n\t"
    "v_accvgpr_write_b32 a33, v250\n\t"
    "v_accvgpr_read_b32 v245, a34\n\t"
    "v_mul_f32 v245, v245, v244\n\t"
    "v_accvgpr_write_b32 a34, v245\n\t"
    "v_accvgpr_read_b32 v246, a35\n\t"
    "v_mul_f32 v246, v246, v244\n\t"
    "v_accvgpr_write_b32 a35, v246\n\t"
    "v_accvgpr_read_b32 v247, a52\n\t"
    "v_mul_f32 v247, v247, v244\n\t"
    "v_accvgpr_write_b32 a52, v247\n\t"
    "v_accvgpr_read_b32 v248, a53\n\t"
    "v_mul_f32 v248, v248, v244\n\t"
    "v_accvgpr_write_b32 a53, v248\n\t"
    "v_accvgpr_read_b32 v249, a54\n\t"
    "v_mul_f32 v249, v249, v244\n\t"
    "v_accvgpr_write_b32 a54, v249\n\t"
    "v_accvgpr_read_b32 v250, a55\n\t"
    "v_mul_f32 v250, v250, v244\n\t"
    "v_accvgpr_write_b32 a55, v250\n\t"
    "v_accvgpr_read_b32 v245, a72\n\t"
    "v_mul_f32 v245, v245, v244\n\t"
    "v_accvgpr_write_b32 a72, v245\n\t"
    "v_accvgpr_read_b32 v246, a73\n\t"
    "v_mul_f32 v246, v246, v244\n\t"
    "v_accvgpr_write_b32 a73, v246\n\t"
    "v_accvgpr_read_b32 v247, a74\n\t"
    "v_mul_f32 v247, v247, v244\n\t"
    "v_accvgpr_write_b32 a74, v247\n\t"
    "v_accvgpr_read_b32 v248, a75\n\t"
    "v_mul_f32 v248, v248, v244\n\t"
    "v_accvgpr_write_b32 a75, v248\n\t"
    "v_accvgpr_read_b32 v249, a92\n\t"
    "v_mul_f32 v249, v249, v244\n\t"
    "v_accvgpr_write_b32 a92, v249\n\t"
    "v_accvgpr_read_b32 v250, a93\n\t"
    "v_mul_f32 v250, v250, v244\n\t"
    "v_accvgpr_write_b32 a93, v250\n\t"
    "v_accvgpr_read_b32 v245, a94\n\t"
    "v_mul_f32 v245, v245, v244\n\t"
    "v_accvgpr_write_b32 a94, v245\n\t"
    "v_accvgpr_read_b32 v246, a95\n\t"
    "v_mul_f32 v246, v246, v244\n\t"
    "v_accvgpr_write_b32 a95, v246\n\t"
    "v_accvgpr_read_b32 v247, a112\n\t"
    "v_mul_f32 v247, v247, v244\n\t"
    "v_accvgpr_write_b32 a112, v247\n\t"
    "v_accvgpr_read_b32 v248, a113\n\t"
    "v_mul_f32 v248, v248, v244\n\t"
    "v_accvgpr_write_b32 a113, v248\n\t"
    "v_accvgpr_read_b32 v249, a114\n\t"
    "v_mul_f32 v249, v249, v244\n\t"
    "v_accvgpr_write_b32 a114, v249\n\t"
    "v_accvgpr_read_b32 v250, a115\n\t"
    "v_mul_f32 v250, v250, v244\n\t"
    "v_accvgpr_write_b32 a115, v250\n\t"
    "v_accvgpr_read_b32 v245, a132\n\t"
    "v_mul_f32 v245, v245, v244\n\t"
    "v_accvgpr_write_b32 a132, v245\n\t"
    "v_accvgpr_read_b32 v246, a133\n\t"
    "v_mul_f32 v246, v246, v244\n\t"
    "v_accvgpr_write_b32 a133, v246\n\t"
    "v_accvgpr_read_b32 v247, a134\n\t"
    "v_mul_f32 v247, v247, v244\n\t"
    "v_accvgpr_write_b32 a134, v247\n\t"
    "v_accvgpr_read_b32 v248, a135\n\t"
    "v_mul_f32 v248, v248, v244\n\t"
    "v_accvgpr_write_b32 a135, v248\n\t"
    "v_accvgpr_read_b32 v249, a152\n\t"
    "v_mul_f32 v249, v249, v244\n\t"
    "v_accvgpr_write_b32 a152, v249\n\t"
    "v_accvgpr_read_b32 v250, a153\n\t"
    "v_mul_f32 v250, v250, v244\n\t"
    "v_accvgpr_write_b32 a153, v250\n\t"
    "v_accvgpr_read_b32 v245, a154\n\t"
    "v_mul_f32 v245, v245, v244\n\t"
    "v_accvgpr_write_b32 a154, v245\n\t"
    "v_accvgpr_read_b32 v246, a155\n\t"
    "v_mul_f32 v246, v246, v244\n\t"
    "v_accvgpr_write_b32 a155, v246\n\t"
    "v_accvgpr_read_b32 v247, a172\n\t"
    "v_mul_f32 v247, v247, v244\n\t"
    "v_accvgpr_write_b32 a172, v247\n\t"
    "v_accvgpr_read_b32 v248, a173\n\t"
    "v_mul_f32 v248, v248, v244\n\t"
    "v_accvgpr_write_b32 a173, v248\n\t"
    "v_accvgpr_read_b32 v249, a174\n\t"
    "v_mul_f32 v249, v249, v244\n\t"
    "v_accvgpr_write_b32 a174, v249\n\t"
    "v_accvgpr_read_b32 v250, a175\n\t"
    "v_mul_f32 v250, v250, v244\n\t"
    "v_accvgpr_write_b32 a175, v250\n\t"
    "v_max3_f32 v242, v128, v129, v130\n\t"
    "v_max3_f32 v242, v242, v131, v168\n\t"
    "v_max3_f32 v242, v242, v169, v170\n\t"
    "v_max_f32 v242, v242, v171\n\t"
    "v_mov_b32 v243, v242\n\t"
    "s_nop 1\n\t"
    "v_permlane32_swap_b32 v242, v243\n\t"
    "v_max_f32 v242, v242, v243\n\t"
    "v_mov_b32 v243, v242\n\t"
    "s_nop 1\n\t"
    "v_permlane16_swap_b32 v242, v243\n\t"
    "v_max3_f32 v242, v242, v243, 0\n\t"
    "v_sub_f32 v244, 0, v242\n\t"
    "v_exp_f32 v244, v244\n\t"
    "v_sub_f32 v128, v128, v242\n\t"
    "v_sub_f32 v129, v129, v242\n\t"
    "v_sub_f32 v130, v130, v242\n\t"
    "v_sub_f32 v131, v131, v242\n\t"
    "v_sub_f32 v168, v168, v242\n\t"
    "v_sub_f32 v169, v169, v242\n\t"
    "v_sub_f32 v170, v170, v242\n\t"
    "v_sub_f32 v171, v171, v242\n\t"
    "v_sub_f32 v208, v208, v242\n\t"
    "v_sub_f32 v209, v209, v242\n\t"
    "v_sub_f32 v210, v210, v242\n\t"
    "v_sub_f32 v211, v211, v242\n\t"
    "v_accvgpr_read_b32 v245, a16\n\t"
    "v_mul_f32 v245, v245, v244\n\t"
    "v_accvgpr_write_b32 a16, v245\n\t"
    "v_accvgpr_read_b32 v246, a17\n\t"
    "v_mul_f32 v246, v246, v244\n\t"
    "v_accvgpr_write_b32 a17, v246\n\t"
    "v_accvgpr_read_b32 v247, a18\n\t"
    "v_mul_f32 v247, v247, v244\n\t"
    "v_accvgpr_write_b32 a18, v247\n\t"
    "v_accvgpr_read_b32 v248, a19\n\t"
    "v_mul_f32 v248, v248, v244\n\t"
    "v_accvgpr_write_b32 a19, v248\n\t"
    "v_accvgpr_read_b32 v249, a36\n\t"
    "v_mul_f32 v249, v249, v244\n\t"
    "v_accvgpr_write_b32 a36, v249\n\t"
    "v_accvgpr_read_b32 v250, a37\n\t"
    "v_mul_f32 v250, v250, v244\n\t"
    "v_accvgpr_write_b32 a37, v250\n\t"
    "v_accvgpr_read_b32 v245, a38\n\t"
    "v_mul_f32 v245, v245, v244\n\t"
    "v_accvgpr_write_b32 a38, v245\n\t"
    "v_accvgpr_read_b32 v246, a39\n\t"
    "v_mul_f32 v246, v246, v244\n\t"
    "v_accvgpr_write_b32 a39, v246\n\t"
    "v_accvgpr_read_b32 v247, a56\n\t"
    "v_mul_f32 v247, v247, v244\n\t"
    "v_accvgpr_write_b32 a56, v247\n\t"
    "v_accvgpr_read_b32 v248, a57\n\t"
    "v_mul_f32 v248, v248, v244\n\t"
    "v_accvgpr_write_b32 a57, v248\n\t"
    "v_accvgpr_read_b32 v249, a58\n\t"
    "v_mul_f32 v249, v249, v244\n\t"
    "v_accvgpr_write_b32 a58, v249\n\t"
    "v_accvgpr_read_b32 v250, a59\n\t"
    "v_mul_f32 v250, v250, v244\n\t"
    "v_accvgpr_write_b32 a59, v250\n\t"
    "v_accvgpr_read_b32 v245, a76\n\t"
    "v_mul_f32 v245, v245, v244\n\t"
    "v_accvgpr_write_b32 a76, v245\n\t"
    "v_accvgpr_read_b32 v246, a77\n\t"
    "v_mul_f32 v246, v246, v244\n\t"
    "v_accvgpr_write_b32 a77, v246\n\t"
    "v_accvgpr_read_b32 v247, a78\n\t"
    "v_mul_f32 v247, v247, v244\n\t"
    "v_accvgpr_write_b32 a78, v247\n\t"
    "v_accvgpr_read_b32 v248, a79\n\t"
    "v_mul_f32 v248, v248, v244\n\t"
    "v_accvgpr_write_b32 a79, v248\n\t"
    "v_accvgpr_read_b32 v249, a96\n\t"
    "v_mul_f32 v249, v249, v244\n\t"
    "v_accvgpr_write_b32 a96, v249\n\t"
    "v_accvgpr_read_b32 v250, a97\n\t"
    "v_mul_f32 v250, v250, v244\n\t"
    "v_accvgpr_write_b32 a97, v250\n\t"
    "v_accvgpr_read_b32 v245, a98\n\t"
    "v_mul_f32 v245, v245, v244\n\t"
    "v_accvgpr_write_b32 a98, v245\n\t"
    "v_accvgpr_read_b32 v246, a99\n\t"
    "v_mul_f32 v246, v246, v244\n\t"
    "v_accvgpr_write_b32 a99, v246\n\t"
    "v_accvgpr_read_b32 v247, a116\n\t"
    "v_mul_f32 v247, v247, v244\n\t"
    "v_accvgpr_write_b32 a116, v247\n\t"
    "v_accvgpr_read_b32 v248, a117\n\t"
    "v_mul_f32 v248, v248, v244\n\t"
    "v_accvgpr_write_b32 a117, v248\n\t"
    "v_accvgpr_read_b32 v249, a118\n\t"
    "v_mul_f32 v249, v249, v244\n\t"
    "v_accvgpr_write_b32 a118, v249\n\t"
    "v_accvgpr_read_b32 v250, a119\n\t"
    "v_mul_f32 v250, v250, v244\n\t"
    "v_accvgpr_write_b32 a119, v250\n\t"
    "v_accvgpr_read_b32 v245, a136\n\t"
    "v_mul_f32 v245, v245, v244\n\t"
    "v_accvgpr_write_b32 a136, v245\n\t"
    "v_accvgpr_read_b32 v246, a137\n\t"
    "v_mul_f32 v246, v246, v244\n\t"
    "v_accvgpr_write_b32 a137, v246\n\t"
    "v_accvgpr_read_b32 v247, a138\n\t"
    "v_mul_f32 v247, v247, v244\n\t"
    "v_accvgpr_write_b32 a138, v247\n\t"
    "v_accvgpr_read_b32 v248, a139\n\t"
    "v_mul_f32 v248, v248, v244\n\t"
    "v_accvgpr_write_b32 a139, v248\n\t"
    "v_accvgpr_read_b32 v249, a156\n\t"
    "v_mul_f32 v249, v249, v244\n\t"
    "v_accvgpr_write_b32 a156, v249\n\t"
    "v_accvgpr_read_b32 v250, a157\n\t"
    "v_mul_f32 v250, v250, v244\n\t"
    "v_accvgpr_write_b32 a157, v250\n\t"
    "v_accvgpr_read_b32 v245, a158\n\t"
    "v_mul_f32 v245, v245, v244\n\t"
    "v_accvgpr_write_b32 a158, v245\n\t"
    "v_accvgpr_read_b32 v246, a159\n\t"
    "v_mul_f32 v246, v246, v244\n\t"
    "v_accvgpr_write_b32 a159, v246\n\t"
    "v_accvgpr_read_b32 v247, a176\n\t"
    "v_mul_f32 v247, v247, v244\n\t"
    "v_accvgpr_write_b32 a176, v247\n\t"
    "v_accvgpr_read_b32 v248, a177\n\t"
    "v_mul_f32 v248, v248, v244\n\t"
    "v_accvgpr_write_b32 a177, v248\n\t"
    "v_accvgpr_read_b32 v249, a178\n\t"
    "v_mul_f32 v249, v249, v244\n\t"
    "v_accvgpr_write_b32 a178, v249\n\t"
    "v_accvgpr_read_b32 v250, a179\n\t"
    "v_mul_f32 v250, v250, v244\n\t"
    "v_accvgpr_write_b32 a179, v250\n\t"
    "s_nop 3\n\t"
    "s_setpc_b64 s[74:75]\n\t"
    "K1_END:\n\t"
    :
    : "v"(vXrd), "v"(vXd), "v"(vWrd0), "v"(vWd0), "v"(vKd0), "v"(vVd), "v"(vKrd0), "v"(vVrd0), "v"(vSync), "v"(vWoD0),
      "s"(px), "s"(pw), "s"(pk), "s"(pv), "s"(ws), "s"(pwo), "s"(pout),
      "s"(sMw), "s"(sMx), "s"(sXo0), "s"(sW1o), "s"(sWo0), "s"(sRot), "s"(sPack), "s"(sAreaW), "s"(sAreaR), "s"(sPeer0)
    :
      "v12", "v13", "v14", "v15", "v16", "v17", "v18", "v19", "v20", "v21", "v22", "v23", "v24", "v25", "v26", "v27", "v28", "v29", "v30", "v31", "v32", "v33", "v34", "v35",
      "v36", "v37", "v38", "v39", "v40", "v41", "v42", "v43", "v44", "v45", "v46", "v47", "v48", "v49", "v50", "v51", "v52", "v53", "v54", "v55", "v56", "v57", "v58", "v59",
      "v60", "v61", "v62", "v63", "v64", "v65", "v66", "v67", "v68", "v69", "v70", "v71", "v72", "v73", "v74", "v75", "v76", "v77", "v78", "v79", "v80", "v81", "v82", "v83",
      "v84", "v85", "v86", "v87", "v88", "v89", "v90", "v91", "v92", "v93", "v94", "v95", "v96", "v97", "v98", "v99", "v100", "v101", "v102", "v103", "v104", "v105", "v106", "v107",
      "v108", "v109", "v110", "v111", "v112", "v113", "v114", "v115", "v116", "v117", "v118", "v119", "v120", "v121", "v122", "v123", "v124", "v125", "v126", "v127", "v128", "v129", "v130", "v131",
      "v132", "v133", "v134", "v135", "v136", "v137", "v138", "v139", "v140", "v141", "v142", "v143", "v144", "v145", "v146", "v147", "v148", "v149", "v150", "v151", "v152", "v153", "v154", "v155",
      "v156", "v157", "v158", "v159", "v160", "v161", "v162", "v163", "v164", "v165", "v166", "v167", "v168", "v169", "v170", "v171", "v172", "v173", "v174", "v175", "v176", "v177", "v178", "v179",
      "v180", "v181", "v182", "v183", "v184", "v185", "v186", "v187", "v188", "v189", "v190", "v191", "v192", "v193", "v194", "v195", "v196", "v197", "v198", "v199", "v200", "v201", "v202", "v203",
      "v204", "v205", "v206", "v207", "v208", "v209", "v210", "v211", "v212", "v213", "v214", "v215", "v216", "v217", "v218", "v219", "v220", "v221", "v222", "v223", "v224", "v225", "v226", "v227",
      "v228", "v229", "v230", "v231", "v232", "v233", "v234", "v235", "v236", "v237", "v238", "v239", "v240", "v241", "v242", "v243", "v244", "v245", "v246", "v247", "v248", "v249", "v250", "v251",
      "v252", "v253", "v254", "v255", "a0", "a1", "a2", "a3", "a4", "a5", "a6", "a7", "a8", "a9", "a10", "a11", "a12", "a13", "a14", "a15", "a16", "a17", "a18", "a19",
      "a20", "a21", "a22", "a23", "a24", "a25", "a26", "a27", "a28", "a29", "a30", "a31", "a32", "a33", "a34", "a35", "a36", "a37", "a38", "a39", "a40", "a41", "a42", "a43",
      "a44", "a45", "a46", "a47", "a48", "a49", "a50", "a51", "a52", "a53", "a54", "a55", "a56", "a57", "a58", "a59", "a60", "a61", "a62", "a63", "a64", "a65", "a66", "a67",
      "a68", "a69", "a70", "a71", "a72", "a73", "a74", "a75", "a76", "a77", "a78", "a79", "a80", "a81", "a82", "a83", "a84", "a85", "a86", "a87", "a88", "a89", "a90", "a91",
      "a92", "a93", "a94", "a95", "a96", "a97", "a98", "a99", "a100", "a101", "a102", "a103", "a104", "a105", "a106", "a107", "a108", "a109", "a110", "a111", "a112", "a113", "a114", "a115",
      "a116", "a117", "a118", "a119", "a120", "a121", "a122", "a123", "a124", "a125", "a126", "a127", "a128", "a129", "a130", "a131", "a132", "a133", "a134", "a135", "a136", "a137", "a138", "a139",
      "a140", "a141", "a142", "a143", "a144", "a145", "a146", "a147", "a148", "a149", "a150", "a151", "a152", "a153", "a154", "a155", "a156", "a157", "a158", "a159", "a160", "a161", "a162", "a163",
      "a164", "a165", "a166", "a167", "a168", "a169", "a170", "a171", "a172", "a173", "a174", "a175", "a176", "a177", "a178", "a179", "a180", "a181", "a182", "a183", "a184", "a185", "a186", "a187",
      "a188", "a189", "a190", "a191", "a192", "a193", "a194", "a195", "a196", "a197", "a198", "a199", "a200", "a201", "a202", "a203", "a204", "a205", "a206", "a207", "a208", "a209", "a210", "a211",
      "a212", "a213", "a214", "a215", "a216", "a217", "a218", "a219", "a220", "a221", "a222", "a223", "a224", "a225", "a226", "a227", "a228", "a229", "a230", "a231", "a232", "a233", "a234", "a235",
      "a236", "a237", "a238", "a239", "a240", "a241", "a242", "a243", "a244", "a245", "a246", "a247", "a248", "a249", "a250", "a251", "a252", "a253", "a254", "a255", "s36", "s37", "s38", "s39",
      "s40", "s41", "s42", "s43", "s44", "s45", "s46", "s47", "s48", "s49", "s50", "s51", "s52", "s53", "s54", "s55", "s56", "s57", "s58", "s59", "s60", "s61", "s62", "s63",
      "s64", "s65", "s66", "s67", "s68", "s69", "s70", "s71", "s72", "s73", "s74", "s75", "s76", "s77", "s78", "s79", "s80", "s81", "s82", "s83", "s84", "s85", "s86", "s87",
      "s88", "s89", "s90", "s91", "s92", "s93", "s94", "s95", "s96", "s97", "s98", "s99", "vcc", "scc", "memory");
}

extern "C" void kernel_launch(void* const* d_in, const int* in_sizes, int n_in, void* d_out, int out_size, void* d_ws, size_t ws_size, hipStream_t stream) {
  const u16* Ax = (const u16*)d_in[0]; const u16* Ak = (const u16*)d_in[1]; const u16* Av = (const u16*)d_in[2];
  const u16* Awq = (const u16*)d_in[3]; const u16* Awo = (const u16*)d_in[4];
  const u16* Bx = (const u16*)d_in[5]; const u16* Bk = (const u16*)d_in[6]; const u16* Bv = (const u16*)d_in[7];
  const u16* Bwq = (const u16*)d_in[8]; const u16* Bwo = (const u16*)d_in[9];
  const size_t WSBASE = 0;
  fused_attn<<<256, 256, 0, stream>>>(Ax, Ak, Av, Awq, Awo, Bx, Bk, Bv, Bwq, Bwo, (char*)d_ws + WSBASE, (u16*)d_out);
}
```
